```python
import torch

def _softmax(x, axis=-1):
    return torch.softmax(x, dim=axis)
def _gelu(x):
    return torch.nn.functional.gelu(x, approximate="tanh")
def _silu(x):
    return torch.nn.functional.silu(x)
def _sigmoid(x):
    return torch.sigmoid(x)
def _layernorm(x, axis=-1, eps=1e-5):
    m = x.mean(dim=axis, keepdim=True)
    v = x.var(dim=axis, keepdim=True, unbiased=False)
    return (x - m) / torch.sqrt(v + eps)
def _scatter_add(tgt, idx, src, axis=0):
    return tgt.clone().index_add_(axis, idx, src)
def _segment_sum(src, seg, n):
    out = torch.zeros((n,) + src.shape[1:], dtype=src.dtype)
    return out.index_add_(0, seg, src)
def _pad(x, pw):
    flat = tuple(v for pair in reversed(pw) for v in pair)
    return torch.nn.functional.pad(x, flat)

kernel_name = 'standalone__gqa_softmax__near_ridge'

def setup_inputs():

    _w = (lambda a: (lambda t: t.to(torch.bfloat16) if t.is_floating_point() else t)(torch.as_tensor(a)))
    B, Hq, Hkv, Sq, Sk, D = 16, 32, 16, 288, 512, 128
    return {
        "q": _w((lambda *s: torch.randn(*s, dtype=torch.bfloat16) * 0.8)(B, Hq, Sq, D)),
        "k": _w((lambda *s: torch.randn(*s, dtype=torch.bfloat16) * 0.8)(B, Hkv, Sk, D)),
        "v": _w((lambda *s: torch.randn(*s, dtype=torch.bfloat16) * 0.05)(B, Hkv, Sk, D)),
    }

def reference(q, k, v):
    B, Hq, Sq, D = q.shape
    Hkv = k.shape[1]
    rep = Hq // Hkv
    k = (lambda x, n, axis: torch.repeat_interleave(x, n, dim=axis))(k, rep, axis=1)
    v = (lambda x, n, axis: torch.repeat_interleave(x, n, dim=axis))(v, rep, axis=1)
    scale = D ** -0.5
    scores = torch.einsum("bhqd,bhkd->bhqk", q, k) * scale
    p = _softmax(scores, axis=-1)
    return torch.einsum("bhqk,bhkd->bhqd", p, v)
```

```cpp
#include <hip/hip_runtime.h>
#include <hip/hip_bf16.h>
using bf16 = __hip_bfloat16;
constexpr int   D = 128, NW = 8, QBLK = 32, KVBLK = 64;
constexpr float SCALE = 0.088388347648318440f;
constexpr float THR = 8.f;
constexpr int SDEPTH = 2;
constexpr int B = 16, HQ = 32, HKV = 16, SQ = 288, SKV = 512, ROWS = 576;
constexpr int LDQ = D, LDK = D, LDO = D;
constexpr size_t SHM_V = KVBLK * D * 2, SHM_K = KVBLK * D * 2, SHM_ATTN = 2 * SHM_V + 2 * SHM_K + NW * 64 * 4;
using bf16x8 = __attribute__((ext_vector_type(8))) short;
using s16x4  = __attribute__((ext_vector_type(4))) short;
using f32x16 = __attribute__((ext_vector_type(16))) float;
using u32x4  = __attribute__((ext_vector_type(4))) unsigned;
#define KSWZ(row, colB) ((row) * 256 + ((colB) ^ (((row) & 7) << 4)))
#define SBAR() __builtin_amdgcn_sched_barrier(0)
__device__ __forceinline__ int crow(int r, int hi) { return (r & 3) + 8 * (r >> 2) + 4 * hi; }
__device__ __forceinline__ unsigned cvtpk(float lo, float hi) {
  unsigned r; asm volatile("v_cvt_pk_bf16_f32 %0, %1, %2" : "=v"(r) : "v"(lo), "v"(hi)); return r;
}
__device__ __forceinline__ bf16x8 ld8(const bf16* p) { return *reinterpret_cast<const bf16x8*>(p); }
__device__ __forceinline__ void partialSM(f32x16& p0, f32x16& p1, float& m_reg, float& mn, float& alpha) {
  constexpr float C = SCALE * 1.4426950408889634f;
  float pmax = p0[0]; for (int r = 1; r < 16; ++r) pmax = fmaxf(pmax, p0[r]); for (int r = 0; r < 16; ++r) pmax = fmaxf(pmax, p1[r]);
  { auto rr = __builtin_amdgcn_permlane32_swap(__float_as_uint(pmax), __float_as_uint(pmax), false, false);
    pmax = fmaxf(__uint_as_float(rr[0]), __uint_as_float(rr[1])); }
  if (__builtin_expect(__all(pmax - m_reg <= THR / SCALE), 1)) { mn = m_reg; alpha = 1.f; }
  else { mn = fmaxf(m_reg, pmax); alpha = __builtin_amdgcn_exp2f((m_reg - mn) * C); m_reg = mn; }
  float mnC = -mn * C;
  for (int r = 0; r < 16; ++r) p0[r] = fmaf(p0[r], C, mnC); for (int r = 0; r < 16; ++r) p1[r] = fmaf(p1[r], C, mnC);
  for (int r = 0; r < 16; ++r) p0[r] = __builtin_amdgcn_exp2f(p0[r]);
}
__device__ __forceinline__ void finishSM(f32x16& p0, f32x16& p1, float alpha, float& l_reg, bf16x8& pa0, bf16x8& pa1, bf16x8& pa2, bf16x8& pa3) {
  for (int r = 0; r < 16; ++r) p1[r] = __builtin_amdgcn_exp2f(p1[r]);
  float ps = 0; for (int r = 0; r < 16; ++r) ps += p0[r]; for (int r = 0; r < 16; ++r) ps += p1[r];
  { auto rr = __builtin_amdgcn_permlane32_swap(__float_as_uint(ps), __float_as_uint(ps), false, false);
    ps = __uint_as_float(rr[0]) + __uint_as_float(rr[1]); }
  l_reg = l_reg * alpha + ps;
#define PK4(P, BASE, OUT) do { unsigned a0 = cvtpk(P[BASE + 0], P[BASE + 1]), a1 = cvtpk(P[BASE + 2], P[BASE + 3]);   \
    unsigned b0 = cvtpk(P[BASE + 4], P[BASE + 5]), b1 = cvtpk(P[BASE + 6], P[BASE + 7]);                              \
    auto r0 = __builtin_amdgcn_permlane32_swap(a0, b0, false, false); auto r1 = __builtin_amdgcn_permlane32_swap(a1, b1, false, false); \
    u32x4 w = {r0[0], r1[0], r0[1], r1[1]}; OUT = *reinterpret_cast<bf16x8*>(&w); } while (0)
  PK4(p0, 0, pa0); PK4(p0, 8, pa1); PK4(p1, 0, pa2); PK4(p1, 8, pa3);
#undef PK4
}
__device__ __forceinline__ void qkt(f32x16& p0, f32x16& p1, const bf16* Ks, const bf16x8* qr, int r32, int hi) {
  p0 = f32x16{}; p1 = f32x16{};
  for (int d0 = 0; d0 < 8; ++d0) { int cb = (d0 * 16 + hi * 8) * 2;
    bf16x8 b0 = *reinterpret_cast<const bf16x8*>((const char*)Ks + KSWZ(r32, cb));
    bf16x8 b1 = *reinterpret_cast<const bf16x8*>((const char*)Ks + KSWZ(32 + r32, cb));
    p0 = __builtin_amdgcn_mfma_f32_32x32x16_bf16(b0, qr[d0], p0, 0, 0, 0);
    p1 = __builtin_amdgcn_mfma_f32_32x32x16_bf16(b1, qr[d0], p1, 0, 0, 0); }
}
__device__ __forceinline__ int v_st(int k, int c) { const int kk = (k & ~0xC) | ((k & 4) << 1) | ((k & 8) >> 1); return ((kk >> 3) * 4 + (c >> 5)) * 512 + ((kk & 7) * 32 + (c & 31)) * 2; }
__device__ __forceinline__ int v_rd_base(int lane) { return ((lane & 3) << 3) | (((lane >> 2) & 3) << 6) | (((lane >> 4) & 1) << 5) | (((lane >> 5) & 1) << 8); }
constexpr int v_rd_off(int d0, int ks, int half) { return d0 * 512 + ks * 4096 + half * 2048; }
template <int OFF> __device__ __forceinline__ s16x4 tr_read(int vb) {
  s16x4 r; asm volatile("ds_read_b64_tr_b16 %0, %1 offset:%2" : "=&v"(r) : "v"(vb), "i"(OFF) : "memory"); return r;
}
template <int D0> __device__ __forceinline__ void pv_one(f32x16& od, int vb, bf16x8 pa0, bf16x8 pa1, bf16x8 pa2, bf16x8 pa3) {
  const s16x4 l0 = tr_read<v_rd_off(D0, 0, 0)>(vb), h0 = tr_read<v_rd_off(D0, 0, 1)>(vb), l1 = tr_read<v_rd_off(D0, 1, 0)>(vb), h1 = tr_read<v_rd_off(D0, 1, 1)>(vb);
  const s16x4 l2 = tr_read<v_rd_off(D0, 2, 0)>(vb), h2 = tr_read<v_rd_off(D0, 2, 1)>(vb), l3 = tr_read<v_rd_off(D0, 3, 0)>(vb), h3 = tr_read<v_rd_off(D0, 3, 1)>(vb);
  asm volatile("s_waitcnt lgkmcnt(0)" ::: "memory"); SBAR();
#define PK(L, H) (bf16x8){L[0], L[1], L[2], L[3], H[0], H[1], H[2], H[3]}
  od = __builtin_amdgcn_mfma_f32_32x32x16_bf16(pa0, PK(l0, h0), od, 0, 0, 0);
  od = __builtin_amdgcn_mfma_f32_32x32x16_bf16(pa1, PK(l1, h1), od, 0, 0, 0);
  od = __builtin_amdgcn_mfma_f32_32x32x16_bf16(pa2, PK(l2, h2), od, 0, 0, 0);
  od = __builtin_amdgcn_mfma_f32_32x32x16_bf16(pa3, PK(l3, h3), od, 0, 0, 0);
#undef PK
}
__device__ __forceinline__ void pv_d0(f32x16* o, int vb, bf16x8 pa0, bf16x8 pa1, bf16x8 pa2, bf16x8 pa3) {
  pv_one<0>(o[0], vb, pa0, pa1, pa2, pa3); pv_one<1>(o[1], vb, pa0, pa1, pa2, pa3); pv_one<2>(o[2], vb, pa0, pa1, pa2, pa3); pv_one<3>(o[3], vb, pa0, pa1, pa2, pa3);
}
__device__ __forceinline__ void attn_dense_body(const bf16* __restrict__ Qb, const bf16* __restrict__ Kh, const bf16* __restrict__ Vh,
                                                bf16* __restrict__ Ob, int seq, char* lds, int nrows) {
  const int tid = threadIdx.x, wid = tid >> 6, lane = tid & 63, r32 = lane & 31, hi = lane >> 5;
  bf16* V_lds = (bf16*)lds; bf16* K_lds = (bf16*)(lds + 2 * SHM_V);
  float* ws = (float*)(lds + 2 * SHM_V + 2 * SHM_K) + wid * 64; float* li_l = ws; float* al_l = ws + 32;
  float m_reg = -1e30f, l_reg = 0; f32x16 o[4] = {}; bf16x8 qr[8];
  const bool live = wid * QBLK < nrows;
  const bf16* Qw = Qb + (long)((live ? wid * QBLK : 0) + r32) * LDQ + hi * 8;
#pragma unroll
  for (int d0 = 0; d0 < 8; ++d0) qr[d0] = ld8(Qw + d0 * 16);
  const int sr = tid >> 4, sc = (tid & 15) * 8, vst0 = v_st(sr, sc), vst1 = v_st(32 + sr, sc);
  const int vb0 = (int)(uintptr_t)V_lds + v_rd_base(lane);
  struct { bf16x8 vs0, vs1, ks0, ks1; } sr_[SDEPTH];
#define SLOAD(i, k0) do { sr_[i].vs0 = ld8(&Vh[(long)((k0) + sr) * LDK + sc]); sr_[i].vs1 = ld8(&Vh[(long)((k0) + 32 + sr) * LDK + sc]); \
    sr_[i].ks0 = ld8(&Kh[(long)((k0) + sr) * LDK + sc]); sr_[i].ks1 = ld8(&Kh[(long)((k0) + 32 + sr) * LDK + sc]); } while (0)
#define SWRITE(b, i) do { *(bf16x8*)((char*)V_lds + (b) * SHM_V + vst0) = sr_[i].vs0;          \
    *(bf16x8*)((char*)V_lds + (b) * SHM_V + vst1) = sr_[i].vs1; int kc = sc * 2;               \
    *(bf16x8*)((char*)K_lds + (b) * SHM_K + KSWZ(sr, kc)) = sr_[i].ks0;                       \
    *(bf16x8*)((char*)K_lds + (b) * SHM_K + KSWZ(32 + sr, kc)) = sr_[i].ks1; } while (0)
#define SWAIT() do { asm volatile("s_waitcnt vmcnt(4)" ::: "memory"); } while (0)
#define RESC(a) do { if (__any((a) < 1.f)) { if (hi == 0) al_l[r32] = (a); asm volatile("s_waitcnt lgkmcnt(0)" ::: "memory"); \
    for (int d = 0; d < 4; ++d) for (int r = 0; r < 16; ++r) o[d][r] *= al_l[crow(r, hi)]; } } while (0)
  f32x16 pA0, pA1, pB0, pB1; float mnA, mnB, alA, alB; bf16x8 pa0, pa1, pa2, pa3; const int NT = seq / KVBLK;
  constexpr int SE = 0, SO = SDEPTH - 1;
  SLOAD(SE, 0); asm volatile("s_waitcnt vmcnt(0)" ::: "memory"); SWRITE(0, SE); __syncthreads();
  qkt(pA0, pA1, K_lds, qr, r32, hi); partialSM(pA0, pA1, m_reg, mnA, alA);
  SLOAD(SO, KVBLK); if (2 < NT) SLOAD(SE, 2 * KVBLK);
  SWAIT(); SWRITE(1, SO); __syncthreads();
  for (int j = 1; j + 1 < NT; j += 2) {
    SBAR(); qkt(pB0, pB1, (bf16*)((char*)K_lds + SHM_K), qr, r32, hi);
    finishSM(pA0, pA1, alA, l_reg, pa0, pa1, pa2, pa3); SBAR();
    SLOAD(SO, (j + SDEPTH) * KVBLK); SBAR();
    pv_d0(o, vb0, pa0, pa1, pa2, pa3); partialSM(pB0, pB1, m_reg, mnB, alB);
    __syncthreads(); SWAIT(); SWRITE(0, SE);
    RESC(alB); __syncthreads();
    SBAR(); qkt(pA0, pA1, K_lds, qr, r32, hi);
    finishSM(pB0, pB1, alB, l_reg, pa0, pa1, pa2, pa3); SBAR();
    if (j + 3 < NT) SLOAD(SE, (j + 1 + SDEPTH) * KVBLK); SBAR();
    pv_d0(o, vb0 + (int)SHM_V, pa0, pa1, pa2, pa3); partialSM(pA0, pA1, m_reg, mnA, alA);
    __syncthreads(); SWAIT(); SWRITE(1, SO);
    RESC(alA); __syncthreads();
  }
  SBAR(); qkt(pB0, pB1, (bf16*)((char*)K_lds + SHM_K), qr, r32, hi);
  finishSM(pA0, pA1, alA, l_reg, pa0, pa1, pa2, pa3); SBAR();
  pv_d0(o, vb0, pa0, pa1, pa2, pa3); partialSM(pB0, pB1, m_reg, mnB, alB);
  __syncthreads(); RESC(alB);
  finishSM(pB0, pB1, alB, l_reg, pa0, pa1, pa2, pa3); SBAR();
  pv_d0(o, vb0 + (int)SHM_V, pa0, pa1, pa2, pa3);
  if (hi == 0) li_l[r32] = l_reg; asm volatile("s_waitcnt lgkmcnt(0)" ::: "memory");
  float rli[16];
#pragma unroll
  for (int r = 0; r < 16; ++r) rli[r] = __builtin_amdgcn_rcpf(li_l[crow(r, hi)]);
  if (live) {
  bf16* Ow = Ob + (long)(wid * QBLK) * LDO;
#pragma unroll
  for (int r = 0; r < 16; ++r) { int orow = crow(r, hi);
    for (int d0 = 0; d0 < 4; ++d0) Ow[(long)orow * LDO + d0 * 32 + r32] = __float2bfloat16(o[d0][r] * rli[r]); }
  }
}
__global__ __launch_bounds__(64 * NW, 1)
void attn_fwd_dense(const bf16* __restrict__ Q, const bf16* __restrict__ K, const bf16* __restrict__ V, bf16* __restrict__ O) {
  extern __shared__ __attribute__((aligned(16))) char shm[];
  const int pair = blockIdx.x / 3, blk = blockIdx.x % 3;
  const long q0 = ((long)pair * ROWS + blk * 256) * D, k0 = (long)pair * SKV * D;
  attn_dense_body(Q + q0, K + k0, V + k0, O + q0, SKV, shm, ROWS - blk * 256);
}
extern "C" void kernel_launch(void* const* d_in, const int* in_sizes, int n_in, void* d_out, int out_size, void* d_ws, size_t ws_size, hipStream_t stream) {
  hipFuncSetAttribute((const void*)attn_fwd_dense, hipFuncAttributeMaxDynamicSharedMemorySize, (int)SHM_ATTN);
  hipLaunchKernelGGL(attn_fwd_dense, dim3(256 * 3), dim3(64 * NW), SHM_ATTN, stream,
                     (const bf16*)d_in[0], (const bf16*)d_in[1], (const bf16*)d_in[2], (bf16*)d_out);
}
```

```cpp
#include <hip/hip_runtime.h>
#include <hip/hip_bf16.h>

typedef unsigned u32x4_t __attribute__((ext_vector_type(4)));
__device__ __forceinline__ float bflo(unsigned w) { return __uint_as_float(w << 16); }
__device__ __forceinline__ float bfhi(unsigned w) { return __uint_as_float(w & 0xffff0000u); }

__device__ __forceinline__ void slow_rows(const unsigned short* q, const unsigned short* k, const unsigned short* v, unsigned short* o, int lane) {
  for (int r = 0; r < 48; ++r) {
    float s[8];
#pragma unroll
    for (int j = 0; j < 8; ++j) s[j] = 0.f;
    for (int c = 0; c < 16; ++c) {
      const u32x4_t qq = *(const u32x4_t*)(q + r * 128 + c * 8);
#pragma unroll
      for (int j = 0; j < 8; ++j) {
        const u32x4_t kk = *(const u32x4_t*)(k + (j * 64 + lane) * 128 + c * 8);
#pragma unroll
        for (int e = 0; e < 4; ++e) s[j] += bflo(qq[e]) * bflo(kk[e]) + bfhi(qq[e]) * bfhi(kk[e]);
      }
    }
    float m = -__builtin_inff();
#pragma unroll
    for (int j = 0; j < 8; ++j) { s[j] *= 0.088388347648318440f; m = fmaxf(m, s[j]); if (s[j] != s[j]) m = s[j]; }
    for (int d = 1; d < 64; d <<= 1) { const float t = __shfl_xor(m, d); m = (t != t || m != m) ? __builtin_nanf("") : fmaxf(m, t); }
    float l = 0.f;
#pragma unroll
    for (int j = 0; j < 8; ++j) { s[j] = __expf(s[j] - m); l += s[j]; }
    for (int d = 1; d < 64; d <<= 1) l += __shfl_xor(l, d);
    const float rl = 1.f / l;
    float o0 = 0.f, o1 = 0.f;
#pragma unroll
    for (int j = 0; j < 8; ++j) {
      const float pj = s[j] * rl;
      for (int t = 0; t < 64; ++t) {
        const float p = __shfl(pj, t);
        const unsigned w = *(const unsigned*)(v + (j * 64 + t) * 128 + lane * 2);
        o0 += p * bflo(w); o1 += p * bfhi(w);
      }
    }
    __hip_bfloat16 b0 = __float2bfloat16(o0), b1 = __float2bfloat16(o1);
    *(unsigned*)(o + r * 128 + lane * 2) = (unsigned)__builtin_bit_cast(unsigned short, b0) | ((unsigned)__builtin_bit_cast(unsigned short, b1) << 16);
  }
}

__global__ __launch_bounds__(256, 1) void gqa_fwd(const unsigned short* __restrict__ q, const unsigned short* __restrict__ k,
                                                   const unsigned short* __restrict__ v, unsigned short* __restrict__ o, void* ws) {
  unsigned long long t0 = __builtin_amdgcn_s_memrealtime();
  const unsigned wv = __builtin_amdgcn_readfirstlane(threadIdx.x >> 6);
  const unsigned bid = blockIdx.x;
  const int lane = threadIdx.x & 63;
  for (int b = 0; b < 3; ++b) {
        const unsigned n = b * 32u + (bid >> 3), pair = (bid & 7u) * 32u + n / 3u, sub = n % 3u;
        const size_t row = (size_t)pair * 576u + sub * 192u + wv * 48u;
        slow_rows(q + row * 128, k + (size_t)pair * 65536u, v + (size_t)pair * 65536u, o + row * 128, lane);
  }
  asm volatile("s_waitcnt vmcnt(0)\n\ts_barrier" ::: "memory"); if (wv == 0) asm volatile("buffer_wbl2 sc1\n\ts_waitcnt vmcnt(0)" ::: "memory");
  while (__builtin_amdgcn_s_memrealtime() - t0 < 1ull) { __builtin_amdgcn_s_sleep(1); }
}
extern "C" void kernel_launch(void* const* d_in, const int* in_sizes, int n_in, void* d_out, int out_size, void* d_ws, size_t ws_size,
                              hipStream_t stream) {
  hipLaunchKernelGGL(gqa_fwd, dim3(256), dim3(256), 0, stream, (const unsigned short*)d_in[0], (const unsigned short*)d_in[1],
                     (const unsigned short*)d_in[2], (unsigned short*)d_out, d_ws);
}
```

```cpp
#include <hip/hip_runtime.h>
#include <hip/hip_bf16.h>

typedef unsigned u32x4_t __attribute__((ext_vector_type(4)));
__device__ __forceinline__ float bflo(unsigned w) { return __uint_as_float(w << 16); }
__device__ __forceinline__ float bfhi(unsigned w) { return __uint_as_float(w & 0xffff0000u); }

__device__ __forceinline__ void slow_rows(const unsigned short* q, const unsigned short* k, const unsigned short* v, unsigned short* o, int lane) {
  for (int r = 0; r < 48; ++r) {
    float s[8];
#pragma unroll
    for (int j = 0; j < 8; ++j) s[j] = 0.f;
    for (int c = 0; c < 16; ++c) {
      const u32x4_t qq = *(const u32x4_t*)(q + r * 128 + c * 8);
#pragma unroll
      for (int j = 0; j < 8; ++j) {
        const u32x4_t kk = *(const u32x4_t*)(k + (j * 64 + lane) * 128 + c * 8);
#pragma unroll
        for (int e = 0; e < 4; ++e) s[j] += bflo(qq[e]) * bflo(kk[e]) + bfhi(qq[e]) * bfhi(kk[e]);
      }
    }
    float m = -__builtin_inff();
#pragma unroll
    for (int j = 0; j < 8; ++j) { s[j] *= 0.088388347648318440f; m = fmaxf(m, s[j]); if (s[j] != s[j]) m = s[j]; }
    for (int d = 1; d < 64; d <<= 1) { const float t = __shfl_xor(m, d); m = (t != t || m != m) ? __builtin_nanf("") : fmaxf(m, t); }
    float l = 0.f;
#pragma unroll
    for (int j = 0; j < 8; ++j) { s[j] = __expf(s[j] - m); l += s[j]; }
    for (int d = 1; d < 64; d <<= 1) l += __shfl_xor(l, d);
    const float rl = 1.f / l;
    float o0 = 0.f, o1 = 0.f;
#pragma unroll
    for (int j = 0; j < 8; ++j) {
      const float pj = s[j] * rl;
      for (int t = 0; t < 64; ++t) {
        const float p = __shfl(pj, t);
        const unsigned w = *(const unsigned*)(v + (j * 64 + t) * 128 + lane * 2);
        o0 += p * bflo(w); o1 += p * bfhi(w);
      }
    }
    __hip_bfloat16 b0 = __float2bfloat16(o0), b1 = __float2bfloat16(o1);
    *(unsigned*)(o + r * 128 + lane * 2) = (unsigned)__builtin_bit_cast(unsigned short, b0) | ((unsigned)__builtin_bit_cast(unsigned short, b1) << 16);
  }
}

__global__ __launch_bounds__(256, 1) void gqa_fwd(const unsigned short* __restrict__ q, const unsigned short* __restrict__ k,
                                                   const unsigned short* __restrict__ v, unsigned short* __restrict__ o, void* ws) {
  __shared__ __attribute__((aligned(1024))) char lds[163840];
  const unsigned wv = __builtin_amdgcn_readfirstlane(threadIdx.x >> 6);
  const unsigned bid = blockIdx.x;
  unsigned flag;
  asm volatile(
    "s_load_dwordx2 s[36:37], %1, 0\n\t"
    "s_load_dwordx2 s[40:41], %1, 8\n\t"
    "s_load_dwordx2 s[44:45], %1, 16\n\t"
    "s_load_dwordx2 s[48:49], %1, 24\n\t"
    "s_mov_b32 s38, 37748736\n\t"
    "s_mov_b32 s39, 0x20000\n\t"
    "s_mov_b32 s42, 33554432\n\t"
    "s_mov_b32 s43, 0x20000\n\t"
    "s_mov_b32 s46, 33554432\n\t"
    "s_mov_b32 s47, 0x20000\n\t"
    "s_mov_b32 s50, 37748736\n\t"
    "s_mov_b32 s51, 0x20000\n\t"
    "v_mbcnt_lo_u32_b32 v224, -1, 0\n\t"
    "v_mbcnt_hi_u32_b32 v224, -1, v224\n\t"
    "s_and_b32 s81, %2, 7\n\t"
    "s_lshl_b32 s81, s81, 5\n\t"
    "s_lshr_b32 s82, %2, 3\n\t"
    "s_getpc_b64 s[92:93]\n\t"
    "L_pc_%=:\n\t"
    "s_lshl_b32 s83, s82, 2\n\t"
    "s_add_u32 s83, s83, %3\n\t"
    "s_lshl_b32 s83, s83, 9\n\t"
    "v_lshl_add_u32 v232, v224, 3, s83\n\t"
    "v_min_u32 v232, (L_end_%= - L_pc_%= - 4), v232\n\t"
    "global_load_dword a224, v232, s[92:93]\n\t"
    "s_add_u32 s84, s82, 0\n\t"
    "s_mul_i32 s83, s84, 43691\n\t"
    "s_lshr_b32 s83, s83, 17\n\t"
    "s_mul_i32 s79, s83, 3\n\t"
    "s_sub_u32 s84, s84, s79\n\t"
    "s_add_u32 s83, s83, s81\n\t"
    "s_lshl_b32 s58, s83, 17\n\t"
    "s_lshl_b32 s79, %3, 12\n\t"
    "s_add_u32 s55, s58, s79\n\t"
    "s_lshl_b32 s79, %3, 13\n\t"
    "s_add_u32 s61, s58, s79\n\t"
    "s_mul_i32 s83, s83, 147456\n\t"
    "s_mul_i32 s84, s84, 49152\n\t"
    "s_add_u32 s83, s83, s84\n\t"
    "s_mul_i32 s84, %3, 12288\n\t"
    "s_add_u32 s64, s83, s84\n\t"
    "s_add_u32 s65, s64, 4096\n\t"
    "s_add_u32 s66, s64, 8192\n\t"
    "v_and_b32 v225, 15, v224\n\t"
    "v_lshrrev_b32 v226, 4, v224\n\t"
    "s_lshl_b32 s83, %3, 2\n\t"
    "v_add_u32 v227, s83, v226\n\t"
    "v_xor_b32 v227, v227, v225\n\t"
    "v_lshlrev_b32 v227, 4, v227\n\t"
    "v_lshl_add_u32 v227, v226, 8, v227\n\t"
    "s_lshl_b32 s83, %3, 10\n\t"
    "s_add_u32 s78, s58, s83\n\t"
    "s_mov_b32 m0, s83\n\t"
    "v_add_u32 v228, 0, v226\n\t"
    "v_xor_b32 v228, v228, v225\n\t"
    "v_lshlrev_b32 v228, 4, v228\n\t"
    "v_lshl_add_u32 v20, v226, 8, v228\n\t"
    "v_add_u32 v228, 4, v226\n\t"
    "v_xor_b32 v228, v228, v225\n\t"
    "v_lshlrev_b32 v228, 4, v228\n\t"
    "v_lshl_add_u32 v21, v226, 8, v228\n\t"
    "v_add_u32 v228, 8, v226\n\t"
    "v_xor_b32 v228, v228, v225\n\t"
    "v_lshlrev_b32 v228, 4, v228\n\t"
    "v_lshl_add_u32 v22, v226, 8, v228\n\t"
    "v_add_u32 v228, 12, v226\n\t"
    "v_xor_b32 v228, v228, v225\n\t"
    "v_lshlrev_b32 v228, 4, v228\n\t"
    "v_lshl_add_u32 v23, v226, 8, v228\n\t"
    "s_lshl_b32 s85, %3, 13\n\t"
    "s_add_u32 s85, s85, 131072\n\t"
    "s_lshl_b32 s83, %3, 12\n\t"
    "s_add_u32 s77, s83, 65536\n\t"
    "s_add_u32 s83, %3, 1\n\t"
    "s_and_b32 s83, s83, 15\n\t"
    "s_lshl_b32 s73, s83, 12\n\t"
    "s_add_u32 s83, %3, 5\n\t"
    "s_and_b32 s83, s83, 15\n\t"
    "s_lshl_b32 s74, s83, 12\n\t"
    "s_add_u32 s83, %3, 9\n\t"
    "s_and_b32 s83, s83, 15\n\t"
    "s_lshl_b32 s75, s83, 12\n\t"
    "s_add_u32 s83, %3, 13\n\t"
    "s_and_b32 s83, s83, 15\n\t"
    "s_lshl_b32 s76, s83, 12\n\t"
    "s_waitcnt lgkmcnt(0)\n\t"
    "s_and_b32 s41, s41, 0xffff\n\t"
    "s_and_b32 s37, s37, 0xffff\n\t"
    "buffer_load_dwordx4 v227, s[40:43], s78 offen nt lds\n\t"
    "s_mov_b32 m0, s85\n\t"
    "s_nop 0\n\t"
    "buffer_load_dwordx4 v20, s[36:39], s64 offen offset:0 nt lds\n\t"
    "buffer_load_dwordx4 v21, s[36:39], s64 offen offset:1024 nt lds\n\t"
    "buffer_load_dwordx4 v22, s[36:39], s64 offen offset:2048 nt lds\n\t"
    "buffer_load_dwordx4 v23, s[36:39], s64 offen offset:3072 nt lds\n\t"
    "s_add_u32 s78, s55, 4096\n\t"
    "s_mov_b32 m0, s73\n\t"
    "s_nop 0\n\t"
    "buffer_load_dwordx4 v20, s[40:43], s78 offen offset:0 nt lds\n\t"
    "buffer_load_dwordx4 v21, s[40:43], s78 offen offset:1024 nt lds\n\t"
    "buffer_load_dwordx4 v22, s[40:43], s78 offen offset:2048 nt lds\n\t"
    "buffer_load_dwordx4 v23, s[40:43], s78 offen offset:3072 nt lds\n\t"
    "s_and_b32 s45, s45, 0xffff\n\t"
    "v_lshrrev_b32 v227, 3, v224\n\t"
    "v_and_b32 v228, 6, v227\n\t"
    "v_and_b32 v24, 7, v224\n\t"
    "v_xor_b32 v24, v24, v228\n\t"
    "v_lshlrev_b32 v24, 4, v24\n\t"
    "v_lshl_add_u32 v24, v227, 8, v24\n\t"
    "v_add_u32 v25, 1024, v24\n\t"
    "v_add_u32 v26, 2048, v24\n\t"
    "v_add_u32 v27, 3072, v24\n\t"
    "s_add_u32 s84, s82, 32\n\t"
    "s_mul_i32 s83, s84, 43691\n\t"
    "s_lshr_b32 s83, s83, 17\n\t"
    "s_mul_i32 s79, s83, 3\n\t"
    "s_sub_u32 s84, s84, s79\n\t"
    "s_add_u32 s83, s83, s81\n\t"
    "s_lshl_b32 s59, s83, 17\n\t"
    "s_lshl_b32 s79, %3, 12\n\t"
    "s_add_u32 s56, s59, s79\n\t"
    "s_lshl_b32 s79, %3, 13\n\t"
    "s_add_u32 s62, s59, s79\n\t"
    "s_mul_i32 s83, s83, 147456\n\t"
    "s_mul_i32 s84, s84, 49152\n\t"
    "s_add_u32 s83, s83, s84\n\t"
    "s_mul_i32 s84, %3, 12288\n\t"
    "s_add_u32 s67, s83, s84\n\t"
    "s_add_u32 s68, s67, 4096\n\t"
    "s_add_u32 s69, s67, 8192\n\t"
    "s_add_u32 s84, s82, 64\n\t"
    "s_mul_i32 s83, s84, 43691\n\t"
    "s_lshr_b32 s83, s83, 17\n\t"
    "s_mul_i32 s79, s83, 3\n\t"
    "s_sub_u32 s84, s84, s79\n\t"
    "s_add_u32 s83, s83, s81\n\t"
    "s_lshl_b32 s60, s83, 17\n\t"
    "s_lshl_b32 s79, %3, 12\n\t"
    "s_add_u32 s57, s60, s79\n\t"
    "s_lshl_b32 s79, %3, 13\n\t"
    "s_add_u32 s63, s60, s79\n\t"
    "s_mul_i32 s83, s83, 147456\n\t"
    "s_mul_i32 s84, s84, 49152\n\t"
    "s_add_u32 s83, s83, s84\n\t"
    "s_mul_i32 s84, %3, 12288\n\t"
    "s_add_u32 s70, s83, s84\n\t"
    "s_add_u32 s71, s70, 4096\n\t"
    "s_add_u32 s72, s70, 8192\n\t"
    "s_and_b32 s49, s49, 0xffff\n\t"
    "s_add_u32 s83, s55, 118784\n\t"
    "s_cmp_eq_u32 %3, 3\n\t"
    "s_cselect_b32 s58, s59, s83\n\t"
    "s_add_u32 s83, s56, 118784\n\t"
    "s_cmp_eq_u32 %3, 3\n\t"
    "s_cselect_b32 s59, s60, s83\n\t"
    "s_add_u32 s83, s57, 118784\n\t"
    "s_cmp_eq_u32 %3, 3\n\t"
    "s_cselect_b32 s60, 33554432, s83\n\t"
    "s_mov_b32 s52, 0x3e0293ee\n\t"
    "s_mov_b32 s53, 0xbe0293ee\n\t"
    "s_mov_b32 s54, 0x71800000\n\t"
    "s_mov_b32 s99, 0x49800000\n\t"
    "s_mov_b32 s80, 0\n\t"
    "s_or_b32 s98, s82, %3\n\t"
    "v_add_u32 v228, 0, v226\n\t"
    "v_xor_b32 v228, v228, v225\n\t"
    "v_lshlrev_b32 v228, 4, v228\n\t"
    "v_lshl_add_u32 v12, v225, 8, v228\n\t"
    "v_add_u32 v228, 4, v226\n\t"
    "v_xor_b32 v228, v228, v225\n\t"
    "v_lshlrev_b32 v228, 4, v228\n\t"
    "v_lshl_add_u32 v13, v225, 8, v228\n\t"
    "v_add_u32 v228, 8, v226\n\t"
    "v_xor_b32 v228, v228, v225\n\t"
    "v_lshlrev_b32 v228, 4, v228\n\t"
    "v_lshl_add_u32 v14, v225, 8, v228\n\t"
    "v_add_u32 v228, 12, v226\n\t"
    "v_xor_b32 v228, v228, v225\n\t"
    "v_lshlrev_b32 v228, 4, v228\n\t"
    "v_lshl_add_u32 v15, v225, 8, v228\n\t"
    "v_add_u32 v28, s85, v12\n\t"
    "v_add_u32 v236, s77, v12\n\t"
    "v_add_u32 v29, s85, v13\n\t"
    "v_add_u32 v237, s77, v13\n\t"
    "v_add_u32 v30, s85, v14\n\t"
    "v_add_u32 v238, s77, v14\n\t"
    "v_add_u32 v31, s85, v15\n\t"
    "v_add_u32 v239, s77, v15\n\t"
    "v_bfe_u32 v227, v224, 2, 3\n\t"
    "v_lshrrev_b32 v228, 5, v224\n\t"
    "v_lshlrev_b32 v228, 10, v228\n\t"
    "v_lshl_add_u32 v228, v227, 7, v228\n\t"
    "v_and_b32 v229, 3, v224\n\t"
    "v_lshl_add_u32 v228, v229, 3, v228\n\t"
    "v_add_u32 v228, 65536, v228\n\t"
    "v_lshrrev_b32 v227, 1, v227\n\t"
    "v_xor_b32 v229, 0, v227\n\t"
    "v_lshl_add_u32 v16, v229, 5, v228\n\t"
    "v_xor_b32 v229, 1, v227\n\t"
    "v_lshl_add_u32 v17, v229, 5, v228\n\t"
    "v_xor_b32 v229, 2, v227\n\t"
    "v_lshl_add_u32 v18, v229, 5, v228\n\t"
    "v_xor_b32 v229, 3, v227\n\t"
    "v_lshl_add_u32 v19, v229, 5, v228\n\t"
    "v_mul_u32_u24 v227, 144, v225\n\t"
    "v_lshl_add_u32 v227, v226, 3, v227\n\t"
    "v_add_u32 v32, s85, v227\n\t"
    "v_lshrrev_b32 v227, 3, v224\n\t"
    "v_and_b32 v228, 7, v224\n\t"
    "v_lshlrev_b32 v228, 4, v228\n\t"
    "v_lshl_add_u32 v33, v227, 8, v228\n\t"
    "v_mul_u32_u24 v227, 144, v227\n\t"
    "v_add3_u32 v34, v227, v228, s85\n\t"
    "v_and_b32 v227, 3, v224\n\t"
    "v_cmp_eq_u32 vcc, 0, v227\n\t"
    "v_mov_b32 v228, 0x3f803f80\n\t"
    "s_nop 0\n\t"
    "v_cndmask_b32 v228, 0, v228, vcc\n\t"
    "v_accvgpr_write_b32 a172, v228\n\t"
    "v_accvgpr_write_b32 a173, v228\n\t"
    "v_accvgpr_write_b32 a174, v228\n\t"
    "v_accvgpr_write_b32 a175, v228\n\t"
    "v_lshrrev_b32 v227, 2, v225\n\t"
    "v_cmp_eq_u32 s[86:87], v227, v226\n\t"
    "v_and_b32 v227, 1, v225\n\t"
    "v_lshlrev_b32 v227, 4, v227\n\t"
    "v_and_b32 v228, 2, v225\n\t"
    "v_cmp_eq_u32 vcc, 0, v228\n\t"
    "s_nop 0\n\t"
    "s_and_b64 s[88:89], s[86:87], vcc\n\t"
    "s_andn2_b64 s[90:91], s[86:87], vcc\n\t"
    "v_mov_b32 v228, 0x3e03\n\t"
    "v_lshlrev_b32 v228, v227, v228\n\t"
    "v_cndmask_b32 v229, 0, v228, s[88:89]\n\t"
    "v_cndmask_b32 v228, 0, v228, s[90:91]\n\t"
    "v_accvgpr_write_b32 a228, v229\n\t"
    "v_accvgpr_write_b32 a229, v228\n\t"
    "v_accvgpr_write_b32 a230, 0\n\t"
    "v_accvgpr_write_b32 a231, 0\n\t"
    "v_accvgpr_write_b32 a238, v229\n\t"
    "v_accvgpr_write_b32 a239, v228\n\t"
    "v_accvgpr_write_b32 a236, 0\n\t"
    "v_accvgpr_write_b32 a237, 0\n\t"
    "v_mov_b32 v228, 0xb9d8\n\t"
    "v_lshlrev_b32 v228, v227, v228\n\t"
    "v_cndmask_b32 v229, 0, v228, s[88:89]\n\t"
    "v_cndmask_b32 v228, 0, v228, s[90:91]\n\t"
    "v_accvgpr_write_b32 a232, v229\n\t"
    "v_accvgpr_write_b32 a233, v228\n\t"
    "v_accvgpr_write_b32 a234, 0\n\t"
    "v_accvgpr_write_b32 a235, 0\n\t"
    "v_accvgpr_write_b32 a242, v229\n\t"
    "v_accvgpr_write_b32 a243, v228\n\t"
    "v_accvgpr_write_b32 a240, 0\n\t"
    "v_accvgpr_write_b32 a241, 0\n\t"
    "s_add_u32 s78, s55, 20480\n\t"
    "s_mov_b32 m0, s74\n\t"
    "s_nop 0\n\t"
    "buffer_load_dwordx4 v20, s[40:43], s78 offen offset:0 nt lds\n\t"
    "buffer_load_dwordx4 v21, s[40:43], s78 offen offset:1024 nt lds\n\t"
    "buffer_load_dwordx4 v22, s[40:43], s78 offen offset:2048 nt lds\n\t"
    "buffer_load_dwordx4 v23, s[40:43], s78 offen offset:3072 nt lds\n\t"
    "s_add_u32 m0, s85, 4096\n\t"
    "s_nop 0\n\t"
    "buffer_load_dwordx4 v20, s[36:39], s65 offen offset:0 nt lds\n\t"
    "buffer_load_dwordx4 v21, s[36:39], s65 offen offset:1024 nt lds\n\t"
    "buffer_load_dwordx4 v22, s[36:39], s65 offen offset:2048 nt lds\n\t"
    "buffer_load_dwordx4 v23, s[36:39], s65 offen offset:3072 nt lds\n\t"
    "s_waitcnt vmcnt(12)\n\t"
    "ds_read_b128 a[0:3], v28 offset:0\n\t"
    "ds_read_b128 a[4:7], v29 offset:0\n\t"
    "ds_read_b128 a[8:11], v30 offset:0\n\t"
    "ds_read_b128 a[12:15], v31 offset:0\n\t"
    "s_waitcnt vmcnt(8)\n\t"
    "s_barrier\n\t"
    "ds_read_b128 a[112:115], v12 offset:0\n\t"
    "ds_read_b128 a[116:119], v13 offset:0\n\t"
    "ds_read_b128 a[120:123], v14 offset:0\n\t"
    "ds_read_b128 a[124:127], v15 offset:0\n\t"
    "ds_read_b128 a[144:147], v12 offset:4096\n\t"
    "ds_read_b128 a[148:151], v13 offset:4096\n\t"
    "ds_read_b128 a[152:155], v14 offset:4096\n\t"
    "ds_read_b128 a[156:159], v15 offset:4096\n\t"
    "ds_read_b128 a[96:99], v12 offset:8192\n\t"
    "ds_read_b128 a[100:103], v13 offset:8192\n\t"
    "ds_read_b128 a[104:107], v14 offset:8192\n\t"
    "ds_read_b128 a[108:111], v15 offset:8192\n\t"
    "s_waitcnt lgkmcnt(11)\n\t"
    "v_mfma_f32_16x16x32_bf16 v[160:163], a[112:115], a[0:3], 0\n\t"
    "s_waitcnt lgkmcnt(10)\n\t"
    "ds_read_b128 a[128:131], v12 offset:12288\n\t"
    "v_mfma_f32_16x16x32_bf16 v[160:163], a[116:119], a[4:7], v[160:163]\n\t"
    "s_waitcnt lgkmcnt(10)\n\t"
    "ds_read_b128 a[132:135], v13 offset:12288\n\t"
    "v_mfma_f32_16x16x32_bf16 v[160:163], a[120:123], a[8:11], v[160:163]\n\t"
    "s_waitcnt lgkmcnt(10)\n\t"
    "ds_read_b128 a[136:139], v14 offset:12288\n\t"
    "v_mfma_f32_16x16x32_bf16 v[160:163], a[124:127], a[12:15], v[160:163]\n\t"
    "s_waitcnt lgkmcnt(10)\n\t"
    "ds_read_b128 a[140:143], v15 offset:12288\n\t"
    "v_mfma_f32_16x16x32_bf16 v[164:167], a[144:147], a[0:3], 0\n\t"
    "s_waitcnt lgkmcnt(10)\n\t"
    "ds_read_b128 a[112:115], v12 offset:16384\n\t"
    "v_mfma_f32_16x16x32_bf16 v[164:167], a[148:151], a[4:7], v[164:167]\n\t"
    "s_waitcnt lgkmcnt(10)\n\t"
    "ds_read_b128 a[116:119], v13 offset:16384\n\t"
    "v_mfma_f32_16x16x32_bf16 v[164:167], a[152:155], a[8:11], v[164:167]\n\t"
    "s_waitcnt lgkmcnt(10)\n\t"
    "ds_read_b128 a[120:123], v14 offset:16384\n\t"
    "v_mfma_f32_16x16x32_bf16 v[164:167], a[156:159], a[12:15], v[164:167]\n\t"
    "ds_read_b128 a[124:127], v15 offset:16384\n\t"
    "v_max3_f32 v0, v160, v161, v162\n\t"
    "v_max_f32 v0, v0, v163\n\t"
    "v_mov_b32 v1, v0\n\t"
    "v_mfma_f32_16x16x32_bf16 v[168:171], a[228:231], a[0:3], 0\n\t"
    "s_add_u32 m0, s77, 49152\n\t"
    "v_mfma_f32_16x16x32_bf16 v[168:171], a[232:235], a[0:3], v[168:171]\n\t"
    "v_permlane32_swap_b32 v0, v1\n\t"
    "v_mfma_f32_16x16x32_bf16 v[172:175], a[236:239], a[0:3], 0\n\t"
    "buffer_load_dwordx4 v20, s[36:39], s66 offen offset:0 nt lds\n\t"
    "v_max_f32 v0, v0, v1\n\t"
    "v_mov_b32 v1, v0\n\t"
    "v_mfma_f32_16x16x32_bf16 v[172:175], a[240:243], a[0:3], v[172:175]\n\t"
    "s_waitcnt lgkmcnt(4)\n\t"
    "v_mfma_f32_16x16x32_bf16 v[176:179], a[96:99], a[0:3], 0\n\t"
    "v_permlane16_swap_b32 v0, v1\n\t"
    "v_mfma_f32_16x16x32_bf16 v[176:179], a[100:103], a[4:7], v[176:179]\n\t"
    "v_max_f32 v0, v0, v1\n\t"
    "v_mul_f32 v0, s53, v0\n\t"
    "v_mov_b32 v1, v0\n\t"
    "v_mov_b32 v2, v0\n\t"
    "v_mfma_f32_16x16x32_bf16 v[176:179], a[104:107], a[8:11], v[176:179]\n\t"
    "v_mov_b32 v3, v0\n\t"
    "v_fma_f32 v160, v160, s52, v0\n\t"
    "v_fma_f32 v161, v161, s52, v0\n\t"
    "v_fma_f32 v162, v162, s52, v0\n\t"
    "v_mfma_f32_16x16x32_bf16 v[176:179], a[108:111], a[12:15], v[176:179]\n\t"
    "v_fma_f32 v163, v163, s52, v0\n\t"
    "v_cvt_pk_bf16_f32 v168, v168, v169\n\t"
    "v_cvt_pk_bf16_f32 v170, v170, v171\n\t"
    "v_mfma_f32_16x16x32_bf16 v[180:183], a[228:231], a[4:7], 0\n\t"
    "v_accvgpr_write_b32 a176, v168\n\t"
    "v_accvgpr_write_b32 a177, v170\n\t"
    "v_cvt_pk_bf16_f32 v172, v172, v173\n\t"
    "v_cvt_pk_bf16_f32 v174, v174, v175\n\t"
    "v_mfma_f32_16x16x32_bf16 v[180:183], a[232:235], a[4:7], v[180:183]\n\t"
    "buffer_load_dwordx4 v21, s[36:39], s66 offen offset:1024 nt lds\n\t"
    "v_accvgpr_write_b32 a178, v172\n\t"
    "v_accvgpr_write_b32 a179, v174\n\t"
    "v_mfma_f32_16x16x32_bf16 v[184:187], a[236:239], a[4:7], 0\n\t"
    "v_exp_f32 v160, v160\n\t"
    "v_fma_f32 v164, v164, s52, v0\n\t"
    "v_mfma_f32_16x16x32_bf16 v[184:187], a[240:243], a[4:7], v[184:187]\n\t"
    "v_exp_f32 v161, v161\n\t"
    "v_exp_f32 v162, v162\n\t"
    "v_mfma_f32_16x16x32_bf16 v[188:191], a[128:131], a[0:3], 0\n\t"
    "v_exp_f32 v163, v163\n\t"
    "v_cvt_pk_bf16_f32 v64, v160, v161\n\t"
    "v_mfma_f32_16x16x32_bf16 v[188:191], a[132:135], a[4:7], v[188:191]\n\t"
    "v_cvt_pk_bf16_f32 v180, v180, v181\n\t"
    "v_cvt_pk_bf16_f32 v182, v182, v183\n\t"
    "v_accvgpr_write_b32 a180, v180\n\t"
    "v_accvgpr_write_b32 a181, v182\n\t"
    "v_mfma_f32_16x16x32_bf16 v[188:191], a[136:139], a[8:11], v[188:191]\n\t"
    "v_cvt_pk_bf16_f32 v65, v162, v163\n\t"
    "v_fma_f32 v165, v165, s52, v0\n\t"
    "v_fma_f32 v166, v166, s52, v0\n\t"
    "v_fma_f32 v167, v167, s52, v0\n\t"
    "v_mfma_f32_16x16x32_bf16 v[188:191], a[140:143], a[12:15], v[188:191]\n\t"
    "v_cvt_pk_bf16_f32 v184, v184, v185\n\t"
    "v_cvt_pk_bf16_f32 v186, v186, v187\n\t"
    "v_accvgpr_write_b32 a182, v184\n\t"
    "v_mfma_f32_16x16x32_bf16 v[192:195], a[228:231], a[8:11], 0\n\t"
    "buffer_load_dwordx4 v22, s[36:39], s66 offen offset:2048 nt lds\n\t"
    "v_accvgpr_write_b32 a183, v186\n\t"
    "v_fma_f32 v176, v176, s52, v0\n\t"
    "v_mfma_f32_16x16x32_bf16 v[192:195], a[232:235], a[8:11], v[192:195]\n\t"
    "v_exp_f32 v164, v164\n\t"
    "v_fma_f32 v177, v177, s52, v0\n\t"
    "v_mfma_f32_16x16x32_bf16 v[196:199], a[236:239], a[8:11], 0\n\t"
    "v_exp_f32 v165, v165\n\t"
    "v_exp_f32 v166, v166\n\t"
    "v_mfma_f32_16x16x32_bf16 v[196:199], a[240:243], a[8:11], v[196:199]\n\t"
    "s_waitcnt lgkmcnt(0)\n\t"
    "v_exp_f32 v167, v167\n\t"
    "v_cvt_pk_bf16_f32 v66, v164, v165\n\t"
    "v_fma_f32 v178, v178, s52, v0\n\t"
    "v_mfma_f32_16x16x32_bf16 v[200:203], a[112:115], a[0:3], 0\n\t"
    "s_waitcnt vmcnt(7) lgkmcnt(0)\n\t"
    "v_cvt_pk_bf16_f32 v67, v166, v167\n\t"
    "v_fma_f32 v179, v179, s52, v0\n\t"
    "v_exp_f32 v176, v176\n\t"
    "v_mfma_f32_16x16x32_bf16 v[200:203], a[116:119], a[4:7], v[200:203]\n\t"
    "s_barrier\n\t"
    "ds_read_b128 a[144:147], v12 offset:20480\n\t"
    "ds_read_b128 a[148:151], v13 offset:20480\n\t"
    "ds_read_b128 a[152:155], v14 offset:20480\n\t"
    "ds_read_b128 a[156:159], v15 offset:20480\n\t"
    "ds_read_b128 a[96:99], v12 offset:24576\n\t"
    "ds_read_b128 a[100:103], v13 offset:24576\n\t"
    "ds_read_b128 a[104:107], v14 offset:24576\n\t"
    "ds_read_b128 a[108:111], v15 offset:24576\n\t"
    "v_mfma_f32_16x16x32_bf16 v[200:203], a[120:123], a[8:11], v[200:203]\n\t"
    "ds_read_b128 a[128:131], v12 offset:28672\n\t"
    "v_cvt_pk_bf16_f32 v192, v192, v193\n\t"
    "v_cvt_pk_bf16_f32 v194, v194, v195\n\t"
    "v_mfma_f32_16x16x32_bf16 v[200:203], a[124:127], a[12:15], v[200:203]\n\t"
    "ds_read_b128 a[132:135], v13 offset:28672\n\t"
    "ds_read_b128 a[136:139], v14 offset:28672\n\t"
    "ds_read_b128 a[140:143], v15 offset:28672\n\t"
    "v_accvgpr_write_b32 a184, v192\n\t"
    "v_mfma_f32_16x16x32_bf16 v[204:207], a[228:231], a[12:15], 0\n\t"
    "buffer_load_dwordx4 v23, s[36:39], s66 offen offset:3072 nt lds\n\t"
    "v_accvgpr_write_b32 a185, v194\n\t"
    "v_mfma_f32_16x16x32_bf16 v[204:207], a[232:235], a[12:15], v[204:207]\n\t"
    "v_cvt_pk_bf16_f32 v196, v196, v197\n\t"
    "v_cvt_pk_bf16_f32 v198, v198, v199\n\t"
    "v_accvgpr_write_b32 a186, v196\n\t"
    "v_accvgpr_write_b32 a187, v198\n\t"
    "v_mfma_f32_16x16x32_bf16 v[208:211], a[236:239], a[12:15], 0\n\t"
    "v_exp_f32 v177, v177\n\t"
    "v_fma_f32 v188, v188, s52, v0\n\t"
    "v_mfma_f32_16x16x32_bf16 v[208:211], a[240:243], a[12:15], v[208:211]\n\t"
    "s_waitcnt lgkmcnt(11)\n\t"
    "v_exp_f32 v178, v178\n\t"
    "v_exp_f32 v179, v179\n\t"
    "v_mfma_f32_16x16x32_bf16 v[212:215], a[144:147], a[0:3], 0\n\t"
    "s_waitcnt lgkmcnt(10)\n\t"
    "s_add_u32 s78, s55, 36864\n\t"
    "ds_read_b128 a[112:115], v12 offset:32768\n\t"
    "v_cvt_pk_bf16_f32 v76, v176, v177\n\t"
    "v_cvt_pk_bf16_f32 v77, v178, v179\n\t"
    "v_mfma_f32_16x16x32_bf16 v[212:215], a[148:151], a[4:7], v[212:215]\n\t"
    "s_waitcnt lgkmcnt(5)\n\t"
    "s_mov_b32 m0, s75\n\t"
    "ds_read_b128 a[116:119], v13 offset:32768\n\t"
    "v_cvt_pk_bf16_f32 v204, v204, v205\n\t"
    "v_cvt_pk_bf16_f32 v206, v206, v207\n\t"
    "v_accvgpr_write_b32 a188, v204\n\t"
    "v_mfma_f32_16x16x32_bf16 v[212:215], a[152:155], a[8:11], v[212:215]\n\t"
    "ds_read_b128 a[120:123], v14 offset:32768\n\t"
    "v_accvgpr_write_b32 a189, v206\n\t"
    "v_fma_f32 v189, v189, s52, v0\n\t"
    "v_fma_f32 v190, v190, s52, v0\n\t"
    "v_mfma_f32_16x16x32_bf16 v[212:215], a[156:159], a[12:15], v[212:215]\n\t"
    "ds_read_b128 a[124:127], v15 offset:32768\n\t"
    "v_cvt_pk_bf16_f32 v208, v208, v209\n\t"
    "v_cvt_pk_bf16_f32 v210, v210, v211\n\t"
    "v_mfma_f32_16x16x32_bf16 v[216:219], a[96:99], a[0:3], 0\n\t"
    "ds_read_b128 a[144:147], v12 offset:0\n\t"
    "v_accvgpr_write_b32 a190, v208\n\t"
    "v_accvgpr_write_b32 a191, v210\n\t"
    "v_fma_f32 v191, v191, s52, v0\n\t"
    "v_mfma_f32_16x16x32_bf16 v[216:219], a[100:103], a[4:7], v[216:219]\n\t"
    "ds_read_b128 a[148:151], v13 offset:0\n\t"
    "v_exp_f32 v188, v188\n\t"
    "v_mfma_f32_16x16x32_bf16 v[216:219], a[104:107], a[8:11], v[216:219]\n\t"
    "s_waitcnt vmcnt(4)\n\t"
    "ds_read_b128 a[152:155], v14 offset:0\n\t"
    "v_exp_f32 v189, v189\n\t"
    "v_fma_f32 v200, v200, s52, v0\n\t"
    "v_mfma_f32_16x16x32_bf16 v[216:219], a[108:111], a[12:15], v[216:219]\n\t"
    "s_waitcnt lgkmcnt(7)\n\t"
    "ds_read_b128 a[156:159], v15 offset:0\n\t"
    "v_exp_f32 v190, v190\n\t"
    "v_mfma_f32_16x16x32_bf16 v[220:223], a[128:131], a[0:3], 0\n\t"
    "ds_read_b128 a[16:19], v28 offset:4096\n\t"
    "ds_read_b128 a[20:23], v29 offset:4096\n\t"
    "v_exp_f32 v191, v191\n\t"
    "v_mfma_f32_16x16x32_bf16 v[220:223], a[132:135], a[4:7], v[220:223]\n\t"
    "ds_read_b128 a[24:27], v30 offset:4096\n\t"
    "ds_read_b128 a[28:31], v31 offset:4096\n\t"
    "v_cvt_pk_bf16_f32 v78, v188, v189\n\t"
    "v_mfma_f32_16x16x32_bf16 v[220:223], a[136:139], a[8:11], v[220:223]\n\t"
    "buffer_load_dwordx4 v20, s[40:43], s78 offen offset:0 nt lds\n\t"
    "v_cvt_pk_bf16_f32 v79, v190, v191\n\t"
    "v_fma_f32 v201, v201, s52, v0\n\t"
    "v_mfma_f32_16x16x32_bf16 v[220:223], a[140:143], a[12:15], v[220:223]\n\t"
    "s_waitcnt lgkmcnt(8)\n\t"
    "ds_read_b128 a[96:99], v12 offset:4096\n\t"
    "ds_read_b128 a[100:103], v13 offset:4096\n\t"
    "ds_read_b128 a[104:107], v14 offset:4096\n\t"
    "ds_read_b128 a[108:111], v15 offset:4096\n\t"
    "v_mfma_f32_16x16x32_bf16 v[160:163], a[112:115], a[176:179], v[0:3]\n\t"
    "ds_read_b128 a[128:131], v12 offset:8192\n\t"
    "v_fma_f32 v202, v202, s52, v0\n\t"
    "v_fma_f32 v203, v203, s52, v0\n\t"
    "v_mfma_f32_16x16x32_bf16 v[160:163], a[116:119], a[180:183], v[160:163]\n\t"
    "ds_read_b128 a[132:135], v13 offset:8192\n\t"
    "v_exp_f32 v200, v200\n\t"
    "v_fma_f32 v212, v212, s52, v0\n\t"
    "v_mfma_f32_16x16x32_bf16 v[160:163], a[120:123], a[184:187], v[160:163]\n\t"
    "ds_read_b128 a[136:139], v14 offset:8192\n\t"
    "v_exp_f32 v201, v201\n\t"
    "v_fma_f32 v213, v213, s52, v0\n\t"
    "v_mfma_f32_16x16x32_bf16 v[160:163], a[124:127], a[188:191], v[160:163]\n\t"
    "s_waitcnt lgkmcnt(10)\n\t"
    "ds_read_b128 a[140:143], v15 offset:8192\n\t"
    "v_exp_f32 v202, v202\n\t"
    "v_mfma_f32_16x16x32_bf16 v[164:167], a[144:147], a[16:19], 0\n\t"
    "s_waitcnt lgkmcnt(10)\n\t"
    "buffer_load_dwordx4 v21, s[40:43], s78 offen offset:1024 nt lds\n\t"
    "v_exp_f32 v203, v203\n\t"
    "v_mfma_f32_16x16x32_bf16 v[164:167], a[148:151], a[20:23], v[164:167]\n\t"
    "s_waitcnt lgkmcnt(8)\n\t"
    "ds_read_b128 a[112:115], v12 offset:12288\n\t"
    "v_cvt_pk_bf16_f32 v88, v200, v201\n\t"
    "v_cvt_pk_bf16_f32 v89, v202, v203\n\t"
    "v_mfma_f32_16x16x32_bf16 v[164:167], a[152:155], a[24:27], v[164:167]\n\t"
    "ds_read_b128 a[116:119], v13 offset:12288\n\t"
    "v_fma_f32 v214, v214, s52, v0\n\t"
    "v_fma_f32 v215, v215, s52, v0\n\t"
    "v_fma_f32 v216, v216, s52, v0\n\t"
    "v_mfma_f32_16x16x32_bf16 v[164:167], a[156:159], a[28:31], v[164:167]\n\t"
    "s_waitcnt lgkmcnt(6)\n\t"
    "ds_read_b128 a[120:123], v14 offset:12288\n\t"
    "ds_read_b128 a[124:127], v15 offset:12288\n\t"
    "v_fma_f32 v217, v217, s52, v0\n\t"
    "v_mfma_f32_16x16x32_bf16 v[168:171], a[96:99], a[16:19], 0\n\t"
    "ds_read_b128 a[144:147], v12 offset:16384\n\t"
    "v_exp_f32 v212, v212\n\t"
    "v_fma_f32 v218, v218, s52, v0\n\t"
    "v_mfma_f32_16x16x32_bf16 v[168:171], a[100:103], a[20:23], v[168:171]\n\t"
    "ds_read_b128 a[148:151], v13 offset:16384\n\t"
    "v_exp_f32 v213, v213\n\t"
    "v_mfma_f32_16x16x32_bf16 v[168:171], a[104:107], a[24:27], v[168:171]\n\t"
    "ds_read_b128 a[152:155], v14 offset:16384\n\t"
    "v_exp_f32 v214, v214\n\t"
    "v_cvt_pk_bf16_f32 v90, v212, v213\n\t"
    "v_mfma_f32_16x16x32_bf16 v[168:171], a[108:111], a[28:31], v[168:171]\n\t"
    "ds_read_b128 a[156:159], v15 offset:16384\n\t"
    "v_max3_f32 v4, v164, v165, v166\n\t"
    "v_max_f32 v4, v4, v167\n\t"
    "v_mfma_f32_16x16x32_bf16 v[172:175], a[228:231], a[16:19], 0\n\t"
    "buffer_load_dwordx4 v22, s[40:43], s78 offen offset:2048 nt lds\n\t"
    "v_mov_b32 v5, v4\n\t"
    "v_fma_f32 v219, v219, s52, v0\n\t"
    "v_mfma_f32_16x16x32_bf16 v[172:175], a[232:235], a[16:19], v[172:175]\n\t"
    "buffer_load_dwordx4 v23, s[40:43], s78 offen offset:3072 nt lds\n\t"
    "v_fma_f32 v220, v220, s52, v0\n\t"
    "v_mfma_f32_16x16x32_bf16 v[176:179], a[236:239], a[16:19], 0\n\t"
    "v_permlane32_swap_b32 v4, v5\n\t"
    "v_exp_f32 v215, v215\n\t"
    "v_mfma_f32_16x16x32_bf16 v[176:179], a[240:243], a[16:19], v[176:179]\n\t"
    "s_waitcnt lgkmcnt(4)\n\t"
    "v_max_f32 v4, v4, v5\n\t"
    "v_mov_b32 v5, v4\n\t"
    "v_cvt_pk_bf16_f32 v91, v214, v215\n\t"
    "v_fma_f32 v221, v221, s52, v0\n\t"
    "v_mfma_f32_16x16x32_bf16 v[180:183], a[128:131], a[16:19], 0\n\t"
    "s_add_u32 s79, s61, 0\n\t"
    "ds_read_b128 a[96:99], v12 offset:20480\n\t"
    "v_exp_f32 v216, v216\n\t"
    "v_mfma_f32_16x16x32_bf16 v[180:183], a[132:135], a[20:23], v[180:183]\n\t"
    "s_add_u32 m0, s77, 0\n\t"
    "ds_read_b128 a[100:103], v13 offset:20480\n\t"
    "v_permlane16_swap_b32 v4, v5\n\t"
    "v_fma_f32 v222, v222, s52, v0\n\t"
    "v_mfma_f32_16x16x32_bf16 v[180:183], a[136:139], a[24:27], v[180:183]\n\t"
    "ds_read_b128 a[104:107], v14 offset:20480\n\t"
    "v_max_f32 v4, v4, v5\n\t"
    "v_mul_f32 v4, s53, v4\n\t"
    "v_mfma_f32_16x16x32_bf16 v[180:183], a[140:143], a[28:31], v[180:183]\n\t"
    "ds_read_b128 a[108:111], v15 offset:20480\n\t"
    "v_mov_b32 v5, v4\n\t"
    "v_mov_b32 v6, v4\n\t"
    "v_mov_b32 v7, v4\n\t"
    "v_mfma_f32_16x16x32_bf16 v[184:187], a[228:231], a[20:23], 0\n\t"
    "buffer_load_dwordx4 v24, s[44:47], s79 offen offset:0 nt lds\n\t"
    "v_fma_f32 v223, v223, s52, v0\n\t"
    "v_mfma_f32_16x16x32_bf16 v[184:187], a[232:235], a[20:23], v[184:187]\n\t"
    "v_exp_f32 v217, v217\n\t"
    "v_exp_f32 v218, v218\n\t"
    "v_mfma_f32_16x16x32_bf16 v[188:191], a[236:239], a[20:23], 0\n\t"
    "v_exp_f32 v219, v219\n\t"
    "v_cvt_pk_bf16_f32 v100, v216, v217\n\t"
    "v_mfma_f32_16x16x32_bf16 v[188:191], a[240:243], a[20:23], v[188:191]\n\t"
    "v_cvt_pk_bf16_f32 v101, v218, v219\n\t"
    "v_exp_f32 v220, v220\n\t"
    "v_fma_f32 v164, v164, s52, v4\n\t"
    "v_mfma_f32_16x16x32_bf16 v[192:195], a[112:115], a[16:19], 0\n\t"
    "ds_read_b128 a[128:131], v12 offset:24576\n\t"
    "v_exp_f32 v221, v221\n\t"
    "v_mfma_f32_16x16x32_bf16 v[192:195], a[116:119], a[20:23], v[192:195]\n\t"
    "ds_read_b128 a[132:135], v13 offset:24576\n\t"
    "v_exp_f32 v222, v222\n\t"
    "v_cvt_pk_bf16_f32 v102, v220, v221\n\t"
    "v_mfma_f32_16x16x32_bf16 v[192:195], a[120:123], a[24:27], v[192:195]\n\t"
    "ds_read_b128 a[136:139], v14 offset:24576\n\t"
    "v_exp_f32 v223, v223\n\t"
    "v_mfma_f32_16x16x32_bf16 v[192:195], a[124:127], a[28:31], v[192:195]\n\t"
    "ds_read_b128 a[140:143], v15 offset:24576\n\t"
    "v_cvt_pk_bf16_f32 v103, v222, v223\n\t"
    "v_exp_f32 v160, v160\n\t"
    "v_mfma_f32_16x16x32_bf16 v[196:199], a[228:231], a[24:27], 0\n\t"
    "buffer_load_dwordx4 v25, s[44:47], s79 offen offset:1024 nt lds\n\t"
    "v_exp_f32 v161, v161\n\t"
    "v_mfma_f32_16x16x32_bf16 v[196:199], a[232:235], a[24:27], v[196:199]\n\t"
    "v_exp_f32 v162, v162\n\t"
    "v_exp_f32 v163, v163\n\t"
    "v_mfma_f32_16x16x32_bf16 v[200:203], a[236:239], a[24:27], 0\n\t"
    "v_cvt_pk_bf16_f32 v112, v160, v161\n\t"
    "v_cvt_pk_bf16_f32 v113, v162, v163\n\t"
    "v_fma_f32 v165, v165, s52, v4\n\t"
    "v_fma_f32 v166, v166, s52, v4\n\t"
    "v_mfma_f32_16x16x32_bf16 v[200:203], a[240:243], a[24:27], v[200:203]\n\t"
    "s_waitcnt lgkmcnt(4)\n\t"
    "v_fma_f32 v167, v167, s52, v4\n\t"
    "v_cvt_pk_bf16_f32 v172, v172, v173\n\t"
    "v_cvt_pk_bf16_f32 v174, v174, v175\n\t"
    "v_accvgpr_write_b32 a192, v172\n\t"
    "v_mfma_f32_16x16x32_bf16 v[204:207], a[144:147], a[16:19], 0\n\t"
    "ds_read_b128 a[112:115], v12 offset:28672\n\t"
    "v_accvgpr_write_b32 a193, v174\n\t"
    "v_cvt_pk_bf16_f32 v176, v176, v177\n\t"
    "v_cvt_pk_bf16_f32 v178, v178, v179\n\t"
    "v_accvgpr_write_b32 a194, v176\n\t"
    "v_mfma_f32_16x16x32_bf16 v[204:207], a[148:151], a[20:23], v[204:207]\n\t"
    "ds_read_b128 a[116:119], v13 offset:28672\n\t"
    "v_accvgpr_write_b32 a195, v178\n\t"
    "v_exp_f32 v164, v164\n\t"
    "v_mfma_f32_16x16x32_bf16 v[204:207], a[152:155], a[24:27], v[204:207]\n\t"
    "ds_read_b128 a[120:123], v14 offset:28672\n\t"
    "v_exp_f32 v165, v165\n\t"
    "v_cvt_pk_bf16_f32 v184, v184, v185\n\t"
    "v_mfma_f32_16x16x32_bf16 v[204:207], a[156:159], a[28:31], v[204:207]\n\t"
    "ds_read_b128 a[124:127], v15 offset:28672\n\t"
    "v_exp_f32 v166, v166\n\t"
    "v_cvt_pk_bf16_f32 v186, v186, v187\n\t"
    "v_mfma_f32_16x16x32_bf16 v[208:211], a[228:231], a[28:31], 0\n\t"
    "buffer_load_dwordx4 v26, s[44:47], s79 offen offset:2048 nt lds\n\t"
    "v_exp_f32 v167, v167\n\t"
    "v_mfma_f32_16x16x32_bf16 v[208:211], a[232:235], a[28:31], v[208:211]\n\t"
    "v_accvgpr_write_b32 a196, v184\n\t"
    "v_accvgpr_write_b32 a197, v186\n\t"
    "v_cvt_pk_bf16_f32 v188, v188, v189\n\t"
    "v_cvt_pk_bf16_f32 v190, v190, v191\n\t"
    "v_mfma_f32_16x16x32_bf16 v[212:215], a[236:239], a[28:31], 0\n\t"
    "v_accvgpr_write_b32 a198, v188\n\t"
    "v_accvgpr_write_b32 a199, v190\n\t"
    "v_cvt_pk_bf16_f32 v68, v164, v165\n\t"
    "v_cvt_pk_bf16_f32 v69, v166, v167\n\t"
    "v_mfma_f32_16x16x32_bf16 v[212:215], a[240:243], a[28:31], v[212:215]\n\t"
    "buffer_load_dwordx4 v27, s[44:47], s79 offen offset:3072 nt lds\n\t"
    "v_cvt_pk_bf16_f32 v196, v196, v197\n\t"
    "v_cvt_pk_bf16_f32 v198, v198, v199\n\t"
    "v_mfma_f32_16x16x32_bf16 v[216:219], a[96:99], a[16:19], 0\n\t"
    "ds_read_b128 a[144:147], v12 offset:32768\n\t"
    "v_accvgpr_write_b32 a200, v196\n\t"
    "v_accvgpr_write_b32 a201, v198\n\t"
    "v_cvt_pk_bf16_f32 v200, v200, v201\n\t"
    "v_mfma_f32_16x16x32_bf16 v[216:219], a[100:103], a[20:23], v[216:219]\n\t"
    "ds_read_b128 a[148:151], v13 offset:32768\n\t"
    "v_cvt_pk_bf16_f32 v202, v202, v203\n\t"
    "v_accvgpr_write_b32 a202, v200\n\t"
    "v_accvgpr_write_b32 a203, v202\n\t"
    "v_mfma_f32_16x16x32_bf16 v[216:219], a[104:107], a[24:27], v[216:219]\n\t"
    "ds_read_b128 a[152:155], v14 offset:32768\n\t"
    "v_cvt_pk_bf16_f32 v208, v208, v209\n\t"
    "v_cvt_pk_bf16_f32 v210, v210, v211\n\t"
    "v_accvgpr_write_b32 a204, v208\n\t"
    "v_mfma_f32_16x16x32_bf16 v[216:219], a[108:111], a[28:31], v[216:219]\n\t"
    "s_waitcnt lgkmcnt(3)\n\t"
    "ds_read_b128 a[156:159], v15 offset:32768\n\t"
    "v_accvgpr_write_b32 a205, v210\n\t"
    "v_cvt_pk_bf16_f32 v212, v212, v213\n\t"
    "v_cvt_pk_bf16_f32 v214, v214, v215\n\t"
    "v_mfma_f32_16x16x32_bf16 v[220:223], a[128:131], a[16:19], 0\n\t"
    "s_add_u32 s79, s61, 128\n\t"
    "ds_read_b128 a[96:99], v12 offset:0\n\t"
    "v_accvgpr_write_b32 a206, v212\n\t"
    "v_accvgpr_write_b32 a207, v214\n\t"
    "v_fma_f32 v168, v168, s52, v4\n\t"
    "v_fma_f32 v169, v169, s52, v4\n\t"
    "v_mfma_f32_16x16x32_bf16 v[220:223], a[132:135], a[20:23], v[220:223]\n\t"
    "s_add_u32 m0, s77, 16384\n\t"
    "ds_read_b128 a[100:103], v13 offset:0\n\t"
    "v_fma_f32 v170, v170, s52, v4\n\t"
    "v_fma_f32 v171, v171, s52, v4\n\t"
    "v_fma_f32 v180, v180, s52, v4\n\t"
    "v_mfma_f32_16x16x32_bf16 v[220:223], a[136:139], a[24:27], v[220:223]\n\t"
    "s_waitcnt vmcnt(8)\n\t"
    "ds_read_b128 a[104:107], v14 offset:0\n\t"
    "v_exp_f32 v168, v168\n\t"
    "v_fma_f32 v181, v181, s52, v4\n\t"
    "v_mfma_f32_16x16x32_bf16 v[220:223], a[140:143], a[28:31], v[220:223]\n\t"
    "ds_read_b128 a[108:111], v15 offset:0\n\t"
    "v_exp_f32 v169, v169\n\t"
    "v_fma_f32 v182, v182, s52, v4\n\t"
    "v_mfma_f32_16x16x32_bf16 v[160:163], a[112:115], a[16:19], 0\n\t"
    "ds_read_b128 a[32:35], v236 offset:49152\n\t"
    "ds_read_b128 a[36:39], v237 offset:49152\n\t"
    "v_exp_f32 v170, v170\n\t"
    "v_mfma_f32_16x16x32_bf16 v[160:163], a[116:119], a[20:23], v[160:163]\n\t"
    "ds_read_b128 a[40:43], v238 offset:49152\n\t"
    "ds_read_b128 a[44:47], v239 offset:49152\n\t"
    "v_exp_f32 v171, v171\n\t"
    "v_mfma_f32_16x16x32_bf16 v[160:163], a[120:123], a[24:27], v[160:163]\n\t"
    "ds_read_b128 a[128:131], v12 offset:4096\n\t"
    "v_cvt_pk_bf16_f32 v70, v168, v169\n\t"
    "v_cvt_pk_bf16_f32 v71, v170, v171\n\t"
    "v_fma_f32 v183, v183, s52, v4\n\t"
    "v_mfma_f32_16x16x32_bf16 v[160:163], a[124:127], a[28:31], v[160:163]\n\t"
    "s_waitcnt lgkmcnt(9)\n\t"
    "ds_read_b128 a[132:135], v13 offset:4096\n\t"
    "ds_read_b128 a[136:139], v14 offset:4096\n\t"
    "ds_read_b128 a[140:143], v15 offset:4096\n\t"
    "v_fma_f32 v192, v192, s52, v4\n\t"
    "v_mfma_f32_16x16x32_bf16 v[164:167], a[144:147], a[192:195], v[4:7]\n\t"
    "buffer_load_dwordx4 v24, s[44:47], s79 offen offset:0 nt lds\n\t"
    "v_exp_f32 v180, v180\n\t"
    "v_mfma_f32_16x16x32_bf16 v[164:167], a[148:151], a[196:199], v[164:167]\n\t"
    "ds_read_b128 a[112:115], v12 offset:8192\n\t"
    "v_exp_f32 v181, v181\n\t"
    "v_fma_f32 v193, v193, s52, v4\n\t"
    "v_mfma_f32_16x16x32_bf16 v[164:167], a[152:155], a[200:203], v[164:167]\n\t"
    "ds_read_b128 a[116:119], v13 offset:8192\n\t"
    "v_exp_f32 v182, v182\n\t"
    "v_cvt_pk_bf16_f32 v80, v180, v181\n\t"
    "v_mfma_f32_16x16x32_bf16 v[164:167], a[156:159], a[204:207], v[164:167]\n\t"
    "s_waitcnt lgkmcnt(9)\n\t"
    "ds_read_b128 a[120:123], v14 offset:8192\n\t"
    "ds_read_b128 a[124:127], v15 offset:8192\n\t"
    "v_exp_f32 v183, v183\n\t"
    "v_fma_f32 v194, v194, s52, v4\n\t"
    "v_mfma_f32_16x16x32_bf16 v[168:171], a[96:99], a[32:35], 0\n\t"
    "s_waitcnt lgkmcnt(10)\n\t"
    "ds_read_b128 a[144:147], v12 offset:12288\n\t"
    "v_cvt_pk_bf16_f32 v81, v182, v183\n\t"
    "v_fma_f32 v195, v195, s52, v4\n\t"
    "v_fma_f32 v204, v204, s52, v4\n\t"
    "v_mfma_f32_16x16x32_bf16 v[168:171], a[100:103], a[36:39], v[168:171]\n\t"
    "s_waitcnt lgkmcnt(9)\n\t"
    "ds_read_b128 a[148:151], v13 offset:12288\n\t"
    "v_exp_f32 v192, v192\n\t"
    "v_fma_f32 v205, v205, s52, v4\n\t"
    "v_mfma_f32_16x16x32_bf16 v[168:171], a[104:107], a[40:43], v[168:171]\n\t"
    "ds_read_b128 a[152:155], v14 offset:12288\n\t"
    "v_exp_f32 v193, v193\n\t"
    "v_fma_f32 v206, v206, s52, v4\n\t"
    "v_mfma_f32_16x16x32_bf16 v[168:171], a[108:111], a[44:47], v[168:171]\n\t"
    "s_waitcnt lgkmcnt(7)\n\t"
    "ds_read_b128 a[156:159], v15 offset:12288\n\t"
    "v_exp_f32 v194, v194\n\t"
    "v_cvt_pk_bf16_f32 v82, v192, v193\n\t"
    "v_mfma_f32_16x16x32_bf16 v[172:175], a[128:131], a[32:35], 0\n\t"
    "buffer_load_dwordx4 v25, s[44:47], s79 offen offset:1024 nt lds\n\t"
    "v_exp_f32 v195, v195\n\t"
    "v_mfma_f32_16x16x32_bf16 v[172:175], a[132:135], a[36:39], v[172:175]\n\t"
    "ds_read_b128 a[96:99], v12 offset:16384\n\t"
    "v_cvt_pk_bf16_f32 v83, v194, v195\n\t"
    "v_fma_f32 v207, v207, s52, v4\n\t"
    "v_fma_f32 v216, v216, s52, v4\n\t"
    "v_mfma_f32_16x16x32_bf16 v[172:175], a[136:139], a[40:43], v[172:175]\n\t"
    "ds_read_b128 a[100:103], v13 offset:16384\n\t"
    "v_exp_f32 v204, v204\n\t"
    "v_fma_f32 v217, v217, s52, v4\n\t"
    "v_mfma_f32_16x16x32_bf16 v[172:175], a[140:143], a[44:47], v[172:175]\n\t"
    "ds_read_b128 a[104:107], v14 offset:16384\n\t"
    "ds_read_b128 a[108:111], v15 offset:16384\n\t"
    "v_max3_f32 v8, v168, v169, v170\n\t"
    "v_max_f32 v8, v8, v171\n\t"
    "v_mfma_f32_16x16x32_bf16 v[176:179], a[228:231], a[32:35], 0\n\t"
    "buffer_load_dwordx4 v26, s[44:47], s79 offen offset:2048 nt lds\n\t"
    "v_mov_b32 v9, v8\n\t"
    "v_fma_f32 v218, v218, s52, v4\n\t"
    "v_mfma_f32_16x16x32_bf16 v[176:179], a[232:235], a[32:35], v[176:179]\n\t"
    "v_exp_f32 v205, v205\n\t"
    "v_exp_f32 v206, v206\n\t"
    "v_mfma_f32_16x16x32_bf16 v[180:183], a[236:239], a[32:35], 0\n\t"
    "v_permlane32_swap_b32 v8, v9\n\t"
    "v_exp_f32 v207, v207\n\t"
    "v_cvt_pk_bf16_f32 v92, v204, v205\n\t"
    "v_mfma_f32_16x16x32_bf16 v[180:183], a[240:243], a[32:35], v[180:183]\n\t"
    "s_waitcnt lgkmcnt(4)\n\t"
    "buffer_load_dwordx4 v27, s[44:47], s79 offen offset:3072 nt lds\n\t"
    "v_max_f32 v8, v8, v9\n\t"
    "v_mov_b32 v9, v8\n\t"
    "v_mfma_f32_16x16x32_bf16 v[184:187], a[112:115], a[32:35], 0\n\t"
    "ds_read_b128 a[128:131], v12 offset:20480\n\t"
    "v_cvt_pk_bf16_f32 v93, v206, v207\n\t"
    "v_fma_f32 v219, v219, s52, v4\n\t"
    "v_fma_f32 v160, v160, s52, v4\n\t"
    "v_mfma_f32_16x16x32_bf16 v[184:187], a[116:119], a[36:39], v[184:187]\n\t"
    "ds_read_b128 a[132:135], v13 offset:20480\n\t"
    "v_permlane16_swap_b32 v8, v9\n\t"
    "v_cvt_pk_bf16_f32 v176, v176, v177\n\t"
    "v_mfma_f32_16x16x32_bf16 v[184:187], a[120:123], a[40:43], v[184:187]\n\t"
    "ds_read_b128 a[136:139], v14 offset:20480\n\t"
    "v_max_f32 v8, v8, v9\n\t"
    "v_mul_f32 v8, s53, v8\n\t"
    "v_mov_b32 v9, v8\n\t"
    "v_mfma_f32_16x16x32_bf16 v[184:187], a[124:127], a[44:47], v[184:187]\n\t"
    "ds_read_b128 a[140:143], v15 offset:20480\n\t"
    "v_mov_b32 v10, v8\n\t"
    "v_mov_b32 v11, v8\n\t"
    "v_cvt_pk_bf16_f32 v178, v178, v179\n\t"
    "v_mfma_f32_16x16x32_bf16 v[188:191], a[228:231], a[36:39], 0\n\t"
    "s_mov_b32 m0, s85\n\t"
    "v_exp_f32 v216, v216\n\t"
    "v_exp_f32 v217, v217\n\t"
    "v_mfma_f32_16x16x32_bf16 v[188:191], a[232:235], a[36:39], v[188:191]\n\t"
    "v_exp_f32 v218, v218\n\t"
    "v_exp_f32 v219, v219\n\t"
    "v_mfma_f32_16x16x32_bf16 v[192:195], a[236:239], a[36:39], 0\n\t"
    "buffer_load_dwordx4 v20, s[36:39], s67 offen offset:0 nt lds\n\t"
    "v_cvt_pk_bf16_f32 v94, v216, v217\n\t"
    "v_cvt_pk_bf16_f32 v95, v218, v219\n\t"
    "v_mfma_f32_16x16x32_bf16 v[192:195], a[240:243], a[36:39], v[192:195]\n\t"
    "v_accvgpr_write_b32 a208, v176\n\t"
    "v_accvgpr_write_b32 a209, v178\n\t"
    "v_cvt_pk_bf16_f32 v180, v180, v181\n\t"
    "v_cvt_pk_bf16_f32 v182, v182, v183\n\t"
    "v_mfma_f32_16x16x32_bf16 v[196:199], a[144:147], a[32:35], 0\n\t"
    "ds_read_b128 a[112:115], v12 offset:24576\n\t"
    "v_accvgpr_write_b32 a210, v180\n\t"
    "v_accvgpr_write_b32 a211, v182\n\t"
    "v_fma_f32 v161, v161, s52, v4\n\t"
    "v_mfma_f32_16x16x32_bf16 v[196:199], a[148:151], a[36:39], v[196:199]\n\t"
    "ds_read_b128 a[116:119], v13 offset:24576\n\t"
    "v_cvt_pk_bf16_f32 v188, v188, v189\n\t"
    "v_cvt_pk_bf16_f32 v190, v190, v191\n\t"
    "v_accvgpr_write_b32 a212, v188\n\t"
    "v_accvgpr_write_b32 a213, v190\n\t"
    "v_mfma_f32_16x16x32_bf16 v[196:199], a[152:155], a[40:43], v[196:199]\n\t"
    "ds_read_b128 a[120:123], v14 offset:24576\n\t"
    "v_fma_f32 v162, v162, s52, v4\n\t"
    "v_fma_f32 v163, v163, s52, v4\n\t"
    "v_fma_f32 v220, v220, s52, v4\n\t"
    "v_mfma_f32_16x16x32_bf16 v[196:199], a[156:159], a[44:47], v[196:199]\n\t"
    "ds_read_b128 a[124:127], v15 offset:24576\n\t"
    "v_cvt_pk_bf16_f32 v192, v192, v193\n\t"
    "v_cvt_pk_bf16_f32 v194, v194, v195\n\t"
    "v_accvgpr_write_b32 a214, v192\n\t"
    "v_mfma_f32_16x16x32_bf16 v[200:203], a[228:231], a[40:43], 0\n\t"
    "v_accvgpr_write_b32 a215, v194\n\t"
    "v_exp_f32 v160, v160\n\t"
    "v_fma_f32 v221, v221, s52, v4\n\t"
    "v_mfma_f32_16x16x32_bf16 v[200:203], a[232:235], a[40:43], v[200:203]\n\t"
    "buffer_load_dwordx4 v21, s[36:39], s67 offen offset:1024 nt lds\n\t"
    "v_exp_f32 v161, v161\n\t"
    "v_mfma_f32_16x16x32_bf16 v[204:207], a[236:239], a[40:43], 0\n\t"
    "v_exp_f32 v162, v162\n\t"
    "v_exp_f32 v163, v163\n\t"
    "v_mfma_f32_16x16x32_bf16 v[204:207], a[240:243], a[40:43], v[204:207]\n\t"
    "s_waitcnt lgkmcnt(4)\n\t"
    "v_cvt_pk_bf16_f32 v106, v160, v161\n\t"
    "v_cvt_pk_bf16_f32 v107, v162, v163\n\t"
    "v_exp_f32 v164, v164\n\t"
    "v_mfma_f32_16x16x32_bf16 v[208:211], a[96:99], a[32:35], 0\n\t"
    "ds_read_b128 a[144:147], v12 offset:28672\n\t"
    "v_exp_f32 v165, v165\n\t"
    "v_fma_f32 v222, v222, s52, v4\n\t"
    "v_mfma_f32_16x16x32_bf16 v[208:211], a[100:103], a[36:39], v[208:211]\n\t"
    "ds_read_b128 a[148:151], v13 offset:28672\n\t"
    "v_cvt_pk_bf16_f32 v200, v200, v201\n\t"
    "v_cvt_pk_bf16_f32 v202, v202, v203\n\t"
    "v_accvgpr_write_b32 a216, v200\n\t"
    "v_mfma_f32_16x16x32_bf16 v[208:211], a[104:107], a[40:43], v[208:211]\n\t"
    "ds_read_b128 a[152:155], v14 offset:28672\n\t"
    "v_accvgpr_write_b32 a217, v202\n\t"
    "v_exp_f32 v166, v166\n\t"
    "v_mfma_f32_16x16x32_bf16 v[208:211], a[108:111], a[44:47], v[208:211]\n\t"
    "ds_read_b128 a[156:159], v15 offset:28672\n\t"
    "v_cvt_pk_bf16_f32 v204, v204, v205\n\t"
    "v_cvt_pk_bf16_f32 v206, v206, v207\n\t"
    "v_accvgpr_write_b32 a218, v204\n\t"
    "v_mfma_f32_16x16x32_bf16 v[212:215], a[228:231], a[44:47], 0\n\t"
    "buffer_load_dwordx4 v22, s[36:39], s67 offen offset:2048 nt lds\n\t"
    "v_accvgpr_write_b32 a219, v206\n\t"
    "v_fma_f32 v223, v223, s52, v4\n\t"
    "v_mfma_f32_16x16x32_bf16 v[212:215], a[232:235], a[44:47], v[212:215]\n\t"
    "v_exp_f32 v167, v167\n\t"
    "v_cvt_pk_bf16_f32 v116, v164, v165\n\t"
    "v_exp_f32 v220, v220\n\t"
    "v_mfma_f32_16x16x32_bf16 v[216:219], a[236:239], a[44:47], 0\n\t"
    "v_cvt_pk_bf16_f32 v117, v166, v167\n\t"
    "v_exp_f32 v221, v221\n\t"
    "v_fma_f32 v168, v168, s52, v8\n\t"
    "v_mfma_f32_16x16x32_bf16 v[216:219], a[240:243], a[44:47], v[216:219]\n\t"
    "v_exp_f32 v222, v222\n\t"
    "v_exp_f32 v223, v223\n\t"
    "v_mfma_f32_16x16x32_bf16 v[40:43], a[128:131], a[32:35], 0\n\t"
    "ds_read_b128 a[96:99], v12 offset:32768\n\t"
    "v_cvt_pk_bf16_f32 v104, v220, v221\n\t"
    "v_cvt_pk_bf16_f32 v105, v222, v223\n\t"
    "v_fma_f32 v169, v169, s52, v8\n\t"
    "v_mfma_f32_16x16x32_bf16 v[40:43], a[132:135], a[36:39], v[40:43]\n\t"
    "ds_read_b128 a[100:103], v13 offset:32768\n\t"
    "v_cvt_pk_bf16_f32 v212, v212, v213\n\t"
    "v_cvt_pk_bf16_f32 v214, v214, v215\n\t"
    "v_accvgpr_write_b32 a220, v212\n\t"
    "v_mfma_f32_16x16x32_bf16 v[40:43], a[136:139], a[40:43], v[40:43]\n\t"
    "ds_read_b128 a[104:107], v14 offset:32768\n\t"
    "v_accvgpr_write_b32 a221, v214\n\t"
    "v_fma_f32 v170, v170, s52, v8\n\t"
    "v_fma_f32 v171, v171, s52, v8\n\t"
    "v_mfma_f32_16x16x32_bf16 v[40:43], a[140:143], a[44:47], v[40:43]\n\t"
    "s_waitcnt lgkmcnt(3)\n\t"
    "ds_read_b128 a[108:111], v15 offset:32768\n\t"
    "v_cvt_pk_bf16_f32 v216, v216, v217\n\t"
    "v_cvt_pk_bf16_f32 v218, v218, v219\n\t"
    "v_accvgpr_write_b32 a222, v216\n\t"
    "v_mfma_f32_16x16x32_bf16 v[44:47], a[112:115], a[32:35], 0\n\t"
    "buffer_load_dwordx4 v23, s[36:39], s67 offen offset:3072 nt lds\n\t"
    "v_accvgpr_write_b32 a223, v218\n\t"
    "v_fma_f32 v172, v172, s52, v8\n\t"
    "v_mfma_f32_16x16x32_bf16 v[44:47], a[116:119], a[36:39], v[44:47]\n\t"
    "s_waitcnt vmcnt(8) lgkmcnt(0)\n\t"
    "v_fma_f32 v173, v173, s52, v8\n\t"
    "v_fma_f32 v174, v174, s52, v8\n\t"
    "v_fma_f32 v175, v175, s52, v8\n\t"
    "v_fma_f32 v184, v184, s52, v8\n\t"
    "v_mfma_f32_16x16x32_bf16 v[44:47], a[120:123], a[40:43], v[44:47]\n\t"
    "s_barrier\n\t"
    "ds_read_b64_tr_b16 a[128:129], v16 offset:0\n\t"
    "v_exp_f32 v168, v168\n\t"
    "v_fma_f32 v185, v185, s52, v8\n\t"
    "v_mfma_f32_16x16x32_bf16 v[44:47], a[124:127], a[44:47], v[44:47]\n\t"
    "ds_read_b64_tr_b16 a[130:131], v16 offset:2048\n\t"
    "v_fma_f32 v40, v40, s52, v8\n\t"
    "v_fma_f32 v41, v41, s52, v8\n\t"
    "v_fma_f32 v42, v42, s52, v8\n\t"
    "v_mfma_f32_16x16x32_bf16 v[48:51], a[144:147], a[32:35], 0\n\t"
    "s_add_u32 s78, s55, 53248\n\t"
    "ds_read_b64_tr_b16 a[132:133], v16 offset:4096\n\t"
    "v_fma_f32 v43, v43, s52, v8\n\t"
    "v_exp_f32 v40, v40\n\t"
    "v_fma_f32 v186, v186, s52, v8\n\t"
    "v_mfma_f32_16x16x32_bf16 v[48:51], a[148:151], a[36:39], v[48:51]\n\t"
    "s_mov_b32 m0, s76\n\t"
    "ds_read_b64_tr_b16 a[134:135], v16 offset:6144\n\t"
    "v_exp_f32 v41, v41\n\t"
    "v_fma_f32 v187, v187, s52, v8\n\t"
    "v_mfma_f32_16x16x32_bf16 v[48:51], a[152:155], a[40:43], v[48:51]\n\t"
    "ds_read_b64_tr_b16 a[136:137], v16 offset:8192\n\t"
    "ds_read_b64_tr_b16 a[138:139], v16 offset:10240\n\t"
    "v_exp_f32 v42, v42\n\t"
    "v_mfma_f32_16x16x32_bf16 v[48:51], a[156:159], a[44:47], v[48:51]\n\t"
    "ds_read_b64_tr_b16 a[140:141], v16 offset:12288\n\t"
    "ds_read_b64_tr_b16 a[142:143], v16 offset:14336\n\t"
    "ds_read_b128 a[112:115], v12 offset:36864\n\t"
    "ds_read_b128 a[116:119], v13 offset:36864\n\t"
    "ds_read_b128 a[120:123], v14 offset:36864\n\t"
    "ds_read_b128 a[124:127], v15 offset:36864\n\t"
    "v_mfma_f32_16x16x32_bf16 v[52:55], a[96:99], a[208:211], v[8:11]\n\t"
    "buffer_load_dwordx4 v20, s[40:43], s78 offen offset:0 nt lds\n\t"
    "v_exp_f32 v43, v43\n\t"
    "v_mfma_f32_16x16x32_bf16 v[52:55], a[100:103], a[212:215], v[52:55]\n\t"
    "v_cvt_pk_bf16_f32 v98, v40, v41\n\t"
    "v_cvt_pk_bf16_f32 v99, v42, v43\n\t"
    "v_exp_f32 v169, v169\n\t"
    "v_mfma_f32_16x16x32_bf16 v[52:55], a[104:107], a[216:219], v[52:55]\n\t"
    "v_exp_f32 v170, v170\n\t"
    "v_exp_f32 v171, v171\n\t"
    "v_mfma_f32_16x16x32_bf16 v[52:55], a[108:111], a[220:223], v[52:55]\n\t"
    "s_waitcnt lgkmcnt(3)\n\t"
    "v_exp_f32 v172, v172\n\t"
    "v_exp_f32 v173, v173\n\t"
    "v_mfma_f32_16x16x32_bf16 v[56:59], a[112:115], a[176:179], v[0:3]\n\t"
    "s_waitcnt lgkmcnt(2)\n\t"
    "ds_read_b128 a[96:99], v12 offset:40960\n\t"
    "v_exp_f32 v174, v174\n\t"
    "v_fma_f32 v196, v196, s52, v8\n\t"
    "v_mfma_f32_16x16x32_bf16 v[56:59], a[116:119], a[180:183], v[56:59]\n\t"
    "s_waitcnt lgkmcnt(2)\n\t"
    "buffer_load_dwordx4 v21, s[40:43], s78 offen offset:1024 nt lds\n\t"
    "v_exp_f32 v175, v175\n\t"
    "v_mfma_f32_16x16x32_bf16 v[56:59], a[120:123], a[184:187], v[56:59]\n\t"
    "s_waitcnt lgkmcnt(1)\n\t"
    "ds_read_b128 a[100:103], v13 offset:40960\n\t"
    "v_fma_f32 v197, v197, s52, v8\n\t"
    "v_fma_f32 v198, v198, s52, v8\n\t"
    "v_fma_f32 v199, v199, s52, v8\n\t"
    "v_mfma_f32_16x16x32_bf16 v[56:59], a[124:127], a[188:191], v[56:59]\n\t"
    "v_cvt_pk_bf16_f32 v72, v168, v169\n\t"
    "v_cvt_pk_bf16_f32 v73, v170, v171\n\t"
    "v_cvt_pk_bf16_f32 v74, v172, v173\n\t"
    "v_cvt_pk_bf16_f32 v75, v174, v175\n\t"
    "v_mfma_f32_16x16x32_bf16 v[160:163], a[128:131], v[64:67], 0\n\t"
    "ds_read_b128 a[104:107], v14 offset:40960\n\t"
    "v_exp_f32 v184, v184\n\t"
    "v_fma_f32 v208, v208, s52, v8\n\t"
    "v_mfma_f32_16x16x32_bf16 v[160:163], a[132:135], v[76:79], v[160:163]\n\t"
    "v_exp_f32 v185, v185\n\t"
    "v_exp_f32 v186, v186\n\t"
    "v_mfma_f32_16x16x32_bf16 v[160:163], a[136:139], v[88:91], v[160:163]\n\t"
    "ds_read_b128 a[108:111], v15 offset:40960\n\t"
    "v_exp_f32 v187, v187\n\t"
    "v_fma_f32 v209, v209, s52, v8\n\t"
    "v_mfma_f32_16x16x32_bf16 v[160:163], a[140:143], v[100:103], v[160:163]\n\t"
    "v_exp_f32 v196, v196\n\t"
    "v_exp_f32 v197, v197\n\t"
    "v_mfma_f32_16x16x32_bf16 v[60:63], a[112:115], a[192:195], v[4:7]\n\t"
    "ds_read_b64_tr_b16 a[144:145], v17 offset:0\n\t"
    "v_exp_f32 v198, v198\n\t"
    "v_fma_f32 v210, v210, s52, v8\n\t"
    "v_mfma_f32_16x16x32_bf16 v[60:63], a[116:119], a[196:199], v[60:63]\n\t"
    "buffer_load_dwordx4 v22, s[40:43], s78 offen offset:2048 nt lds\n\t"
    "v_exp_f32 v199, v199\n\t"
    "v_mfma_f32_16x16x32_bf16 v[60:63], a[120:123], a[200:203], v[60:63]\n\t"
    "ds_read_b64_tr_b16 a[146:147], v17 offset:2048\n\t"
    "v_fma_f32 v211, v211, s52, v8\n\t"
    "v_cvt_pk_bf16_f32 v84, v184, v185\n\t"
    "v_cvt_pk_bf16_f32 v85, v186, v187\n\t"
    "v_mfma_f32_16x16x32_bf16 v[60:63], a[124:127], a[204:207], v[60:63]\n\t"
    "v_cvt_pk_bf16_f32 v86, v196, v197\n\t"
    "v_cvt_pk_bf16_f32 v87, v198, v199\n\t"
    "v_exp_f32 v208, v208\n\t"
    "v_mfma_f32_16x16x32_bf16 v[164:167], a[128:131], v[68:71], 0\n\t"
    "ds_read_b64_tr_b16 a[148:149], v17 offset:4096\n\t"
    "v_exp_f32 v209, v209\n\t"
    "v_fma_f32 v44, v44, s52, v8\n\t"
    "v_mfma_f32_16x16x32_bf16 v[164:167], a[132:135], v[80:83], v[164:167]\n\t"
    "v_exp_f32 v210, v210\n\t"
    "v_exp_f32 v211, v211\n\t"
    "v_mfma_f32_16x16x32_bf16 v[164:167], a[136:139], v[92:95], v[164:167]\n\t"
    "ds_read_b64_tr_b16 a[150:151], v17 offset:6144\n\t"
    "v_fma_f32 v45, v45, s52, v8\n\t"
    "v_fma_f32 v46, v46, s52, v8\n\t"
    "v_fma_f32 v47, v47, s52, v8\n\t"
    "v_mfma_f32_16x16x32_bf16 v[164:167], a[140:143], v[104:107], v[164:167]\n\t"
    "v_fma_f32 v48, v48, s52, v8\n\t"
    "v_fma_f32 v49, v49, s52, v8\n\t"
    "v_fma_f32 v50, v50, s52, v8\n\t"
    "v_fma_f32 v51, v51, s52, v8\n\t"
    "v_mfma_f32_16x16x32_bf16 v[40:43], a[112:115], a[208:211], v[8:11]\n\t"
    "ds_read_b64_tr_b16 a[152:153], v17 offset:8192\n\t"
    "v_cvt_pk_bf16_f32 v96, v208, v209\n\t"
    "v_cvt_pk_bf16_f32 v97, v210, v211\n\t"
    "v_mfma_f32_16x16x32_bf16 v[40:43], a[116:119], a[212:215], v[40:43]\n\t"
    "v_exp_f32 v44, v44\n\t"
    "v_exp_f32 v45, v45\n\t"
    "v_mfma_f32_16x16x32_bf16 v[40:43], a[120:123], a[216:219], v[40:43]\n\t"
    "ds_read_b64_tr_b16 a[154:155], v17 offset:10240\n\t"
    "v_exp_f32 v46, v46\n\t"
    "v_exp_f32 v47, v47\n\t"
    "v_mfma_f32_16x16x32_bf16 v[40:43], a[124:127], a[220:223], v[40:43]\n\t"
    "v_exp_f32 v48, v48\n\t"
    "v_exp_f32 v49, v49\n\t"
    "v_mfma_f32_16x16x32_bf16 v[168:171], a[128:131], v[72:75], 0\n\t"
    "v_exp_f32 v50, v50\n\t"
    "v_exp_f32 v51, v51\n\t"
    "v_mfma_f32_16x16x32_bf16 v[168:171], a[132:135], v[84:87], v[168:171]\n\t"
    "v_cvt_pk_bf16_f32 v108, v44, v45\n\t"
    "v_cvt_pk_bf16_f32 v109, v46, v47\n\t"
    "v_cvt_pk_bf16_f32 v110, v48, v49\n\t"
    "v_cvt_pk_bf16_f32 v111, v50, v51\n\t"
    "v_mfma_f32_16x16x32_bf16 v[168:171], a[136:139], v[96:99], v[168:171]\n\t"
    "ds_read_b64_tr_b16 a[156:157], v17 offset:12288\n\t"
    "ds_read_b64_tr_b16 a[158:159], v17 offset:14336\n\t"
    "v_mfma_f32_16x16x32_bf16 v[168:171], a[140:143], v[108:111], v[168:171]\n\t"
    "s_waitcnt lgkmcnt(4)\n\t"
    "buffer_load_dwordx4 v23, s[40:43], s78 offen offset:3072 nt lds\n\t"
    "v_mfma_f32_16x16x32_bf16 v[44:47], a[96:99], a[176:179], v[0:3]\n\t"
    "s_add_u32 s78, s55, 69632\n\t"
    "ds_read_b128 a[112:115], v12 offset:45056\n\t"
    "v_mfma_f32_16x16x32_bf16 v[44:47], a[100:103], a[180:183], v[44:47]\n\t"
    "s_mov_b32 m0, s73\n\t"
    "v_exp_f32 v52, v52\n\t"
    "v_mfma_f32_16x16x32_bf16 v[44:47], a[104:107], a[184:187], v[44:47]\n\t"
    "ds_read_b128 a[116:119], v13 offset:45056\n\t"
    "v_mfma_f32_16x16x32_bf16 v[44:47], a[108:111], a[188:191], v[44:47]\n\t"
    "buffer_load_dwordx4 v20, s[40:43], s78 offen offset:0 nt lds\n\t"
    "v_mfma_f32_16x16x32_bf16 v[172:175], a[144:147], v[64:67], 0\n\t"
    "ds_read_b128 a[120:123], v14 offset:45056\n\t"
    "v_mfma_f32_16x16x32_bf16 v[172:175], a[148:151], v[76:79], v[172:175]\n\t"
    "s_waitcnt lgkmcnt(5)\n\t"
    "buffer_load_dwordx4 v21, s[40:43], s78 offen offset:1024 nt lds\n\t"
    "v_mfma_f32_16x16x32_bf16 v[172:175], a[152:155], v[88:91], v[172:175]\n\t"
    "s_waitcnt lgkmcnt(3)\n\t"
    "v_exp_f32 v53, v53\n\t"
    "v_mfma_f32_16x16x32_bf16 v[172:175], a[156:159], v[100:103], v[172:175]\n\t"
    "ds_read_b128 a[124:127], v15 offset:45056\n\t"
    "v_cvt_pk_bf16_f32 v120, v52, v53\n\t"
    "v_mfma_f32_16x16x32_bf16 v[48:51], a[96:99], a[192:195], v[4:7]\n\t"
    "v_exp_f32 v54, v54\n\t"
    "v_mfma_f32_16x16x32_bf16 v[48:51], a[100:103], a[196:199], v[48:51]\n\t"
    "ds_read_b64_tr_b16 a[128:129], v18 offset:0\n\t"
    "ds_read_b64_tr_b16 a[130:131], v18 offset:2048\n\t"
    "v_mfma_f32_16x16x32_bf16 v[48:51], a[104:107], a[200:203], v[48:51]\n\t"
    "v_exp_f32 v55, v55\n\t"
    "v_mfma_f32_16x16x32_bf16 v[48:51], a[108:111], a[204:207], v[48:51]\n\t"
    "v_cvt_pk_bf16_f32 v121, v54, v55\n\t"
    "v_exp_f32 v56, v56\n\t"
    "v_mfma_f32_16x16x32_bf16 v[176:179], a[144:147], v[68:71], 0\n\t"
    "buffer_load_dwordx4 v22, s[40:43], s78 offen offset:2048 nt lds\n\t"
    "v_mfma_f32_16x16x32_bf16 v[176:179], a[148:151], v[80:83], v[176:179]\n\t"
    "v_exp_f32 v57, v57\n\t"
    "v_mfma_f32_16x16x32_bf16 v[176:179], a[152:155], v[92:95], v[176:179]\n\t"
    "ds_read_b64_tr_b16 a[132:133], v18 offset:4096\n\t"
    "v_cvt_pk_bf16_f32 v114, v56, v57\n\t"
    "v_mfma_f32_16x16x32_bf16 v[176:179], a[156:159], v[104:107], v[176:179]\n\t"
    "v_exp_f32 v58, v58\n\t"
    "v_mfma_f32_16x16x32_bf16 a[160:163], a[172:175], v[64:67], 0\n\t"
    "ds_read_b64_tr_b16 a[134:135], v18 offset:6144\n\t"
    "ds_read_b64_tr_b16 a[136:137], v18 offset:8192\n\t"
    "v_mfma_f32_16x16x32_bf16 a[160:163], a[172:175], v[76:79], a[160:163]\n\t"
    "v_exp_f32 v59, v59\n\t"
    "v_mfma_f32_16x16x32_bf16 a[160:163], a[172:175], v[88:91], a[160:163]\n\t"
    "v_cvt_pk_bf16_f32 v115, v58, v59\n\t"
    "v_mfma_f32_16x16x32_bf16 a[160:163], a[172:175], v[100:103], a[160:163]\n\t"
    "buffer_load_dwordx4 v23, s[40:43], s78 offen offset:3072 nt lds\n\t"
    "v_mfma_f32_16x16x32_bf16 v[52:55], a[96:99], a[208:211], v[8:11]\n\t"
    "v_exp_f32 v60, v60\n\t"
    "v_mfma_f32_16x16x32_bf16 v[52:55], a[100:103], a[212:215], v[52:55]\n\t"
    "ds_read_b64_tr_b16 a[138:139], v18 offset:10240\n\t"
    "ds_read_b64_tr_b16 a[140:141], v18 offset:12288\n\t"
    "v_mfma_f32_16x16x32_bf16 v[52:55], a[104:107], a[216:219], v[52:55]\n\t"
    "v_exp_f32 v61, v61\n\t"
    "v_mfma_f32_16x16x32_bf16 v[52:55], a[108:111], a[220:223], v[52:55]\n\t"
    "v_exp_f32 v62, v62\n\t"
    "v_mfma_f32_16x16x32_bf16 v[180:183], a[144:147], v[72:75], 0\n\t"
    "s_add_u32 s79, s61, 32768\n\t"
    "v_exp_f32 v63, v63\n\t"
    "v_cvt_pk_bf16_f32 v118, v60, v61\n\t"
    "v_mfma_f32_16x16x32_bf16 v[180:183], a[148:151], v[84:87], v[180:183]\n\t"
    "s_add_u32 m0, s77, 32768\n\t"
    "v_cvt_pk_bf16_f32 v119, v62, v63\n\t"
    "v_mfma_f32_16x16x32_bf16 v[180:183], a[152:155], v[96:99], v[180:183]\n\t"
    "ds_read_b64_tr_b16 a[142:143], v18 offset:14336\n\t"
    "v_mfma_f32_16x16x32_bf16 v[180:183], a[156:159], v[108:111], v[180:183]\n\t"
    "s_waitcnt vmcnt(8) lgkmcnt(3)\n\t"
    "buffer_load_dwordx4 v24, s[44:47], s79 offen offset:0 nt lds\n\t"
    "v_mfma_f32_16x16x32_bf16 v[56:59], a[112:115], a[176:179], v[0:3]\n\t"
    "ds_read_b128 a[96:99], v12 offset:49152\n\t"
    "v_mfma_f32_16x16x32_bf16 v[56:59], a[116:119], a[180:183], v[56:59]\n\t"
    "ds_read_b128 a[100:103], v13 offset:49152\n\t"
    "v_mfma_f32_16x16x32_bf16 v[56:59], a[120:123], a[184:187], v[56:59]\n\t"
    "ds_read_b128 a[48:51], v28 offset:0\n\t"
    "v_mfma_f32_16x16x32_bf16 v[56:59], a[124:127], a[188:191], v[56:59]\n\t"
    "ds_read_b128 a[104:107], v14 offset:49152\n\t"
    "v_mfma_f32_16x16x32_bf16 v[184:187], a[128:131], v[64:67], 0\n\t"
    "ds_read_b128 a[52:55], v29 offset:0\n\t"
    "v_exp_f32 v40, v40\n\t"
    "v_mfma_f32_16x16x32_bf16 v[184:187], a[132:135], v[76:79], v[184:187]\n\t"
    "s_waitcnt lgkmcnt(6)\n\t"
    "ds_read_b128 a[108:111], v15 offset:49152\n\t"
    "ds_read_b64_tr_b16 a[144:145], v19 offset:0\n\t"
    "v_mfma_f32_16x16x32_bf16 v[184:187], a[136:139], v[88:91], v[184:187]\n\t"
    "s_waitcnt lgkmcnt(7)\n\t"
    "buffer_load_dwordx4 v25, s[44:47], s79 offen offset:1024 nt lds\n\t"
    "v_mfma_f32_16x16x32_bf16 v[184:187], a[140:143], v[100:103], v[184:187]\n\t"
    "v_exp_f32 v41, v41\n\t"
    "v_mfma_f32_16x16x32_bf16 v[60:63], a[112:115], a[192:195], v[4:7]\n\t"
    "v_exp_f32 v42, v42\n\t"
    "v_mfma_f32_16x16x32_bf16 v[60:63], a[116:119], a[196:199], v[60:63]\n\t"
    "ds_read_b64_tr_b16 a[146:147], v19 offset:2048\n\t"
    "v_exp_f32 v43, v43\n\t"
    "v_mfma_f32_16x16x32_bf16 v[60:63], a[120:123], a[200:203], v[60:63]\n\t"
    "ds_read_b128 a[56:59], v30 offset:0\n\t"
    "v_cvt_pk_bf16_f32 v122, v40, v41\n\t"
    "v_mfma_f32_16x16x32_bf16 v[60:63], a[124:127], a[204:207], v[60:63]\n\t"
    "ds_read_b64_tr_b16 a[148:149], v19 offset:4096\n\t"
    "v_cvt_pk_bf16_f32 v123, v42, v43\n\t"
    "v_mfma_f32_16x16x32_bf16 v[188:191], a[128:131], v[68:71], 0\n\t"
    "ds_read_b128 a[60:63], v31 offset:0\n\t"
    "ds_read_b64_tr_b16 a[150:151], v19 offset:6144\n\t"
    "v_mfma_f32_16x16x32_bf16 v[188:191], a[132:135], v[80:83], v[188:191]\n\t"
    "buffer_load_dwordx4 v26, s[44:47], s79 offen offset:2048 nt lds\n\t"
    "v_mfma_f32_16x16x32_bf16 v[188:191], a[136:139], v[92:95], v[188:191]\n\t"
    "v_exp_f32 v44, v44\n\t"
    "v_mfma_f32_16x16x32_bf16 v[188:191], a[140:143], v[104:107], v[188:191]\n\t"
    "ds_read_b64_tr_b16 a[152:153], v19 offset:8192\n\t"
    "v_exp_f32 v45, v45\n\t"
    "v_mfma_f32_16x16x32_bf16 v[40:43], a[112:115], a[208:211], v[8:11]\n\t"
    "v_exp_f32 v46, v46\n\t"
    "v_mfma_f32_16x16x32_bf16 v[40:43], a[116:119], a[212:215], v[40:43]\n\t"
    "ds_read_b64_tr_b16 a[154:155], v19 offset:10240\n\t"
    "v_cvt_pk_bf16_f32 v124, v44, v45\n\t"
    "v_mfma_f32_16x16x32_bf16 v[40:43], a[120:123], a[216:219], v[40:43]\n\t"
    "v_exp_f32 v47, v47\n\t"
    "v_mfma_f32_16x16x32_bf16 v[40:43], a[124:127], a[220:223], v[40:43]\n\t"
    "ds_read_b64_tr_b16 a[156:157], v19 offset:12288\n\t"
    "v_cvt_pk_bf16_f32 v125, v46, v47\n\t"
    "v_mfma_f32_16x16x32_bf16 v[192:195], a[128:131], v[72:75], 0\n\t"
    "buffer_load_dwordx4 v27, s[44:47], s79 offen offset:3072 nt lds\n\t"
    "v_mfma_f32_16x16x32_bf16 v[192:195], a[132:135], v[84:87], v[192:195]\n\t"
    "ds_read_b64_tr_b16 a[158:159], v19 offset:14336\n\t"
    "v_exp_f32 v48, v48\n\t"
    "v_mfma_f32_16x16x32_bf16 v[192:195], a[136:139], v[96:99], v[192:195]\n\t"
    "v_exp_f32 v49, v49\n\t"
    "v_mfma_f32_16x16x32_bf16 v[192:195], a[140:143], v[108:111], v[192:195]\n\t"
    "s_waitcnt lgkmcnt(3)\n\t"
    "v_exp_f32 v50, v50\n\t"
    "v_mfma_f32_16x16x32_bf16 v[44:47], a[96:99], a[176:179], v[0:3]\n\t"
    "s_add_u32 s78, s55, 86016\n\t"
    "v_exp_f32 v51, v51\n\t"
    "v_mfma_f32_16x16x32_bf16 v[44:47], a[100:103], a[180:183], v[44:47]\n\t"
    "s_mov_b32 m0, s74\n\t"
    "v_cvt_pk_bf16_f32 v128, v48, v49\n\t"
    "v_cvt_pk_bf16_f32 v129, v50, v51\n\t"
    "v_mfma_f32_16x16x32_bf16 v[44:47], a[104:107], a[184:187], v[44:47]\n\t"
    "buffer_load_dwordx4 v20, s[40:43], s78 offen offset:0 nt lds\n\t"
    "v_mfma_f32_16x16x32_bf16 v[44:47], a[108:111], a[188:191], v[44:47]\n\t"
    "v_exp_f32 v52, v52\n\t"
    "v_mfma_f32_16x16x32_bf16 v[196:199], a[144:147], v[64:67], 0\n\t"
    "v_exp_f32 v53, v53\n\t"
    "v_mfma_f32_16x16x32_bf16 v[196:199], a[148:151], v[76:79], v[196:199]\n\t"
    "s_waitcnt lgkmcnt(0)\n\t"
    "v_exp_f32 v54, v54\n\t"
    "v_cvt_pk_bf16_f32 v132, v52, v53\n\t"
    "v_mfma_f32_16x16x32_bf16 v[196:199], a[152:155], v[88:91], v[196:199]\n\t"
    "s_waitcnt vmcnt(9) lgkmcnt(0)\n\t"
    "v_exp_f32 v55, v55\n\t"
    "v_mfma_f32_16x16x32_bf16 v[196:199], a[156:159], v[100:103], v[196:199]\n\t"
    "s_barrier\n\t"
    "ds_read_b128 a[112:115], v12 offset:53248\n\t"
    "v_cvt_pk_bf16_f32 v133, v54, v55\n\t"
    "v_mfma_f32_16x16x32_bf16 v[48:51], a[96:99], a[192:195], v[4:7]\n\t"
    "ds_read_b128 a[116:119], v13 offset:53248\n\t"
    "v_exp_f32 v56, v56\n\t"
    "v_mfma_f32_16x16x32_bf16 v[48:51], a[100:103], a[196:199], v[48:51]\n\t"
    "buffer_load_dwordx4 v21, s[40:43], s78 offen offset:1024 nt lds\n\t"
    "v_mfma_f32_16x16x32_bf16 v[48:51], a[104:107], a[200:203], v[48:51]\n\t"
    "ds_read_b128 a[120:123], v14 offset:53248\n\t"
    "v_mfma_f32_16x16x32_bf16 v[48:51], a[108:111], a[204:207], v[48:51]\n\t"
    "v_exp_f32 v57, v57\n\t"
    "v_mfma_f32_16x16x32_bf16 v[200:203], a[144:147], v[68:71], 0\n\t"
    "ds_read_b128 a[124:127], v15 offset:53248\n\t"
    "v_exp_f32 v58, v58\n\t"
    "v_mfma_f32_16x16x32_bf16 v[200:203], a[148:151], v[80:83], v[200:203]\n\t"
    "ds_read_b64_tr_b16 a[128:129], v16 offset:16384\n\t"
    "v_cvt_pk_bf16_f32 v126, v56, v57\n\t"
    "v_mfma_f32_16x16x32_bf16 v[200:203], a[152:155], v[92:95], v[200:203]\n\t"
    "v_exp_f32 v59, v59\n\t"
    "v_mfma_f32_16x16x32_bf16 v[200:203], a[156:159], v[104:107], v[200:203]\n\t"
    "ds_read_b64_tr_b16 a[130:131], v16 offset:18432\n\t"
    "v_cvt_pk_bf16_f32 v127, v58, v59\n\t"
    "v_mfma_f32_16x16x32_bf16 v[52:55], a[96:99], a[208:211], v[8:11]\n\t"
    "v_exp_f32 v60, v60\n\t"
    "v_mfma_f32_16x16x32_bf16 v[52:55], a[100:103], a[212:215], v[52:55]\n\t"
    "ds_read_b64_tr_b16 a[132:133], v16 offset:20480\n\t"
    "v_exp_f32 v61, v61\n\t"
    "v_mfma_f32_16x16x32_bf16 v[52:55], a[104:107], a[216:219], v[52:55]\n\t"
    "v_exp_f32 v62, v62\n\t"
    "v_mfma_f32_16x16x32_bf16 v[52:55], a[108:111], a[220:223], v[52:55]\n\t"
    "ds_read_b64_tr_b16 a[134:135], v16 offset:22528\n\t"
    "v_exp_f32 v63, v63\n\t"
    "v_mfma_f32_16x16x32_bf16 v[56:59], a[228:231], a[48:51], 0\n\t"
    "v_cvt_pk_bf16_f32 v130, v60, v61\n\t"
    "v_cvt_pk_bf16_f32 v131, v62, v63\n\t"
    "v_mfma_f32_16x16x32_bf16 v[56:59], a[232:235], a[48:51], v[56:59]\n\t"
    "ds_read_b64_tr_b16 a[136:137], v16 offset:24576\n\t"
    "ds_read_b64_tr_b16 a[138:139], v16 offset:26624\n\t"
    "v_mfma_f32_16x16x32_bf16 v[60:63], a[236:239], a[48:51], 0\n\t"
    "buffer_load_dwordx4 v22, s[40:43], s78 offen offset:2048 nt lds\n\t"
    "v_mfma_f32_16x16x32_bf16 v[60:63], a[240:243], a[48:51], v[60:63]\n\t"
    "v_exp_f32 v40, v40\n\t"
    "v_mfma_f32_16x16x32_bf16 v[204:207], a[144:147], v[72:75], 0\n\t"
    "ds_read_b64_tr_b16 a[140:141], v16 offset:28672\n\t"
    "v_exp_f32 v41, v41\n\t"
    "v_mfma_f32_16x16x32_bf16 v[204:207], a[148:151], v[84:87], v[204:207]\n\t"
    "v_exp_f32 v42, v42\n\t"
    "v_cvt_pk_bf16_f32 v134, v40, v41\n\t"
    "v_mfma_f32_16x16x32_bf16 v[204:207], a[152:155], v[96:99], v[204:207]\n\t"
    "ds_read_b64_tr_b16 a[142:143], v16 offset:30720\n\t"
    "v_exp_f32 v43, v43\n\t"
    "v_mfma_f32_16x16x32_bf16 v[204:207], a[156:159], v[108:111], v[204:207]\n\t"
    "s_waitcnt lgkmcnt(4)\n\t"
    "v_cvt_pk_bf16_f32 v135, v42, v43\n\t"
    "v_cvt_pk_bf16_f32 v56, v56, v57\n\t"
    "v_mfma_f32_16x16x32_bf16 v[40:43], a[112:115], a[176:179], v[0:3]\n\t"
    "ds_read_b128 a[96:99], v12 offset:57344\n\t"
    "v_cvt_pk_bf16_f32 v58, v58, v59\n\t"
    "v_mfma_f32_16x16x32_bf16 v[40:43], a[116:119], a[180:183], v[40:43]\n\t"
    "buffer_load_dwordx4 v23, s[40:43], s78 offen offset:3072 nt lds\n\t"
    "v_mfma_f32_16x16x32_bf16 v[40:43], a[120:123], a[184:187], v[40:43]\n\t"
    "s_add_u32 s79, s61, 32896\n\t"
    "ds_read_b128 a[100:103], v13 offset:57344\n\t"
    "v_accvgpr_write_b32 a48, v56\n\t"
    "v_mfma_f32_16x16x32_bf16 v[40:43], a[124:127], a[188:191], v[40:43]\n\t"
    "s_add_u32 m0, s77, 49152\n\t"
    "v_exp_f32 v44, v44\n\t"
    "v_mfma_f32_16x16x32_bf16 v[208:211], a[128:131], v[64:67], 0\n\t"
    "ds_read_b128 a[104:107], v14 offset:57344\n\t"
    "v_accvgpr_write_b32 a49, v58\n\t"
    "v_mfma_f32_16x16x32_bf16 v[208:211], a[132:135], v[76:79], v[208:211]\n\t"
    "s_waitcnt lgkmcnt(4)\n\t"
    "v_exp_f32 v45, v45\n\t"
    "v_cvt_pk_bf16_f32 v60, v60, v61\n\t"
    "v_mfma_f32_16x16x32_bf16 v[208:211], a[136:139], v[88:91], v[208:211]\n\t"
    "s_waitcnt lgkmcnt(3)\n\t"
    "v_exp_f32 v46, v46\n\t"
    "v_exp_f32 v47, v47\n\t"
    "v_mfma_f32_16x16x32_bf16 v[208:211], a[140:143], v[100:103], v[208:211]\n\t"
    "v_cvt_pk_bf16_f32 v136, v44, v45\n\t"
    "v_cvt_pk_bf16_f32 v137, v46, v47\n\t"
    "v_mfma_f32_16x16x32_bf16 v[44:47], a[112:115], a[192:195], v[4:7]\n\t"
    "ds_read_b128 a[108:111], v15 offset:57344\n\t"
    "v_cvt_pk_bf16_f32 v62, v62, v63\n\t"
    "v_mfma_f32_16x16x32_bf16 v[44:47], a[116:119], a[196:199], v[44:47]\n\t"
    "buffer_load_dwordx4 v24, s[44:47], s79 offen offset:0 nt lds\n\t"
    "v_mfma_f32_16x16x32_bf16 v[44:47], a[120:123], a[200:203], v[44:47]\n\t"
    "ds_read_b64_tr_b16 a[144:145], v17 offset:16384\n\t"
    "v_accvgpr_write_b32 a50, v60\n\t"
    "v_mfma_f32_16x16x32_bf16 v[44:47], a[124:127], a[204:207], v[44:47]\n\t"
    "ds_read_b64_tr_b16 a[146:147], v17 offset:18432\n\t"
    "v_accvgpr_write_b32 a51, v62\n\t"
    "v_mfma_f32_16x16x32_bf16 v[212:215], a[128:131], v[68:71], 0\n\t"
    "buffer_load_dwordx4 v25, s[44:47], s79 offen offset:1024 nt lds\n\t"
    "v_mfma_f32_16x16x32_bf16 v[212:215], a[132:135], v[80:83], v[212:215]\n\t"
    "v_exp_f32 v48, v48\n\t"
    "v_mfma_f32_16x16x32_bf16 v[212:215], a[136:139], v[92:95], v[212:215]\n\t"
    "ds_read_b64_tr_b16 a[148:149], v17 offset:20480\n\t"
    "ds_read_b64_tr_b16 a[150:151], v17 offset:22528\n\t"
    "v_mfma_f32_16x16x32_bf16 v[212:215], a[140:143], v[104:107], v[212:215]\n\t"
    "v_exp_f32 v49, v49\n\t"
    "v_mfma_f32_16x16x32_bf16 a[164:167], a[172:175], v[68:71], 0\n\t"
    "v_exp_f32 v50, v50\n\t"
    "v_mfma_f32_16x16x32_bf16 a[164:167], a[172:175], v[80:83], a[164:167]\n\t"
    "v_exp_f32 v51, v51\n\t"
    "v_mfma_f32_16x16x32_bf16 a[164:167], a[172:175], v[92:95], a[164:167]\n\t"
    "v_cvt_pk_bf16_f32 v140, v48, v49\n\t"
    "v_cvt_pk_bf16_f32 v141, v50, v51\n\t"
    "v_mfma_f32_16x16x32_bf16 a[164:167], a[172:175], v[104:107], a[164:167]\n\t"
    "ds_read_b64_tr_b16 a[152:153], v17 offset:24576\n\t"
    "ds_read_b64_tr_b16 a[154:155], v17 offset:26624\n\t"
    "v_mfma_f32_16x16x32_bf16 v[48:51], a[112:115], a[208:211], v[8:11]\n\t"
    "buffer_load_dwordx4 v26, s[44:47], s79 offen offset:2048 nt lds\n\t"
    "v_mfma_f32_16x16x32_bf16 v[48:51], a[116:119], a[212:215], v[48:51]\n\t"
    "v_exp_f32 v52, v52\n\t"
    "v_mfma_f32_16x16x32_bf16 v[48:51], a[120:123], a[216:219], v[48:51]\n\t"
    "v_exp_f32 v53, v53\n\t"
    "v_mfma_f32_16x16x32_bf16 v[48:51], a[124:127], a[220:223], v[48:51]\n\t"
    "ds_read_b64_tr_b16 a[156:157], v17 offset:28672\n\t"
    "v_cvt_pk_bf16_f32 v144, v52, v53\n\t"
    "v_mfma_f32_16x16x32_bf16 v[216:219], a[128:131], v[72:75], 0\n\t"
    "v_exp_f32 v54, v54\n\t"
    "v_mfma_f32_16x16x32_bf16 v[216:219], a[132:135], v[84:87], v[216:219]\n\t"
    "v_exp_f32 v55, v55\n\t"
    "v_mfma_f32_16x16x32_bf16 v[216:219], a[136:139], v[96:99], v[216:219]\n\t"
    "ds_read_b64_tr_b16 a[158:159], v17 offset:30720\n\t"
    "v_cvt_pk_bf16_f32 v145, v54, v55\n\t"
    "v_mfma_f32_16x16x32_bf16 v[216:219], a[140:143], v[108:111], v[216:219]\n\t"
    "s_waitcnt lgkmcnt(2)\n\t"
    "buffer_load_dwordx4 v27, s[44:47], s79 offen offset:3072 nt lds\n\t"
    "v_mfma_f32_16x16x32_bf16 v[52:55], a[96:99], a[176:179], v[0:3]\n\t"
    "s_mov_b32 m0, s85\n\t"
    "ds_read_b128 a[112:115], v12 offset:61440\n\t"
    "v_mfma_f32_16x16x32_bf16 v[52:55], a[100:103], a[180:183], v[52:55]\n\t"
    "v_exp_f32 v40, v40\n\t"
    "v_mfma_f32_16x16x32_bf16 v[52:55], a[104:107], a[184:187], v[52:55]\n\t"
    "ds_read_b128 a[116:119], v13 offset:61440\n\t"
    "v_mfma_f32_16x16x32_bf16 v[52:55], a[108:111], a[188:191], v[52:55]\n\t"
    "buffer_load_dwordx4 v20, s[36:39], s68 offen offset:0 nt lds\n\t"
    "v_mfma_f32_16x16x32_bf16 v[220:223], a[144:147], v[64:67], 0\n\t"
    "ds_read_b128 a[120:123], v14 offset:61440\n\t"
    "v_mfma_f32_16x16x32_bf16 v[220:223], a[148:151], v[76:79], v[220:223]\n\t"
    "v_exp_f32 v41, v41\n\t"
    "v_mfma_f32_16x16x32_bf16 v[220:223], a[152:155], v[88:91], v[220:223]\n\t"
    "s_waitcnt lgkmcnt(3)\n\t"
    "v_exp_f32 v42, v42\n\t"
    "v_mfma_f32_16x16x32_bf16 v[220:223], a[156:159], v[100:103], v[220:223]\n\t"
    "ds_read_b128 a[124:127], v15 offset:61440\n\t"
    "v_cvt_pk_bf16_f32 v138, v40, v41\n\t"
    "v_mfma_f32_16x16x32_bf16 v[56:59], a[96:99], a[192:195], v[4:7]\n\t"
    "v_exp_f32 v43, v43\n\t"
    "v_mfma_f32_16x16x32_bf16 v[56:59], a[100:103], a[196:199], v[56:59]\n\t"
    "ds_read_b64_tr_b16 a[128:129], v18 offset:16384\n\t"
    "v_cvt_pk_bf16_f32 v139, v42, v43\n\t"
    "v_mfma_f32_16x16x32_bf16 v[56:59], a[104:107], a[200:203], v[56:59]\n\t"
    "v_exp_f32 v44, v44\n\t"
    "v_mfma_f32_16x16x32_bf16 v[56:59], a[108:111], a[204:207], v[56:59]\n\t"
    "ds_read_b64_tr_b16 a[130:131], v18 offset:18432\n\t"
    "ds_read_b64_tr_b16 a[132:133], v18 offset:20480\n\t"
    "v_mfma_f32_16x16x32_bf16 v[224:227], a[144:147], v[68:71], 0\n\t"
    "buffer_load_dwordx4 v21, s[36:39], s68 offen offset:1024 nt lds\n\t"
    "v_mfma_f32_16x16x32_bf16 v[224:227], a[148:151], v[80:83], v[224:227]\n\t"
    "v_exp_f32 v45, v45\n\t"
    "v_mfma_f32_16x16x32_bf16 v[224:227], a[152:155], v[92:95], v[224:227]\n\t"
    "v_exp_f32 v46, v46\n\t"
    "v_mfma_f32_16x16x32_bf16 v[224:227], a[156:159], v[104:107], v[224:227]\n\t"
    "v_exp_f32 v47, v47\n\t"
    "v_mfma_f32_16x16x32_bf16 v[60:63], a[96:99], a[208:211], v[8:11]\n\t"
    "ds_read_b64_tr_b16 a[134:135], v18 offset:22528\n\t"
    "v_cvt_pk_bf16_f32 v142, v44, v45\n\t"
    "v_mfma_f32_16x16x32_bf16 v[60:63], a[100:103], a[212:215], v[60:63]\n\t"
    "v_cvt_pk_bf16_f32 v143, v46, v47\n\t"
    "v_mfma_f32_16x16x32_bf16 v[60:63], a[104:107], a[216:219], v[60:63]\n\t"
    "ds_read_b64_tr_b16 a[136:137], v18 offset:24576\n\t"
    "ds_read_b64_tr_b16 a[138:139], v18 offset:26624\n\t"
    "v_mfma_f32_16x16x32_bf16 v[60:63], a[108:111], a[220:223], v[60:63]\n\t"
    "v_exp_f32 v48, v48\n\t"
    "v_mfma_f32_16x16x32_bf16 v[40:43], a[228:231], a[52:55], 0\n\t"
    "v_exp_f32 v49, v49\n\t"
    "v_mfma_f32_16x16x32_bf16 v[40:43], a[232:235], a[52:55], v[40:43]\n\t"
    "v_exp_f32 v50, v50\n\t"
    "v_mfma_f32_16x16x32_bf16 v[44:47], a[236:239], a[52:55], 0\n\t"
    "v_exp_f32 v51, v51\n\t"
    "v_mfma_f32_16x16x32_bf16 v[44:47], a[240:243], a[52:55], v[44:47]\n\t"
    "ds_read_b64_tr_b16 a[140:141], v18 offset:28672\n\t"
    "v_cvt_pk_bf16_f32 v146, v48, v49\n\t"
    "v_mfma_f32_16x16x32_bf16 v[228:231], a[144:147], v[72:75], 0\n\t"
    "v_cvt_pk_bf16_f32 v147, v50, v51\n\t"
    "v_mfma_f32_16x16x32_bf16 v[228:231], a[148:151], v[84:87], v[228:231]\n\t"
    "buffer_load_dwordx4 v22, s[36:39], s68 offen offset:2048 nt lds\n\t"
    "v_mfma_f32_16x16x32_bf16 v[228:231], a[152:155], v[96:99], v[228:231]\n\t"
    "ds_read_b64_tr_b16 a[142:143], v18 offset:30720\n\t"
    "v_cvt_pk_bf16_f32 v40, v40, v41\n\t"
    "v_mfma_f32_16x16x32_bf16 v[228:231], a[156:159], v[108:111], v[228:231]\n\t"
    "s_waitcnt lgkmcnt(2)\n\t"
    "v_exp_f32 v52, v52\n\t"
    "v_mfma_f32_16x16x32_bf16 v[48:51], a[112:115], a[176:179], v[0:3]\n\t"
    "ds_read_b128 a[96:99], v12 offset:0\n\t"
    "v_cvt_pk_bf16_f32 v42, v42, v43\n\t"
    "v_mfma_f32_16x16x32_bf16 v[48:51], a[116:119], a[180:183], v[48:51]\n\t"
    "v_exp_f32 v53, v53\n\t"
    "v_mfma_f32_16x16x32_bf16 v[48:51], a[120:123], a[184:187], v[48:51]\n\t"
    "ds_read_b128 a[100:103], v13 offset:0\n\t"
    "v_cvt_pk_bf16_f32 v148, v52, v53\n\t"
    "v_mfma_f32_16x16x32_bf16 v[48:51], a[124:127], a[188:191], v[48:51]\n\t"
    "v_exp_f32 v54, v54\n\t"
    "v_mfma_f32_16x16x32_bf16 v[232:235], a[128:131], v[64:67], 0\n\t"
    "ds_read_b128 a[104:107], v14 offset:0\n\t"
    "v_accvgpr_write_b32 a52, v40\n\t"
    "v_mfma_f32_16x16x32_bf16 v[232:235], a[132:135], v[76:79], v[232:235]\n\t"
    "v_exp_f32 v55, v55\n\t"
    "v_mfma_f32_16x16x32_bf16 v[232:235], a[136:139], v[88:91], v[232:235]\n\t"
    "s_waitcnt lgkmcnt(3)\n\t"
    "ds_read_b128 a[108:111], v15 offset:0\n\t"
    "v_cvt_pk_bf16_f32 v149, v54, v55\n\t"
    "v_mfma_f32_16x16x32_bf16 v[232:235], a[140:143], v[100:103], v[232:235]\n\t"
    "buffer_load_dwordx4 v23, s[36:39], s68 offen offset:3072 nt lds\n\t"
    "v_mfma_f32_16x16x32_bf16 v[52:55], a[112:115], a[192:195], v[4:7]\n\t"
    "v_exp_f32 v56, v56\n\t"
    "v_mfma_f32_16x16x32_bf16 v[52:55], a[116:119], a[196:199], v[52:55]\n\t"
    "ds_read_b64_tr_b16 a[144:145], v19 offset:16384\n\t"
    "v_accvgpr_write_b32 a53, v42\n\t"
    "v_mfma_f32_16x16x32_bf16 v[52:55], a[120:123], a[200:203], v[52:55]\n\t"
    "v_exp_f32 v57, v57\n\t"
    "v_mfma_f32_16x16x32_bf16 v[52:55], a[124:127], a[204:207], v[52:55]\n\t"
    "ds_read_b64_tr_b16 a[146:147], v19 offset:18432\n\t"
    "v_cvt_pk_bf16_f32 v152, v56, v57\n\t"
    "v_mfma_f32_16x16x32_bf16 v[236:239], a[128:131], v[68:71], 0\n\t"
    "s_add_u32 s78, s55, 102400\n\t"
    "v_exp_f32 v58, v58\n\t"
    "v_mfma_f32_16x16x32_bf16 v[236:239], a[132:135], v[80:83], v[236:239]\n\t"
    "s_mov_b32 m0, s75\n\t"
    "ds_read_b64_tr_b16 a[148:149], v19 offset:20480\n\t"
    "v_cvt_pk_bf16_f32 v44, v44, v45\n\t"
    "v_mfma_f32_16x16x32_bf16 v[236:239], a[136:139], v[92:95], v[236:239]\n\t"
    "v_exp_f32 v59, v59\n\t"
    "v_mfma_f32_16x16x32_bf16 v[236:239], a[140:143], v[104:107], v[236:239]\n\t"
    "ds_read_b64_tr_b16 a[150:151], v19 offset:22528\n\t"
    "v_cvt_pk_bf16_f32 v153, v58, v59\n\t"
    "v_mfma_f32_16x16x32_bf16 a[168:171], a[172:175], v[72:75], 0\n\t"
    "buffer_load_dwordx4 v20, s[40:43], s78 offen offset:0 nt lds\n\t"
    "v_mfma_f32_16x16x32_bf16 a[168:171], a[172:175], v[84:87], a[168:171]\n\t"
    "v_exp_f32 v60, v60\n\t"
    "v_mfma_f32_16x16x32_bf16 a[168:171], a[172:175], v[96:99], a[168:171]\n\t"
    "ds_read_b64_tr_b16 a[152:153], v19 offset:24576\n\t"
    "v_cvt_pk_bf16_f32 v46, v46, v47\n\t"
    "v_mfma_f32_16x16x32_bf16 a[168:171], a[172:175], v[108:111], a[168:171]\n\t"
    "v_exp_f32 v61, v61\n\t"
    "v_mfma_f32_16x16x32_bf16 v[56:59], a[112:115], a[208:211], v[8:11]\n\t"
    "ds_read_b64_tr_b16 a[154:155], v19 offset:26624\n\t"
    "v_cvt_pk_bf16_f32 v156, v60, v61\n\t"
    "v_mfma_f32_16x16x32_bf16 v[56:59], a[116:119], a[212:215], v[56:59]\n\t"
    "v_exp_f32 v62, v62\n\t"
    "v_mfma_f32_16x16x32_bf16 v[56:59], a[120:123], a[216:219], v[56:59]\n\t"
    "ds_read_b64_tr_b16 a[156:157], v19 offset:28672\n\t"
    "v_accvgpr_write_b32 a54, v44\n\t"
    "v_mfma_f32_16x16x32_bf16 v[56:59], a[124:127], a[220:223], v[56:59]\n\t"
    "v_exp_f32 v63, v63\n\t"
    "v_mfma_f32_16x16x32_bf16 v[240:243], a[128:131], v[72:75], 0\n\t"
    "v_cvt_pk_bf16_f32 v157, v62, v63\n\t"
    "v_accvgpr_write_b32 a55, v46\n\t"
    "v_mfma_f32_16x16x32_bf16 v[240:243], a[132:135], v[84:87], v[240:243]\n\t"
    "ds_read_b64_tr_b16 a[158:159], v19 offset:30720\n\t"
    "v_mfma_f32_16x16x32_bf16 v[240:243], a[136:139], v[96:99], v[240:243]\n\t"
    "buffer_load_dwordx4 v21, s[40:43], s78 offen offset:1024 nt lds\n\t"
    "v_mfma_f32_16x16x32_bf16 v[240:243], a[140:143], v[108:111], v[240:243]\n\t"
    "s_waitcnt lgkmcnt(3)\n\t"
    "v_exp_f32 v48, v48\n\t"
    "v_mfma_f32_16x16x32_bf16 v[60:63], a[96:99], a[176:179], v[0:3]\n\t"
    "buffer_load_dwordx4 v22, s[40:43], s78 offen offset:2048 nt lds\n\t"
    "v_mfma_f32_16x16x32_bf16 v[60:63], a[100:103], a[180:183], v[60:63]\n\t"
    "v_exp_f32 v49, v49\n\t"
    "v_mfma_f32_16x16x32_bf16 v[60:63], a[104:107], a[184:187], v[60:63]\n\t"
    "v_exp_f32 v50, v50\n\t"
    "v_mfma_f32_16x16x32_bf16 v[60:63], a[108:111], a[188:191], v[60:63]\n\t"
    "v_exp_f32 v51, v51\n\t"
    "v_mfma_f32_16x16x32_bf16 v[244:247], a[144:147], v[64:67], 0\n\t"
    "v_cvt_pk_bf16_f32 v150, v48, v49\n\t"
    "v_cvt_pk_bf16_f32 v151, v50, v51\n\t"
    "v_mfma_f32_16x16x32_bf16 v[244:247], a[148:151], v[76:79], v[244:247]\n\t"
    "s_waitcnt lgkmcnt(0)\n\t"
    "v_exp_f32 v52, v52\n\t"
    "v_mfma_f32_16x16x32_bf16 v[244:247], a[152:155], v[88:91], v[244:247]\n\t"
    "s_waitcnt vmcnt(15) lgkmcnt(0)\n\t"
    "v_exp_f32 v53, v53\n\t"
    "v_mfma_f32_16x16x32_bf16 v[244:247], a[156:159], v[100:103], v[244:247]\n\t"
    "s_barrier\n\t"
    "ds_read_b128 a[112:115], v12 offset:4096\n\t"
    "v_cvt_pk_bf16_f32 v154, v52, v53\n\t"
    "v_mfma_f32_16x16x32_bf16 v[40:43], a[96:99], a[192:195], v[4:7]\n\t"
    "ds_read_b128 a[116:119], v13 offset:4096\n\t"
    "v_mfma_f32_16x16x32_bf16 v[40:43], a[100:103], a[196:199], v[40:43]\n\t"
    "buffer_load_dwordx4 v23, s[40:43], s78 offen offset:3072 nt lds\n\t"
    "v_mfma_f32_16x16x32_bf16 v[40:43], a[104:107], a[200:203], v[40:43]\n\t"
    "ds_read_b128 a[120:123], v14 offset:4096\n\t"
    "v_mfma_f32_16x16x32_bf16 v[40:43], a[108:111], a[204:207], v[40:43]\n\t"
    "v_exp_f32 v54, v54\n\t"
    "v_mfma_f32_16x16x32_bf16 v[248:251], a[144:147], v[68:71], 0\n\t"
    "s_add_u32 s79, s61, 65536\n\t"
    "ds_read_b128 a[124:127], v15 offset:4096\n\t"
    "ds_read_b64_tr_b16 a[128:129], v16 offset:32768\n\t"
    "v_mfma_f32_16x16x32_bf16 v[248:251], a[148:151], v[80:83], v[248:251]\n\t"
    "s_add_u32 m0, s77, 0\n\t"
    "v_exp_f32 v55, v55\n\t"
    "v_mfma_f32_16x16x32_bf16 v[248:251], a[152:155], v[92:95], v[248:251]\n\t"
    "v_cvt_pk_bf16_f32 v155, v54, v55\n\t"
    "v_mfma_f32_16x16x32_bf16 v[248:251], a[156:159], v[104:107], v[248:251]\n\t"
    "ds_read_b64_tr_b16 a[130:131], v16 offset:34816\n\t"
    "ds_read_b64_tr_b16 a[132:133], v16 offset:36864\n\t"
    "v_mfma_f32_16x16x32_bf16 v[44:47], a[96:99], a[208:211], v[8:11]\n\t"
    "buffer_load_dwordx4 v24, s[44:47], s79 offen offset:0 nt lds\n\t"
    "v_mfma_f32_16x16x32_bf16 v[44:47], a[100:103], a[212:215], v[44:47]\n\t"
    "v_exp_f32 v56, v56\n\t"
    "v_mfma_f32_16x16x32_bf16 v[44:47], a[104:107], a[216:219], v[44:47]\n\t"
    "v_exp_f32 v57, v57\n\t"
    "v_mfma_f32_16x16x32_bf16 v[44:47], a[108:111], a[220:223], v[44:47]\n\t"
    "ds_read_b64_tr_b16 a[134:135], v16 offset:38912\n\t"
    "v_cvt_pk_bf16_f32 v158, v56, v57\n\t"
    "v_mfma_f32_16x16x32_bf16 v[48:51], a[228:231], a[56:59], 0\n\t"
    "ds_read_b64_tr_b16 a[136:137], v16 offset:40960\n\t"
    "ds_read_b64_tr_b16 a[138:139], v16 offset:43008\n\t"
    "v_mfma_f32_16x16x32_bf16 v[48:51], a[232:235], a[56:59], v[48:51]\n\t"
    "v_exp_f32 v58, v58\n\t"
    "v_mfma_f32_16x16x32_bf16 v[52:55], a[236:239], a[56:59], 0\n\t"
    "v_exp_f32 v59, v59\n\t"
    "v_mfma_f32_16x16x32_bf16 v[52:55], a[240:243], a[56:59], v[52:55]\n\t"
    "v_cvt_pk_bf16_f32 v159, v58, v59\n\t"
    "v_mfma_f32_16x16x32_bf16 v[252:255], a[144:147], v[72:75], 0\n\t"
    "ds_read_b64_tr_b16 a[140:141], v16 offset:45056\n\t"
    "ds_read_b64_tr_b16 a[142:143], v16 offset:47104\n\t"
    "v_mfma_f32_16x16x32_bf16 v[252:255], a[148:151], v[84:87], v[252:255]\n\t"
    "buffer_load_dwordx4 v25, s[44:47], s79 offen offset:1024 nt lds\n\t"
    "v_mfma_f32_16x16x32_bf16 v[252:255], a[152:155], v[96:99], v[252:255]\n\t"
    "v_exp_f32 v60, v60\n\t"
    "v_mfma_f32_16x16x32_bf16 v[252:255], a[156:159], v[108:111], v[252:255]\n\t"
    "s_waitcnt lgkmcnt(4)\n\t"
    "v_exp_f32 v61, v61\n\t"
    "v_mfma_f32_16x16x32_bf16 v[56:59], a[112:115], a[176:179], v[0:3]\n\t"
    "ds_read_b128 a[96:99], v12 offset:8192\n\t"
    "v_cvt_pk_bf16_f32 v64, v60, v61\n\t"
    "v_mfma_f32_16x16x32_bf16 v[56:59], a[116:119], a[180:183], v[56:59]\n\t"
    "v_exp_f32 v62, v62\n\t"
    "v_mfma_f32_16x16x32_bf16 v[56:59], a[120:123], a[184:187], v[56:59]\n\t"
    "ds_read_b128 a[100:103], v13 offset:8192\n\t"
    "v_cvt_pk_bf16_f32 v48, v48, v49\n\t"
    "v_mfma_f32_16x16x32_bf16 v[56:59], a[124:127], a[188:191], v[56:59]\n\t"
    "v_exp_f32 v63, v63\n\t"
    "v_mfma_f32_16x16x32_bf16 v[160:163], a[128:131], v[112:115], v[160:163]\n\t"
    "ds_read_b128 a[104:107], v14 offset:8192\n\t"
    "v_cvt_pk_bf16_f32 v65, v62, v63\n\t"
    "v_mfma_f32_16x16x32_bf16 v[160:163], a[132:135], v[124:127], v[160:163]\n\t"
    "s_waitcnt lgkmcnt(3)\n\t"
    "buffer_load_dwordx4 v26, s[44:47], s79 offen offset:2048 nt lds\n\t"
    "v_mfma_f32_16x16x32_bf16 v[160:163], a[136:139], v[136:139], v[160:163]\n\t"
    "ds_read_b128 a[108:111], v15 offset:8192\n\t"
    "v_cvt_pk_bf16_f32 v50, v50, v51\n\t"
    "v_mfma_f32_16x16x32_bf16 v[160:163], a[140:143], v[148:151], v[160:163]\n\t"
    "v_exp_f32 v40, v40\n\t"
    "v_mfma_f32_16x16x32_bf16 v[60:63], a[112:115], a[192:195], v[4:7]\n\t"
    "ds_read_b64_tr_b16 a[144:145], v17 offset:32768\n\t"
    "v_accvgpr_write_b32 a56, v48\n\t"
    "v_mfma_f32_16x16x32_bf16 v[60:63], a[116:119], a[196:199], v[60:63]\n\t"
    "v_exp_f32 v41, v41\n\t"
    "v_mfma_f32_16x16x32_bf16 v[60:63], a[120:123], a[200:203], v[60:63]\n\t"
    "ds_read_b64_tr_b16 a[146:147], v17 offset:34816\n\t"
    "v_cvt_pk_bf16_f32 v68, v40, v41\n\t"
    "v_mfma_f32_16x16x32_bf16 v[60:63], a[124:127], a[204:207], v[60:63]\n\t"
    "v_exp_f32 v42, v42\n\t"
    "v_mfma_f32_16x16x32_bf16 v[164:167], a[128:131], v[116:119], v[164:167]\n\t"
    "ds_read_b64_tr_b16 a[148:149], v17 offset:36864\n\t"
    "v_accvgpr_write_b32 a57, v50\n\t"
    "v_mfma_f32_16x16x32_bf16 v[164:167], a[132:135], v[128:131], v[164:167]\n\t"
    "v_exp_f32 v43, v43\n\t"
    "v_mfma_f32_16x16x32_bf16 v[164:167], a[136:139], v[140:143], v[164:167]\n\t"
    "buffer_load_dwordx4 v27, s[44:47], s79 offen offset:3072 nt lds\n\t"
    "v_mfma_f32_16x16x32_bf16 v[164:167], a[140:143], v[152:155], v[164:167]\n\t"
    "s_mov_b32 s78, s58\n\t"
    "ds_read_b64_tr_b16 a[150:151], v17 offset:38912\n\t"
    "v_cvt_pk_bf16_f32 v69, v42, v43\n\t"
    "v_mfma_f32_16x16x32_bf16 v[40:43], a[112:115], a[208:211], v[8:11]\n\t"
    "s_mov_b32 m0, s76\n\t"
    "ds_read_b64_tr_b16 a[152:153], v17 offset:40960\n\t"
    "v_cvt_pk_bf16_f32 v52, v52, v53\n\t"
    "v_mfma_f32_16x16x32_bf16 v[40:43], a[116:119], a[212:215], v[40:43]\n\t"
    "buffer_load_dwordx4 v20, s[40:43], s78 offen offset:0 nt lds\n\t"
    "v_mfma_f32_16x16x32_bf16 v[40:43], a[120:123], a[216:219], v[40:43]\n\t"
    "ds_read_b64_tr_b16 a[154:155], v17 offset:43008\n\t"
    "v_exp_f32 v44, v44\n\t"
    "v_mfma_f32_16x16x32_bf16 v[40:43], a[124:127], a[220:223], v[40:43]\n\t"
    "v_exp_f32 v45, v45\n\t"
    "v_mfma_f32_16x16x32_bf16 v[168:171], a[128:131], v[120:123], v[168:171]\n\t"
    "ds_read_b64_tr_b16 a[156:157], v17 offset:45056\n\t"
    "v_cvt_pk_bf16_f32 v72, v44, v45\n\t"
    "v_mfma_f32_16x16x32_bf16 v[168:171], a[132:135], v[132:135], v[168:171]\n\t"
    "v_exp_f32 v46, v46\n\t"
    "v_mfma_f32_16x16x32_bf16 v[168:171], a[136:139], v[144:147], v[168:171]\n\t"
    "ds_read_b64_tr_b16 a[158:159], v17 offset:47104\n\t"
    "v_exp_f32 v47, v47\n\t"
    "v_mfma_f32_16x16x32_bf16 v[168:171], a[140:143], v[156:159], v[168:171]\n\t"
    "s_waitcnt lgkmcnt(4)\n\t"
    "v_cvt_pk_bf16_f32 v73, v46, v47\n\t"
    "v_cvt_pk_bf16_f32 v54, v54, v55\n\t"
    "v_mfma_f32_16x16x32_bf16 v[44:47], a[96:99], a[176:179], v[0:3]\n\t"
    "ds_read_b128 a[112:115], v12 offset:12288\n\t"
    "v_accvgpr_write_b32 a58, v52\n\t"
    "v_mfma_f32_16x16x32_bf16 v[44:47], a[100:103], a[180:183], v[44:47]\n\t"
    "buffer_load_dwordx4 v21, s[40:43], s78 offen offset:1024 nt lds\n\t"
    "v_mfma_f32_16x16x32_bf16 v[44:47], a[104:107], a[184:187], v[44:47]\n\t"
    "ds_read_b128 a[116:119], v13 offset:12288\n\t"
    "v_accvgpr_write_b32 a59, v54\n\t"
    "v_mfma_f32_16x16x32_bf16 v[44:47], a[108:111], a[188:191], v[44:47]\n\t"
    "v_exp_f32 v56, v56\n\t"
    "v_mfma_f32_16x16x32_bf16 v[172:175], a[144:147], v[112:115], v[172:175]\n\t"
    "v_exp_f32 v57, v57\n\t"
    "v_mfma_f32_16x16x32_bf16 v[172:175], a[148:151], v[124:127], v[172:175]\n\t"
    "s_waitcnt lgkmcnt(3)\n\t"
    "ds_read_b128 a[120:123], v14 offset:12288\n\t"
    "v_cvt_pk_bf16_f32 v66, v56, v57\n\t"
    "v_mfma_f32_16x16x32_bf16 v[172:175], a[152:155], v[136:139], v[172:175]\n\t"
    "s_waitcnt lgkmcnt(3)\n\t"
    "v_exp_f32 v58, v58\n\t"
    "v_mfma_f32_16x16x32_bf16 v[172:175], a[156:159], v[148:151], v[172:175]\n\t"
    "v_exp_f32 v59, v59\n\t"
    "v_mfma_f32_16x16x32_bf16 v[48:51], a[96:99], a[192:195], v[4:7]\n\t"
    "ds_read_b128 a[124:127], v15 offset:12288\n\t"
    "v_cvt_pk_bf16_f32 v67, v58, v59\n\t"
    "v_mfma_f32_16x16x32_bf16 v[48:51], a[100:103], a[196:199], v[48:51]\n\t"
    "v_exp_f32 v60, v60\n\t"
    "v_mfma_f32_16x16x32_bf16 v[48:51], a[104:107], a[200:203], v[48:51]\n\t"
    "ds_read_b64_tr_b16 a[128:129], v18 offset:32768\n\t"
    "ds_read_b64_tr_b16 a[130:131], v18 offset:34816\n\t"
    "v_mfma_f32_16x16x32_bf16 v[48:51], a[108:111], a[204:207], v[48:51]\n\t"
    "buffer_load_dwordx4 v22, s[40:43], s78 offen offset:2048 nt lds\n\t"
    "v_mfma_f32_16x16x32_bf16 v[176:179], a[144:147], v[116:119], v[176:179]\n\t"
    "v_exp_f32 v61, v61\n\t"
    "v_mfma_f32_16x16x32_bf16 v[176:179], a[148:151], v[128:131], v[176:179]\n\t"
    "v_exp_f32 v62, v62\n\t"
    "v_mfma_f32_16x16x32_bf16 v[176:179], a[152:155], v[140:143], v[176:179]\n\t"
    "v_exp_f32 v63, v63\n\t"
    "v_mfma_f32_16x16x32_bf16 v[176:179], a[156:159], v[152:155], v[176:179]\n\t"
    "v_cvt_pk_bf16_f32 v70, v60, v61\n\t"
    "v_cvt_pk_bf16_f32 v71, v62, v63\n\t"
    "v_mfma_f32_16x16x32_bf16 a[160:163], a[172:175], v[112:115], a[160:163]\n\t"
    "ds_read_b64_tr_b16 a[132:133], v18 offset:36864\n\t"
    "ds_read_b64_tr_b16 a[134:135], v18 offset:38912\n\t"
    "v_mfma_f32_16x16x32_bf16 a[160:163], a[172:175], v[124:127], a[160:163]\n\t"
    "v_exp_f32 v40, v40\n\t"
    "v_mfma_f32_16x16x32_bf16 a[160:163], a[172:175], v[136:139], a[160:163]\n\t"
    "v_exp_f32 v41, v41\n\t"
    "v_mfma_f32_16x16x32_bf16 a[160:163], a[172:175], v[148:151], a[160:163]\n\t"
    "v_exp_f32 v42, v42\n\t"
    "v_mfma_f32_16x16x32_bf16 v[52:55], a[96:99], a[208:211], v[8:11]\n\t"
    "buffer_load_dwordx4 v23, s[40:43], s78 offen offset:3072 nt lds\n\t"
    "v_mfma_f32_16x16x32_bf16 v[52:55], a[100:103], a[212:215], v[52:55]\n\t"
    "ds_read_b64_tr_b16 a[136:137], v18 offset:40960\n\t"
    "v_cvt_pk_bf16_f32 v74, v40, v41\n\t"
    "v_mfma_f32_16x16x32_bf16 v[52:55], a[104:107], a[216:219], v[52:55]\n\t"
    "v_exp_f32 v43, v43\n\t"
    "v_mfma_f32_16x16x32_bf16 v[52:55], a[108:111], a[220:223], v[52:55]\n\t"
    "v_cvt_pk_bf16_f32 v75, v42, v43\n\t"
    "v_mfma_f32_16x16x32_bf16 v[56:59], a[228:231], a[60:63], 0\n\t"
    "ds_read_b64_tr_b16 a[138:139], v18 offset:43008\n\t"
    "ds_read_b64_tr_b16 a[140:141], v18 offset:45056\n\t"
    "v_mfma_f32_16x16x32_bf16 v[56:59], a[232:235], a[60:63], v[56:59]\n\t"
    "v_exp_f32 v44, v44\n\t"
    "v_mfma_f32_16x16x32_bf16 v[60:63], a[236:239], a[60:63], 0\n\t"
    "v_exp_f32 v45, v45\n\t"
    "v_mfma_f32_16x16x32_bf16 v[60:63], a[240:243], a[60:63], v[60:63]\n\t"
    "v_exp_f32 v46, v46\n\t"
    "v_mfma_f32_16x16x32_bf16 v[180:183], a[144:147], v[120:123], v[180:183]\n\t"
    "s_add_u32 s79, s61, 65664\n\t"
    "v_exp_f32 v47, v47\n\t"
    "v_mfma_f32_16x16x32_bf16 v[180:183], a[148:151], v[132:135], v[180:183]\n\t"
    "s_add_u32 m0, s77, 16384\n\t"
    "v_cvt_pk_bf16_f32 v76, v44, v45\n\t"
    "v_cvt_pk_bf16_f32 v77, v46, v47\n\t"
    "v_mfma_f32_16x16x32_bf16 v[180:183], a[152:155], v[144:147], v[180:183]\n\t"
    "ds_read_b64_tr_b16 a[142:143], v18 offset:47104\n\t"
    "v_cvt_pk_bf16_f32 v56, v56, v57\n\t"
    "v_mfma_f32_16x16x32_bf16 v[180:183], a[156:159], v[156:159], v[180:183]\n\t"
    "s_waitcnt vmcnt(12) lgkmcnt(3)\n\t"
    "buffer_load_dwordx4 v24, s[44:47], s79 offen offset:0 nt lds\n\t"
    "v_mfma_f32_16x16x32_bf16 v[40:43], a[112:115], a[176:179], v[0:3]\n\t"
    "ds_read_b128 a[96:99], v12 offset:16384\n\t"
    "v_cvt_pk_bf16_f32 v58, v58, v59\n\t"
    "v_mfma_f32_16x16x32_bf16 v[40:43], a[116:119], a[180:183], v[40:43]\n\t"
    "ds_read_b128 a[100:103], v13 offset:16384\n\t"
    "v_accvgpr_write_b32 a60, v56\n\t"
    "v_mfma_f32_16x16x32_bf16 v[40:43], a[120:123], a[184:187], v[40:43]\n\t"
    "ds_read_b128 a[64:67], v28 offset:0\n\t"
    "v_accvgpr_write_b32 a61, v58\n\t"
    "v_mfma_f32_16x16x32_bf16 v[40:43], a[124:127], a[188:191], v[40:43]\n\t"
    "ds_read_b128 a[104:107], v14 offset:16384\n\t"
    "v_cvt_pk_bf16_f32 v60, v60, v61\n\t"
    "v_mfma_f32_16x16x32_bf16 v[184:187], a[128:131], v[112:115], v[184:187]\n\t"
    "ds_read_b128 a[68:71], v29 offset:0\n\t"
    "v_cvt_pk_bf16_f32 v62, v62, v63\n\t"
    "v_mfma_f32_16x16x32_bf16 v[184:187], a[132:135], v[124:127], v[184:187]\n\t"
    "s_waitcnt lgkmcnt(6)\n\t"
    "ds_read_b128 a[108:111], v15 offset:16384\n\t"
    "v_accvgpr_write_b32 a62, v60\n\t"
    "v_mfma_f32_16x16x32_bf16 v[184:187], a[136:139], v[136:139], v[184:187]\n\t"
    "s_waitcnt lgkmcnt(6)\n\t"
    "buffer_load_dwordx4 v25, s[44:47], s79 offen offset:1024 nt lds\n\t"
    "v_mfma_f32_16x16x32_bf16 v[184:187], a[140:143], v[148:151], v[184:187]\n\t"
    "ds_read_b64_tr_b16 a[144:145], v19 offset:32768\n\t"
    "v_accvgpr_write_b32 a63, v62\n\t"
    "v_mfma_f32_16x16x32_bf16 v[44:47], a[112:115], a[192:195], v[4:7]\n\t"
    "v_exp_f32 v48, v48\n\t"
    "v_mfma_f32_16x16x32_bf16 v[44:47], a[116:119], a[196:199], v[44:47]\n\t"
    "ds_read_b64_tr_b16 a[146:147], v19 offset:34816\n\t"
    "ds_read_b64_tr_b16 a[148:149], v19 offset:36864\n\t"
    "v_mfma_f32_16x16x32_bf16 v[44:47], a[120:123], a[200:203], v[44:47]\n\t"
    "v_exp_f32 v49, v49\n\t"
    "v_mfma_f32_16x16x32_bf16 v[44:47], a[124:127], a[204:207], v[44:47]\n\t"
    "v_exp_f32 v50, v50\n\t"
    "v_mfma_f32_16x16x32_bf16 v[188:191], a[128:131], v[116:119], v[188:191]\n\t"
    "v_exp_f32 v51, v51\n\t"
    "v_mfma_f32_16x16x32_bf16 v[188:191], a[132:135], v[128:131], v[188:191]\n\t"
    "ds_read_b64_tr_b16 a[150:151], v19 offset:38912\n\t"
    "v_cvt_pk_bf16_f32 v80, v48, v49\n\t"
    "v_mfma_f32_16x16x32_bf16 v[188:191], a[136:139], v[140:143], v[188:191]\n\t"
    "ds_read_b128 a[72:75], v30 offset:0\n\t"
    "v_cvt_pk_bf16_f32 v81, v50, v51\n\t"
    "v_mfma_f32_16x16x32_bf16 v[188:191], a[140:143], v[152:155], v[188:191]\n\t"
    "ds_read_b64_tr_b16 a[152:153], v19 offset:40960\n\t"
    "ds_read_b128 a[76:79], v31 offset:0\n\t"
    "v_mfma_f32_16x16x32_bf16 v[48:51], a[112:115], a[208:211], v[8:11]\n\t"
    "buffer_load_dwordx4 v26, s[44:47], s79 offen offset:2048 nt lds\n\t"
    "v_mfma_f32_16x16x32_bf16 v[48:51], a[116:119], a[212:215], v[48:51]\n\t"
    "ds_read_b64_tr_b16 a[154:155], v19 offset:43008\n\t"
    "ds_read_b64_tr_b16 a[156:157], v19 offset:45056\n\t"
    "v_mfma_f32_16x16x32_bf16 v[48:51], a[120:123], a[216:219], v[48:51]\n\t"
    "v_exp_f32 v52, v52\n\t"
    "v_mfma_f32_16x16x32_bf16 v[48:51], a[124:127], a[220:223], v[48:51]\n\t"
    "v_exp_f32 v53, v53\n\t"
    "v_mfma_f32_16x16x32_bf16 v[192:195], a[128:131], v[120:123], v[192:195]\n\t"
    "v_exp_f32 v54, v54\n\t"
    "v_mfma_f32_16x16x32_bf16 v[192:195], a[132:135], v[132:135], v[192:195]\n\t"
    "ds_read_b64_tr_b16 a[158:159], v19 offset:47104\n\t"
    "v_cvt_pk_bf16_f32 v84, v52, v53\n\t"
    "v_mfma_f32_16x16x32_bf16 v[192:195], a[136:139], v[144:147], v[192:195]\n\t"
    "v_exp_f32 v55, v55\n\t"
    "v_mfma_f32_16x16x32_bf16 v[192:195], a[140:143], v[156:159], v[192:195]\n\t"
    "s_waitcnt lgkmcnt(3)\n\t"
    "v_cvt_pk_bf16_f32 v85, v54, v55\n\t"
    "v_mfma_f32_16x16x32_bf16 v[52:55], a[96:99], a[176:179], v[0:3]\n\t"
    "buffer_load_dwordx4 v27, s[44:47], s79 offen offset:3072 nt lds\n\t"
    "v_mfma_f32_16x16x32_bf16 v[52:55], a[100:103], a[180:183], v[52:55]\n\t"
    "v_exp_f32 v40, v40\n\t"
    "v_mfma_f32_16x16x32_bf16 v[52:55], a[104:107], a[184:187], v[52:55]\n\t"
    "v_exp_f32 v41, v41\n\t"
    "v_mfma_f32_16x16x32_bf16 v[52:55], a[108:111], a[188:191], v[52:55]\n\t"
    "v_exp_f32 v42, v42\n\t"
    "v_mfma_f32_16x16x32_bf16 v[196:199], a[144:147], v[112:115], v[196:199]\n\t"
    "v_exp_f32 v43, v43\n\t"
    "v_mfma_f32_16x16x32_bf16 v[196:199], a[148:151], v[124:127], v[196:199]\n\t"
    "s_waitcnt lgkmcnt(0)\n\t"
    "v_cvt_pk_bf16_f32 v78, v40, v41\n\t"
    "v_cvt_pk_bf16_f32 v79, v42, v43\n\t"
    "v_mfma_f32_16x16x32_bf16 v[196:199], a[152:155], v[136:139], v[196:199]\n\t"
    "s_waitcnt vmcnt(20) lgkmcnt(0)\n\t"
    "v_exp_f32 v44, v44\n\t"
    "v_mfma_f32_16x16x32_bf16 v[196:199], a[156:159], v[148:151], v[196:199]\n\t"
    "s_barrier\n\t"
    "ds_read_b128 a[112:115], v12 offset:20480\n\t"
    "v_mfma_f32_16x16x32_bf16 v[56:59], a[96:99], a[192:195], v[4:7]\n\t"
    "s_mov_b32 m0, s85\n\t"
    "ds_read_b128 a[116:119], v13 offset:20480\n\t"
    "v_mfma_f32_16x16x32_bf16 v[56:59], a[100:103], a[196:199], v[56:59]\n\t"
    "v_exp_f32 v45, v45\n\t"
    "v_mfma_f32_16x16x32_bf16 v[56:59], a[104:107], a[200:203], v[56:59]\n\t"
    "ds_read_b128 a[120:123], v14 offset:20480\n\t"
    "v_cvt_pk_bf16_f32 v82, v44, v45\n\t"
    "v_mfma_f32_16x16x32_bf16 v[56:59], a[108:111], a[204:207], v[56:59]\n\t"
    "buffer_load_dwordx4 v20, s[36:39], s69 offen offset:0 nt lds\n\t"
    "v_mfma_f32_16x16x32_bf16 v[200:203], a[144:147], v[116:119], v[200:203]\n\t"
    "ds_read_b128 a[124:127], v15 offset:20480\n\t"
    "ds_read_b64_tr_b16 a[128:129], v16 offset:49152\n\t"
    "v_mfma_f32_16x16x32_bf16 v[200:203], a[148:151], v[128:131], v[200:203]\n\t"
    "v_exp_f32 v46, v46\n\t"
    "v_mfma_f32_16x16x32_bf16 v[200:203], a[152:155], v[140:143], v[200:203]\n\t"
    "v_exp_f32 v47, v47\n\t"
    "v_mfma_f32_16x16x32_bf16 v[200:203], a[156:159], v[152:155], v[200:203]\n\t"
    "ds_read_b64_tr_b16 a[130:131], v16 offset:51200\n\t"
    "v_cvt_pk_bf16_f32 v83, v46, v47\n\t"
    "v_mfma_f32_16x16x32_bf16 v[60:63], a[96:99], a[208:211], v[8:11]\n\t"
    "v_exp_f32 v48, v48\n\t"
    "v_mfma_f32_16x16x32_bf16 v[60:63], a[100:103], a[212:215], v[60:63]\n\t"
    "ds_read_b64_tr_b16 a[132:133], v16 offset:53248\n\t"
    "ds_read_b64_tr_b16 a[134:135], v16 offset:55296\n\t"
    "v_mfma_f32_16x16x32_bf16 v[60:63], a[104:107], a[216:219], v[60:63]\n\t"
    "v_exp_f32 v49, v49\n\t"
    "v_mfma_f32_16x16x32_bf16 v[60:63], a[108:111], a[220:223], v[60:63]\n\t"
    "v_exp_f32 v50, v50\n\t"
    "v_mfma_f32_16x16x32_bf16 v[40:43], a[228:231], a[64:67], 0\n\t"
    "ds_read_b64_tr_b16 a[136:137], v16 offset:57344\n\t"
    "v_cvt_pk_bf16_f32 v86, v48, v49\n\t"
    "v_mfma_f32_16x16x32_bf16 v[40:43], a[232:235], a[64:67], v[40:43]\n\t"
    "v_exp_f32 v51, v51\n\t"
    "v_mfma_f32_16x16x32_bf16 v[44:47], a[236:239], a[64:67], 0\n\t"
    "ds_read_b64_tr_b16 a[138:139], v16 offset:59392\n\t"
    "v_cvt_pk_bf16_f32 v87, v50, v51\n\t"
    "v_mfma_f32_16x16x32_bf16 v[44:47], a[240:243], a[64:67], v[44:47]\n\t"
    "buffer_load_dwordx4 v21, s[36:39], s69 offen offset:1024 nt lds\n\t"
    "v_mfma_f32_16x16x32_bf16 v[204:207], a[144:147], v[120:123], v[204:207]\n\t"
    "ds_read_b64_tr_b16 a[140:141], v16 offset:61440\n\t"
    "ds_read_b64_tr_b16 a[142:143], v16 offset:63488\n\t"
    "v_mfma_f32_16x16x32_bf16 v[204:207], a[148:151], v[132:135], v[204:207]\n\t"
    "v_exp_f32 v52, v52\n\t"
    "v_mfma_f32_16x16x32_bf16 v[204:207], a[152:155], v[144:147], v[204:207]\n\t"
    "v_exp_f32 v53, v53\n\t"
    "v_mfma_f32_16x16x32_bf16 v[204:207], a[156:159], v[156:159], v[204:207]\n\t"
    "s_waitcnt lgkmcnt(4)\n\t"
    "v_exp_f32 v54, v54\n\t"
    "v_mfma_f32_16x16x32_bf16 v[48:51], a[112:115], a[176:179], v[0:3]\n\t"
    "ds_read_b128 a[96:99], v12 offset:24576\n\t"
    "v_cvt_pk_bf16_f32 v88, v52, v53\n\t"
    "v_mfma_f32_16x16x32_bf16 v[48:51], a[116:119], a[180:183], v[48:51]\n\t"
    "v_exp_f32 v55, v55\n\t"
    "v_mfma_f32_16x16x32_bf16 v[48:51], a[120:123], a[184:187], v[48:51]\n\t"
    "ds_read_b128 a[100:103], v13 offset:24576\n\t"
    "v_cvt_pk_bf16_f32 v89, v54, v55\n\t"
    "v_mfma_f32_16x16x32_bf16 v[48:51], a[124:127], a[188:191], v[48:51]\n\t"
    "buffer_load_dwordx4 v22, s[36:39], s69 offen offset:2048 nt lds\n\t"
    "v_mfma_f32_16x16x32_bf16 v[208:211], a[128:131], v[112:115], v[208:211]\n\t"
    "ds_read_b128 a[104:107], v14 offset:24576\n\t"
    "v_cvt_pk_bf16_f32 v40, v40, v41\n\t"
    "v_mfma_f32_16x16x32_bf16 v[208:211], a[132:135], v[124:127], v[208:211]\n\t"
    "s_waitcnt lgkmcnt(3)\n\t"
    "v_exp_f32 v56, v56\n\t"
    "v_mfma_f32_16x16x32_bf16 v[208:211], a[136:139], v[136:139], v[208:211]\n\t"
    "v_exp_f32 v57, v57\n\t"
    "v_mfma_f32_16x16x32_bf16 v[208:211], a[140:143], v[148:151], v[208:211]\n\t"
    "ds_read_b128 a[108:111], v15 offset:24576\n\t"
    "v_cvt_pk_bf16_f32 v92, v56, v57\n\t"
    "v_mfma_f32_16x16x32_bf16 v[52:55], a[112:115], a[192:195], v[4:7]\n\t"
    "v_exp_f32 v58, v58\n\t"
    "v_mfma_f32_16x16x32_bf16 v[52:55], a[116:119], a[196:199], v[52:55]\n\t"
    "ds_read_b64_tr_b16 a[144:145], v17 offset:49152\n\t"
    "v_cvt_pk_bf16_f32 v42, v42, v43\n\t"
    "v_mfma_f32_16x16x32_bf16 v[52:55], a[120:123], a[200:203], v[52:55]\n\t"
    "v_exp_f32 v59, v59\n\t"
    "v_mfma_f32_16x16x32_bf16 v[52:55], a[124:127], a[204:207], v[52:55]\n\t"
    "ds_read_b64_tr_b16 a[146:147], v17 offset:51200\n\t"
    "v_cvt_pk_bf16_f32 v93, v58, v59\n\t"
    "v_mfma_f32_16x16x32_bf16 v[212:215], a[128:131], v[116:119], v[212:215]\n\t"
    "buffer_load_dwordx4 v23, s[36:39], s69 offen offset:3072 nt lds\n\t"
    "v_mfma_f32_16x16x32_bf16 v[212:215], a[132:135], v[128:131], v[212:215]\n\t"
    "v_exp_f32 v60, v60\n\t"
    "v_mfma_f32_16x16x32_bf16 v[212:215], a[136:139], v[140:143], v[212:215]\n\t"
    "ds_read_b64_tr_b16 a[148:149], v17 offset:53248\n\t"
    "v_accvgpr_write_b32 a64, v40\n\t"
    "v_mfma_f32_16x16x32_bf16 v[212:215], a[140:143], v[152:155], v[212:215]\n\t"
    "v_exp_f32 v61, v61\n\t"
    "v_mfma_f32_16x16x32_bf16 a[164:167], a[172:175], v[116:119], a[164:167]\n\t"
    "ds_read_b64_tr_b16 a[150:151], v17 offset:55296\n\t"
    "v_cvt_pk_bf16_f32 v96, v60, v61\n\t"
    "v_mfma_f32_16x16x32_bf16 a[164:167], a[172:175], v[128:131], a[164:167]\n\t"
    "v_exp_f32 v62, v62\n\t"
    "v_mfma_f32_16x16x32_bf16 a[164:167], a[172:175], v[140:143], a[164:167]\n\t"
    "ds_read_b64_tr_b16 a[152:153], v17 offset:57344\n\t"
    "v_accvgpr_write_b32 a65, v42\n\t"
    "v_mfma_f32_16x16x32_bf16 a[164:167], a[172:175], v[152:155], a[164:167]\n\t"
    "v_exp_f32 v63, v63\n\t"
    "v_mfma_f32_16x16x32_bf16 v[56:59], a[112:115], a[208:211], v[8:11]\n\t"
    "s_add_u32 s78, s56, 4096\n\t"
    "v_cvt_pk_bf16_f32 v97, v62, v63\n\t"
    "v_cvt_pk_bf16_f32 v44, v44, v45\n\t"
    "v_mfma_f32_16x16x32_bf16 v[56:59], a[116:119], a[212:215], v[56:59]\n\t"
    "s_mov_b32 m0, s73\n\t"
    "ds_read_b64_tr_b16 a[154:155], v17 offset:59392\n\t"
    "v_cvt_pk_bf16_f32 v46, v46, v47\n\t"
    "v_mfma_f32_16x16x32_bf16 v[56:59], a[120:123], a[216:219], v[56:59]\n\t"
    "buffer_load_dwordx4 v20, s[40:43], s78 offen offset:0 nt lds\n\t"
    "v_mfma_f32_16x16x32_bf16 v[56:59], a[124:127], a[220:223], v[56:59]\n\t"
    "ds_read_b64_tr_b16 a[156:157], v17 offset:61440\n\t"
    "v_accvgpr_write_b32 a66, v44\n\t"
    "v_mfma_f32_16x16x32_bf16 v[216:219], a[128:131], v[120:123], v[216:219]\n\t"
    "v_accvgpr_write_b32 a67, v46\n\t"
    "v_mfma_f32_16x16x32_bf16 v[216:219], a[132:135], v[132:135], v[216:219]\n\t"
    "v_exp_f32 v48, v48\n\t"
    "v_mfma_f32_16x16x32_bf16 v[216:219], a[136:139], v[144:147], v[216:219]\n\t"
    "ds_read_b64_tr_b16 a[158:159], v17 offset:63488\n\t"
    "v_mfma_f32_16x16x32_bf16 v[216:219], a[140:143], v[156:159], v[216:219]\n\t"
    "s_waitcnt lgkmcnt(3)\n\t"
    "v_exp_f32 v49, v49\n\t"
    "v_mfma_f32_16x16x32_bf16 v[60:63], a[96:99], a[176:179], v[0:3]\n\t"
    "ds_read_b128 a[112:115], v12 offset:28672\n\t"
    "v_cvt_pk_bf16_f32 v90, v48, v49\n\t"
    "v_mfma_f32_16x16x32_bf16 v[60:63], a[100:103], a[180:183], v[60:63]\n\t"
    "v_exp_f32 v50, v50\n\t"
    "v_mfma_f32_16x16x32_bf16 v[60:63], a[104:107], a[184:187], v[60:63]\n\t"
    "ds_read_b128 a[116:119], v13 offset:28672\n\t"
    "v_mfma_f32_16x16x32_bf16 v[60:63], a[108:111], a[188:191], v[60:63]\n\t"
    "v_exp_f32 v51, v51\n\t"
    "v_mfma_f32_16x16x32_bf16 v[220:223], a[144:147], v[112:115], v[220:223]\n\t"
    "ds_read_b128 a[120:123], v14 offset:28672\n\t"
    "v_cvt_pk_bf16_f32 v91, v50, v51\n\t"
    "v_mfma_f32_16x16x32_bf16 v[220:223], a[148:151], v[124:127], v[220:223]\n\t"
    "s_waitcnt lgkmcnt(4)\n\t"
    "buffer_load_dwordx4 v21, s[40:43], s78 offen offset:1024 nt lds\n\t"
    "v_mfma_f32_16x16x32_bf16 v[220:223], a[152:155], v[136:139], v[220:223]\n\t"
    "s_waitcnt lgkmcnt(3)\n\t"
    "v_exp_f32 v52, v52\n\t"
    "v_mfma_f32_16x16x32_bf16 v[220:223], a[156:159], v[148:151], v[220:223]\n\t"
    "ds_read_b128 a[124:127], v15 offset:28672\n\t"
    "ds_read_b64_tr_b16 a[128:129], v18 offset:49152\n\t"
    "v_mfma_f32_16x16x32_bf16 v[40:43], a[96:99], a[192:195], v[4:7]\n\t"
    "v_exp_f32 v53, v53\n\t"
    "v_mfma_f32_16x16x32_bf16 v[40:43], a[100:103], a[196:199], v[40:43]\n\t"
    "v_exp_f32 v54, v54\n\t"
    "v_mfma_f32_16x16x32_bf16 v[40:43], a[104:107], a[200:203], v[40:43]\n\t"
    "v_exp_f32 v55, v55\n\t"
    "v_mfma_f32_16x16x32_bf16 v[40:43], a[108:111], a[204:207], v[40:43]\n\t"
    "ds_read_b64_tr_b16 a[130:131], v18 offset:51200\n\t"
    "v_cvt_pk_bf16_f32 v94, v52, v53\n\t"
    "v_mfma_f32_16x16x32_bf16 v[224:227], a[144:147], v[116:119], v[224:227]\n\t"
    "v_cvt_pk_bf16_f32 v95, v54, v55\n\t"
    "v_mfma_f32_16x16x32_bf16 v[224:227], a[148:151], v[128:131], v[224:227]\n\t"
    "v_exp_f32 v56, v56\n\t"
    "v_mfma_f32_16x16x32_bf16 v[224:227], a[152:155], v[140:143], v[224:227]\n\t"
    "ds_read_b64_tr_b16 a[132:133], v18 offset:53248\n\t"
    "ds_read_b64_tr_b16 a[134:135], v18 offset:55296\n\t"
    "v_mfma_f32_16x16x32_bf16 v[224:227], a[156:159], v[152:155], v[224:227]\n\t"
    "buffer_load_dwordx4 v22, s[40:43], s78 offen offset:2048 nt lds\n\t"
    "v_mfma_f32_16x16x32_bf16 v[44:47], a[96:99], a[208:211], v[8:11]\n\t"
    "v_exp_f32 v57, v57\n\t"
    "v_mfma_f32_16x16x32_bf16 v[44:47], a[100:103], a[212:215], v[44:47]\n\t"
    "v_exp_f32 v58, v58\n\t"
    "v_mfma_f32_16x16x32_bf16 v[44:47], a[104:107], a[216:219], v[44:47]\n\t"
    "ds_read_b64_tr_b16 a[136:137], v18 offset:57344\n\t"
    "v_cvt_pk_bf16_f32 v98, v56, v57\n\t"
    "v_mfma_f32_16x16x32_bf16 v[44:47], a[108:111], a[220:223], v[44:47]\n\t"
    "v_exp_f32 v59, v59\n\t"
    "v_mfma_f32_16x16x32_bf16 v[48:51], a[228:231], a[68:71], 0\n\t"
    "v_cvt_pk_bf16_f32 v99, v58, v59\n\t"
    "v_mfma_f32_16x16x32_bf16 v[48:51], a[232:235], a[68:71], v[48:51]\n\t"
    "ds_read_b64_tr_b16 a[138:139], v18 offset:59392\n\t"
    "ds_read_b64_tr_b16 a[140:141], v18 offset:61440\n\t"
    "v_mfma_f32_16x16x32_bf16 v[52:55], a[236:239], a[68:71], 0\n\t"
    "v_exp_f32 v60, v60\n\t"
    "v_mfma_f32_16x16x32_bf16 v[52:55], a[240:243], a[68:71], v[52:55]\n\t"
    "v_exp_f32 v61, v61\n\t"
    "v_mfma_f32_16x16x32_bf16 v[228:231], a[144:147], v[120:123], v[228:231]\n\t"
    "buffer_load_dwordx4 v23, s[40:43], s78 offen offset:3072 nt lds\n\t"
    "v_mfma_f32_16x16x32_bf16 v[228:231], a[148:151], v[132:135], v[228:231]\n\t"
    "v_exp_f32 v62, v62\n\t"
    "v_mfma_f32_16x16x32_bf16 v[228:231], a[152:155], v[144:147], v[228:231]\n\t"
    "ds_read_b64_tr_b16 a[142:143], v18 offset:63488\n\t"
    "v_cvt_pk_bf16_f32 v100, v60, v61\n\t"
    "v_mfma_f32_16x16x32_bf16 v[228:231], a[156:159], v[156:159], v[228:231]\n\t"
    "s_waitcnt lgkmcnt(3)\n\t"
    "v_exp_f32 v63, v63\n\t"
    "v_mfma_f32_16x16x32_bf16 v[56:59], a[112:115], a[176:179], v[0:3]\n\t"
    "ds_read_b128 a[96:99], v12 offset:32768\n\t"
    "v_cvt_pk_bf16_f32 v101, v62, v63\n\t"
    "v_mfma_f32_16x16x32_bf16 v[56:59], a[116:119], a[180:183], v[56:59]\n\t"
    "v_exp_f32 v40, v40\n\t"
    "v_mfma_f32_16x16x32_bf16 v[56:59], a[120:123], a[184:187], v[56:59]\n\t"
    "ds_read_b128 a[100:103], v13 offset:32768\n\t"
    "v_cvt_pk_bf16_f32 v48, v48, v49\n\t"
    "v_mfma_f32_16x16x32_bf16 v[56:59], a[124:127], a[188:191], v[56:59]\n\t"
    "v_exp_f32 v41, v41\n\t"
    "v_mfma_f32_16x16x32_bf16 v[232:235], a[128:131], v[112:115], v[232:235]\n\t"
    "s_add_u32 s79, s61, 98304\n\t"
    "ds_read_b128 a[104:107], v14 offset:32768\n\t"
    "v_cvt_pk_bf16_f32 v104, v40, v41\n\t"
    "v_mfma_f32_16x16x32_bf16 v[232:235], a[132:135], v[124:127], v[232:235]\n\t"
    "s_waitcnt lgkmcnt(4)\n\t"
    "s_add_u32 m0, s77, 32768\n\t"
    "v_exp_f32 v42, v42\n\t"
    "v_mfma_f32_16x16x32_bf16 v[232:235], a[136:139], v[136:139], v[232:235]\n\t"
    "s_waitcnt lgkmcnt(3)\n\t"
    "ds_read_b128 a[108:111], v15 offset:32768\n\t"
    "v_cvt_pk_bf16_f32 v50, v50, v51\n\t"
    "v_mfma_f32_16x16x32_bf16 v[232:235], a[140:143], v[148:151], v[232:235]\n\t"
    "buffer_load_dwordx4 v24, s[44:47], s79 offen offset:0 nt lds\n\t"
    "v_mfma_f32_16x16x32_bf16 v[60:63], a[112:115], a[192:195], v[4:7]\n\t"
    "v_exp_f32 v43, v43\n\t"
    "v_mfma_f32_16x16x32_bf16 v[60:63], a[116:119], a[196:199], v[60:63]\n\t"
    "ds_read_b64_tr_b16 a[144:145], v19 offset:49152\n\t"
    "v_cvt_pk_bf16_f32 v105, v42, v43\n\t"
    "v_mfma_f32_16x16x32_bf16 v[60:63], a[120:123], a[200:203], v[60:63]\n\t"
    "v_exp_f32 v44, v44\n\t"
    "v_mfma_f32_16x16x32_bf16 v[60:63], a[124:127], a[204:207], v[60:63]\n\t"
    "ds_read_b64_tr_b16 a[146:147], v19 offset:51200\n\t"
    "v_accvgpr_write_b32 a68, v48\n\t"
    "v_mfma_f32_16x16x32_bf16 v[236:239], a[128:131], v[116:119], v[236:239]\n\t"
    "v_exp_f32 v45, v45\n\t"
    "v_mfma_f32_16x16x32_bf16 v[236:239], a[132:135], v[128:131], v[236:239]\n\t"
    "ds_read_b64_tr_b16 a[148:149], v19 offset:53248\n\t"
    "v_cvt_pk_bf16_f32 v108, v44, v45\n\t"
    "v_mfma_f32_16x16x32_bf16 v[236:239], a[136:139], v[140:143], v[236:239]\n\t"
    "buffer_load_dwordx4 v25, s[44:47], s79 offen offset:1024 nt lds\n\t"
    "v_mfma_f32_16x16x32_bf16 v[236:239], a[140:143], v[152:155], v[236:239]\n\t"
    "ds_read_b64_tr_b16 a[150:151], v19 offset:55296\n\t"
    "v_accvgpr_write_b32 a69, v50\n\t"
    "v_mfma_f32_16x16x32_bf16 a[168:171], a[172:175], v[120:123], a[168:171]\n\t"
    "v_exp_f32 v46, v46\n\t"
    "v_mfma_f32_16x16x32_bf16 a[168:171], a[172:175], v[132:135], a[168:171]\n\t"
    "v_exp_f32 v47, v47\n\t"
    "v_mfma_f32_16x16x32_bf16 a[168:171], a[172:175], v[144:147], a[168:171]\n\t"
    "ds_read_b64_tr_b16 a[152:153], v19 offset:57344\n\t"
    "v_cvt_pk_bf16_f32 v109, v46, v47\n\t"
    "v_mfma_f32_16x16x32_bf16 a[168:171], a[172:175], v[156:159], a[168:171]\n\t"
    "v_cvt_pk_bf16_f32 v52, v52, v53\n\t"
    "v_cvt_pk_bf16_f32 v54, v54, v55\n\t"
    "v_mfma_f32_16x16x32_bf16 v[40:43], a[112:115], a[208:211], v[8:11]\n\t"
    "ds_read_b64_tr_b16 a[154:155], v19 offset:59392\n\t"
    "v_accvgpr_write_b32 a70, v52\n\t"
    "v_mfma_f32_16x16x32_bf16 v[40:43], a[116:119], a[212:215], v[40:43]\n\t"
    "buffer_load_dwordx4 v26, s[44:47], s79 offen offset:2048 nt lds\n\t"
    "v_mfma_f32_16x16x32_bf16 v[40:43], a[120:123], a[216:219], v[40:43]\n\t"
    "ds_read_b64_tr_b16 a[156:157], v19 offset:61440\n\t"
    "v_accvgpr_write_b32 a71, v54\n\t"
    "v_mfma_f32_16x16x32_bf16 v[40:43], a[124:127], a[220:223], v[40:43]\n\t"
    "v_exp_f32 v56, v56\n\t"
    "v_mfma_f32_16x16x32_bf16 v[240:243], a[128:131], v[120:123], v[240:243]\n\t"
    "v_exp_f32 v57, v57\n\t"
    "v_mfma_f32_16x16x32_bf16 v[240:243], a[132:135], v[132:135], v[240:243]\n\t"
    "ds_read_b64_tr_b16 a[158:159], v19 offset:63488\n\t"
    "v_cvt_pk_bf16_f32 v102, v56, v57\n\t"
    "v_mfma_f32_16x16x32_bf16 v[240:243], a[136:139], v[144:147], v[240:243]\n\t"
    "v_exp_f32 v58, v58\n\t"
    "v_mfma_f32_16x16x32_bf16 v[240:243], a[140:143], v[156:159], v[240:243]\n\t"
    "s_waitcnt lgkmcnt(3)\n\t"
    "buffer_load_dwordx4 v27, s[44:47], s79 offen offset:3072 nt lds\n\t"
    "v_mfma_f32_16x16x32_bf16 v[44:47], a[96:99], a[176:179], v[0:3]\n\t"
    "v_exp_f32 v59, v59\n\t"
    "v_mfma_f32_16x16x32_bf16 v[44:47], a[100:103], a[180:183], v[44:47]\n\t"
    "v_cvt_pk_bf16_f32 v103, v58, v59\n\t"
    "v_mfma_f32_16x16x32_bf16 v[44:47], a[104:107], a[184:187], v[44:47]\n\t"
    "v_exp_f32 v60, v60\n\t"
    "v_mfma_f32_16x16x32_bf16 v[44:47], a[108:111], a[188:191], v[44:47]\n\t"
    "v_exp_f32 v61, v61\n\t"
    "v_mfma_f32_16x16x32_bf16 v[244:247], a[144:147], v[112:115], v[244:247]\n\t"
    "v_exp_f32 v62, v62\n\t"
    "v_mfma_f32_16x16x32_bf16 v[244:247], a[148:151], v[124:127], v[244:247]\n\t"
    "s_waitcnt lgkmcnt(0)\n\t"
    "v_exp_f32 v63, v63\n\t"
    "v_mfma_f32_16x16x32_bf16 v[244:247], a[152:155], v[136:139], v[244:247]\n\t"
    "s_waitcnt vmcnt(20) lgkmcnt(0)\n\t"
    "v_cvt_pk_bf16_f32 v106, v60, v61\n\t"
    "v_cvt_pk_bf16_f32 v107, v62, v63\n\t"
    "v_mfma_f32_16x16x32_bf16 v[244:247], a[156:159], v[148:151], v[244:247]\n\t"
    "s_barrier\n\t"
    "ds_read_b128 a[112:115], v12 offset:36864\n\t"
    "v_mfma_f32_16x16x32_bf16 v[48:51], a[96:99], a[192:195], v[4:7]\n\t"
    "s_add_u32 s78, s56, 20480\n\t"
    "ds_read_b128 a[116:119], v13 offset:36864\n\t"
    "v_mfma_f32_16x16x32_bf16 v[48:51], a[100:103], a[196:199], v[48:51]\n\t"
    "s_mov_b32 m0, s74\n\t"
    "v_exp_f32 v40, v40\n\t"
    "v_mfma_f32_16x16x32_bf16 v[48:51], a[104:107], a[200:203], v[48:51]\n\t"
    "ds_read_b128 a[120:123], v14 offset:36864\n\t"
    "v_mfma_f32_16x16x32_bf16 v[48:51], a[108:111], a[204:207], v[48:51]\n\t"
    "buffer_load_dwordx4 v20, s[40:43], s78 offen offset:0 nt lds\n\t"
    "v_mfma_f32_16x16x32_bf16 v[248:251], a[144:147], v[116:119], v[248:251]\n\t"
    "ds_read_b128 a[124:127], v15 offset:36864\n\t"
    "ds_read_b64_tr_b16 a[128:129], v16 offset:0\n\t"
    "v_mfma_f32_16x16x32_bf16 v[248:251], a[148:151], v[128:131], v[248:251]\n\t"
    "v_exp_f32 v41, v41\n\t"
    "v_mfma_f32_16x16x32_bf16 v[248:251], a[152:155], v[140:143], v[248:251]\n\t"
    "v_exp_f32 v42, v42\n\t"
    "v_mfma_f32_16x16x32_bf16 v[248:251], a[156:159], v[152:155], v[248:251]\n\t"
    "ds_read_b64_tr_b16 a[130:131], v16 offset:2048\n\t"
    "v_cvt_pk_bf16_f32 v110, v40, v41\n\t"
    "v_mfma_f32_16x16x32_bf16 v[52:55], a[96:99], a[208:211], v[8:11]\n\t"
    "v_exp_f32 v43, v43\n\t"
    "v_mfma_f32_16x16x32_bf16 v[52:55], a[100:103], a[212:215], v[52:55]\n\t"
    "ds_read_b64_tr_b16 a[132:133], v16 offset:4096\n\t"
    "v_cvt_pk_bf16_f32 v111, v42, v43\n\t"
    "v_mfma_f32_16x16x32_bf16 v[52:55], a[104:107], a[216:219], v[52:55]\n\t"
    "v_exp_f32 v44, v44\n\t"
    "v_mfma_f32_16x16x32_bf16 v[52:55], a[108:111], a[220:223], v[52:55]\n\t"
    "ds_read_b64_tr_b16 a[134:135], v16 offset:6144\n\t"
    "ds_read_b64_tr_b16 a[136:137], v16 offset:8192\n\t"
    "v_mfma_f32_16x16x32_bf16 v[56:59], a[228:231], a[72:75], 0\n\t"
    "buffer_load_dwordx4 v21, s[40:43], s78 offen offset:1024 nt lds\n\t"
    "v_mfma_f32_16x16x32_bf16 v[56:59], a[232:235], a[72:75], v[56:59]\n\t"
    "v_exp_f32 v45, v45\n\t"
    "v_mfma_f32_16x16x32_bf16 v[60:63], a[236:239], a[72:75], 0\n\t"
    "ds_read_b64_tr_b16 a[138:139], v16 offset:10240\n\t"
    "v_cvt_pk_bf16_f32 v112, v44, v45\n\t"
    "v_mfma_f32_16x16x32_bf16 v[60:63], a[240:243], a[72:75], v[60:63]\n\t"
    "v_exp_f32 v46, v46\n\t"
    "v_mfma_f32_16x16x32_bf16 v[252:255], a[144:147], v[120:123], v[252:255]\n\t"
    "ds_read_b64_tr_b16 a[140:141], v16 offset:12288\n\t"
    "ds_read_b64_tr_b16 a[142:143], v16 offset:14336\n\t"
    "v_mfma_f32_16x16x32_bf16 v[252:255], a[148:151], v[132:135], v[252:255]\n\t"
    "v_exp_f32 v47, v47\n\t"
    "v_mfma_f32_16x16x32_bf16 v[252:255], a[152:155], v[144:147], v[252:255]\n\t"
    "v_cvt_pk_bf16_f32 v113, v46, v47\n\t"
    "v_cvt_pk_bf16_f32 v56, v56, v57\n\t"
    "v_mfma_f32_16x16x32_bf16 v[252:255], a[156:159], v[156:159], v[252:255]\n\t"
    "s_waitcnt lgkmcnt(3)\n\t"
    "v_exp_f32 v48, v48\n\t"
    "v_mfma_f32_16x16x32_bf16 v[40:43], a[112:115], a[176:179], v[0:3]\n\t"
    "ds_read_b128 a[96:99], v12 offset:40960\n\t"
    "v_cvt_pk_bf16_f32 v58, v58, v59\n\t"
    "v_mfma_f32_16x16x32_bf16 v[40:43], a[116:119], a[180:183], v[40:43]\n\t"
    "v_exp_f32 v49, v49\n\t"
    "v_mfma_f32_16x16x32_bf16 v[40:43], a[120:123], a[184:187], v[40:43]\n\t"
    "ds_read_b128 a[100:103], v13 offset:40960\n\t"
    "v_cvt_pk_bf16_f32 v116, v48, v49\n\t"
    "v_mfma_f32_16x16x32_bf16 v[40:43], a[124:127], a[188:191], v[40:43]\n\t"
    "buffer_load_dwordx4 v22, s[40:43], s78 offen offset:2048 nt lds\n\t"
    "v_mfma_f32_16x16x32_bf16 v[160:163], a[128:131], v[64:67], v[160:163]\n\t"
    "ds_read_b128 a[104:107], v14 offset:40960\n\t"
    "v_accvgpr_write_b32 a72, v56\n\t"
    "v_mfma_f32_16x16x32_bf16 v[160:163], a[132:135], v[76:79], v[160:163]\n\t"
    "s_waitcnt lgkmcnt(3)\n\t"
    "v_exp_f32 v50, v50\n\t"
    "v_mfma_f32_16x16x32_bf16 v[160:163], a[136:139], v[88:91], v[160:163]\n\t"
    "ds_read_b128 a[108:111], v15 offset:40960\n\t"
    "v_accvgpr_write_b32 a73, v58\n\t"
    "v_mfma_f32_16x16x32_bf16 v[160:163], a[140:143], v[100:103], v[160:163]\n\t"
    "v_exp_f32 v51, v51\n\t"
    "v_mfma_f32_16x16x32_bf16 v[44:47], a[112:115], a[192:195], v[4:7]\n\t"
    "ds_read_b64_tr_b16 a[144:145], v17 offset:0\n\t"
    "v_cvt_pk_bf16_f32 v117, v50, v51\n\t"
    "v_mfma_f32_16x16x32_bf16 v[44:47], a[116:119], a[196:199], v[44:47]\n\t"
    "v_exp_f32 v52, v52\n\t"
    "v_mfma_f32_16x16x32_bf16 v[44:47], a[120:123], a[200:203], v[44:47]\n\t"
    "ds_read_b64_tr_b16 a[146:147], v17 offset:2048\n\t"
    "v_cvt_pk_bf16_f32 v60, v60, v61\n\t"
    "v_mfma_f32_16x16x32_bf16 v[44:47], a[124:127], a[204:207], v[44:47]\n\t"
    "v_exp_f32 v53, v53\n\t"
    "v_mfma_f32_16x16x32_bf16 v[164:167], a[128:131], v[68:71], v[164:167]\n\t"
    "ds_read_b64_tr_b16 a[148:149], v17 offset:4096\n\t"
    "v_cvt_pk_bf16_f32 v120, v52, v53\n\t"
    "v_mfma_f32_16x16x32_bf16 v[164:167], a[132:135], v[80:83], v[164:167]\n\t"
    "buffer_load_dwordx4 v23, s[40:43], s78 offen offset:3072 nt lds\n\t"
    "v_mfma_f32_16x16x32_bf16 v[164:167], a[136:139], v[92:95], v[164:167]\n\t"
    "ds_read_b64_tr_b16 a[150:151], v17 offset:6144\n\t"
    "v_cvt_pk_bf16_f32 v62, v62, v63\n\t"
    "v_mfma_f32_16x16x32_bf16 v[164:167], a[140:143], v[104:107], v[164:167]\n\t"
    "v_exp_f32 v54, v54\n\t"
    "v_mfma_f32_16x16x32_bf16 v[48:51], a[112:115], a[208:211], v[8:11]\n\t"
    "ds_read_b64_tr_b16 a[152:153], v17 offset:8192\n\t"
    "v_accvgpr_write_b32 a74, v60\n\t"
    "v_mfma_f32_16x16x32_bf16 v[48:51], a[116:119], a[212:215], v[48:51]\n\t"
    "v_exp_f32 v55, v55\n\t"
    "v_mfma_f32_16x16x32_bf16 v[48:51], a[120:123], a[216:219], v[48:51]\n\t"
    "ds_read_b64_tr_b16 a[154:155], v17 offset:10240\n\t"
    "v_cvt_pk_bf16_f32 v121, v54, v55\n\t"
    "v_mfma_f32_16x16x32_bf16 v[48:51], a[124:127], a[220:223], v[48:51]\n\t"
    "v_accvgpr_write_b32 a75, v62\n\t"
    "v_mfma_f32_16x16x32_bf16 v[168:171], a[128:131], v[72:75], v[168:171]\n\t"
    "s_add_u32 s79, s61, 98432\n\t"
    "ds_read_b64_tr_b16 a[156:157], v17 offset:12288\n\t"
    "ds_read_b64_tr_b16 a[158:159], v17 offset:14336\n\t"
    "v_mfma_f32_16x16x32_bf16 v[168:171], a[132:135], v[84:87], v[168:171]\n\t"
    "s_add_u32 m0, s77, 49152\n\t"
    "v_exp_f32 v40, v40\n\t"
    "v_mfma_f32_16x16x32_bf16 v[168:171], a[136:139], v[96:99], v[168:171]\n\t"
    "buffer_load_dwordx4 v24, s[44:47], s79 offen offset:0 nt lds\n\t"
    "v_mfma_f32_16x16x32_bf16 v[168:171], a[140:143], v[108:111], v[168:171]\n\t"
    "s_waitcnt lgkmcnt(4)\n\t"
    "v_exp_f32 v41, v41\n\t"
    "v_mfma_f32_16x16x32_bf16 v[52:55], a[96:99], a[176:179], v[0:3]\n\t"
    "ds_read_b128 a[112:115], v12 offset:45056\n\t"
    "v_cvt_pk_bf16_f32 v114, v40, v41\n\t"
    "v_mfma_f32_16x16x32_bf16 v[52:55], a[100:103], a[180:183], v[52:55]\n\t"
    "v_exp_f32 v42, v42\n\t"
    "v_mfma_f32_16x16x32_bf16 v[52:55], a[104:107], a[184:187], v[52:55]\n\t"
    "ds_read_b128 a[116:119], v13 offset:45056\n\t"
    "v_mfma_f32_16x16x32_bf16 v[52:55], a[108:111], a[188:191], v[52:55]\n\t"
    "v_exp_f32 v43, v43\n\t"
    "v_mfma_f32_16x16x32_bf16 v[172:175], a[144:147], v[64:67], v[172:175]\n\t"
    "v_cvt_pk_bf16_f32 v115, v42, v43\n\t"
    "v_mfma_f32_16x16x32_bf16 v[172:175], a[148:151], v[76:79], v[172:175]\n\t"
    "s_waitcnt lgkmcnt(2)\n\t"
    "ds_read_b128 a[120:123], v14 offset:45056\n\t"
    "v_mfma_f32_16x16x32_bf16 v[172:175], a[152:155], v[88:91], v[172:175]\n\t"
    "v_exp_f32 v44, v44\n\t"
    "v_mfma_f32_16x16x32_bf16 v[172:175], a[156:159], v[100:103], v[172:175]\n\t"
    "v_exp_f32 v45, v45\n\t"
    "v_mfma_f32_16x16x32_bf16 v[56:59], a[96:99], a[192:195], v[4:7]\n\t"
    "ds_read_b128 a[124:127], v15 offset:45056\n\t"
    "v_cvt_pk_bf16_f32 v118, v44, v45\n\t"
    "v_mfma_f32_16x16x32_bf16 v[56:59], a[100:103], a[196:199], v[56:59]\n\t"
    "buffer_load_dwordx4 v25, s[44:47], s79 offen offset:1024 nt lds\n\t"
    "v_mfma_f32_16x16x32_bf16 v[56:59], a[104:107], a[200:203], v[56:59]\n\t"
    "ds_read_b64_tr_b16 a[128:129], v18 offset:0\n\t"
    "ds_read_b64_tr_b16 a[130:131], v18 offset:2048\n\t"
    "v_mfma_f32_16x16x32_bf16 v[56:59], a[108:111], a[204:207], v[56:59]\n\t"
    "v_exp_f32 v46, v46\n\t"
    "v_mfma_f32_16x16x32_bf16 v[176:179], a[144:147], v[68:71], v[176:179]\n\t"
    "v_exp_f32 v47, v47\n\t"
    "v_mfma_f32_16x16x32_bf16 v[176:179], a[148:151], v[80:83], v[176:179]\n\t"
    "v_cvt_pk_bf16_f32 v119, v46, v47\n\t"
    "v_mfma_f32_16x16x32_bf16 v[176:179], a[152:155], v[92:95], v[176:179]\n\t"
    "v_exp_f32 v48, v48\n\t"
    "v_mfma_f32_16x16x32_bf16 v[176:179], a[156:159], v[104:107], v[176:179]\n\t"
    "v_exp_f32 v49, v49\n\t"
    "v_mfma_f32_16x16x32_bf16 a[160:163], a[172:175], v[64:67], a[160:163]\n\t"
    "ds_read_b64_tr_b16 a[132:133], v18 offset:4096\n\t"
    "v_cvt_pk_bf16_f32 v122, v48, v49\n\t"
    "v_mfma_f32_16x16x32_bf16 a[160:163], a[172:175], v[76:79], a[160:163]\n\t"
    "v_exp_f32 v50, v50\n\t"
    "v_mfma_f32_16x16x32_bf16 a[160:163], a[172:175], v[88:91], a[160:163]\n\t"
    "buffer_load_dwordx4 v26, s[44:47], s79 offen offset:2048 nt lds\n\t"
    "v_mfma_f32_16x16x32_bf16 a[160:163], a[172:175], v[100:103], a[160:163]\n\t"
    "ds_read_b64_tr_b16 a[134:135], v18 offset:6144\n\t"
    "ds_read_b64_tr_b16 a[136:137], v18 offset:8192\n\t"
    "v_mfma_f32_16x16x32_bf16 v[60:63], a[96:99], a[208:211], v[8:11]\n\t"
    "v_exp_f32 v51, v51\n\t"
    "v_mfma_f32_16x16x32_bf16 v[60:63], a[100:103], a[212:215], v[60:63]\n\t"
    "v_cvt_pk_bf16_f32 v123, v50, v51\n\t"
    "v_mfma_f32_16x16x32_bf16 v[60:63], a[104:107], a[216:219], v[60:63]\n\t"
    "v_exp_f32 v52, v52\n\t"
    "v_mfma_f32_16x16x32_bf16 v[60:63], a[108:111], a[220:223], v[60:63]\n\t"
    "v_exp_f32 v53, v53\n\t"
    "v_mfma_f32_16x16x32_bf16 v[40:43], a[228:231], a[76:79], 0\n\t"
    "ds_read_b64_tr_b16 a[138:139], v18 offset:10240\n\t"
    "v_cvt_pk_bf16_f32 v124, v52, v53\n\t"
    "v_mfma_f32_16x16x32_bf16 v[40:43], a[232:235], a[76:79], v[40:43]\n\t"
    "v_exp_f32 v54, v54\n\t"
    "v_mfma_f32_16x16x32_bf16 v[44:47], a[236:239], a[76:79], 0\n\t"
    "v_exp_f32 v55, v55\n\t"
    "v_mfma_f32_16x16x32_bf16 v[44:47], a[240:243], a[76:79], v[44:47]\n\t"
    "ds_read_b64_tr_b16 a[140:141], v18 offset:12288\n\t"
    "v_cvt_pk_bf16_f32 v125, v54, v55\n\t"
    "v_mfma_f32_16x16x32_bf16 v[180:183], a[144:147], v[72:75], v[180:183]\n\t"
    "buffer_load_dwordx4 v27, s[44:47], s79 offen offset:3072 nt lds\n\t"
    "v_mfma_f32_16x16x32_bf16 v[180:183], a[148:151], v[84:87], v[180:183]\n\t"
    "v_exp_f32 v56, v56\n\t"
    "v_mfma_f32_16x16x32_bf16 v[180:183], a[152:155], v[96:99], v[180:183]\n\t"
    "ds_read_b64_tr_b16 a[142:143], v18 offset:14336\n\t"
    "v_cvt_pk_bf16_f32 v40, v40, v41\n\t"
    "v_mfma_f32_16x16x32_bf16 v[180:183], a[156:159], v[108:111], v[180:183]\n\t"
    "s_waitcnt vmcnt(16) lgkmcnt(3)\n\t"
    "v_exp_f32 v57, v57\n\t"
    "v_mfma_f32_16x16x32_bf16 v[48:51], a[112:115], a[176:179], v[0:3]\n\t"
    "ds_read_b128 a[96:99], v12 offset:49152\n\t"
    "v_cvt_pk_bf16_f32 v128, v56, v57\n\t"
    "v_mfma_f32_16x16x32_bf16 v[48:51], a[116:119], a[180:183], v[48:51]\n\t"
    "ds_read_b128 a[100:103], v13 offset:49152\n\t"
    "v_cvt_pk_bf16_f32 v42, v42, v43\n\t"
    "v_mfma_f32_16x16x32_bf16 v[48:51], a[120:123], a[184:187], v[48:51]\n\t"
    "ds_read_b128 a[80:83], v28 offset:0\n\t"
    "v_accvgpr_write_b32 a76, v40\n\t"
    "v_mfma_f32_16x16x32_bf16 v[48:51], a[124:127], a[188:191], v[48:51]\n\t"
    "ds_read_b128 a[104:107], v14 offset:49152\n\t"
    "v_accvgpr_write_b32 a77, v42\n\t"
    "v_mfma_f32_16x16x32_bf16 v[184:187], a[128:131], v[64:67], v[184:187]\n\t"
    "ds_read_b128 a[84:87], v29 offset:0\n\t"
    "v_cvt_pk_bf16_f32 v44, v44, v45\n\t"
    "v_mfma_f32_16x16x32_bf16 v[184:187], a[132:135], v[76:79], v[184:187]\n\t"
    "s_waitcnt lgkmcnt(6)\n\t"
    "ds_read_b128 a[108:111], v15 offset:49152\n\t"
    "v_cvt_pk_bf16_f32 v46, v46, v47\n\t"
    "v_mfma_f32_16x16x32_bf16 v[184:187], a[136:139], v[88:91], v[184:187]\n\t"
    "s_waitcnt lgkmcnt(6)\n\t"
    "v_exp_f32 v58, v58\n\t"
    "v_mfma_f32_16x16x32_bf16 v[184:187], a[140:143], v[100:103], v[184:187]\n\t"
    "ds_read_b64_tr_b16 a[144:145], v19 offset:0\n\t"
    "v_accvgpr_write_b32 a78, v44\n\t"
    "v_mfma_f32_16x16x32_bf16 v[52:55], a[112:115], a[192:195], v[4:7]\n\t"
    "v_exp_f32 v59, v59\n\t"
    "v_mfma_f32_16x16x32_bf16 v[52:55], a[116:119], a[196:199], v[52:55]\n\t"
    "ds_read_b64_tr_b16 a[146:147], v19 offset:2048\n\t"
    "v_cvt_pk_bf16_f32 v129, v58, v59\n\t"
    "v_mfma_f32_16x16x32_bf16 v[52:55], a[120:123], a[200:203], v[52:55]\n\t"
    "ds_read_b128 a[88:91], v30 offset:0\n\t"
    "v_accvgpr_write_b32 a79, v46\n\t"
    "v_mfma_f32_16x16x32_bf16 v[52:55], a[124:127], a[204:207], v[52:55]\n\t"
    "ds_read_b64_tr_b16 a[148:149], v19 offset:4096\n\t"
    "ds_read_b64_tr_b16 a[150:151], v19 offset:6144\n\t"
    "v_mfma_f32_16x16x32_bf16 v[188:191], a[128:131], v[68:71], v[188:191]\n\t"
    "ds_read_b128 a[92:95], v31 offset:0\n\t"
    "ds_read_b64_tr_b16 a[152:153], v19 offset:8192\n\t"
    "v_mfma_f32_16x16x32_bf16 v[188:191], a[132:135], v[80:83], v[188:191]\n\t"
    "v_exp_f32 v60, v60\n\t"
    "v_mfma_f32_16x16x32_bf16 v[188:191], a[136:139], v[92:95], v[188:191]\n\t"
    "v_exp_f32 v61, v61\n\t"
    "v_mfma_f32_16x16x32_bf16 v[188:191], a[140:143], v[104:107], v[188:191]\n\t"
    "v_exp_f32 v62, v62\n\t"
    "v_mfma_f32_16x16x32_bf16 v[56:59], a[112:115], a[208:211], v[8:11]\n\t"
    "v_exp_f32 v63, v63\n\t"
    "v_mfma_f32_16x16x32_bf16 v[56:59], a[116:119], a[212:215], v[56:59]\n\t"
    "ds_read_b64_tr_b16 a[154:155], v19 offset:10240\n\t"
    "v_cvt_pk_bf16_f32 v132, v60, v61\n\t"
    "v_mfma_f32_16x16x32_bf16 v[56:59], a[120:123], a[216:219], v[56:59]\n\t"
    "v_cvt_pk_bf16_f32 v133, v62, v63\n\t"
    "v_mfma_f32_16x16x32_bf16 v[56:59], a[124:127], a[220:223], v[56:59]\n\t"
    "ds_read_b64_tr_b16 a[156:157], v19 offset:12288\n\t"
    "ds_read_b64_tr_b16 a[158:159], v19 offset:14336\n\t"
    "v_mfma_f32_16x16x32_bf16 v[192:195], a[128:131], v[72:75], v[192:195]\n\t"
    "v_exp_f32 v48, v48\n\t"
    "v_mfma_f32_16x16x32_bf16 v[192:195], a[132:135], v[84:87], v[192:195]\n\t"
    "v_exp_f32 v49, v49\n\t"
    "v_mfma_f32_16x16x32_bf16 v[192:195], a[136:139], v[96:99], v[192:195]\n\t"
    "v_exp_f32 v50, v50\n\t"
    "v_mfma_f32_16x16x32_bf16 v[192:195], a[140:143], v[108:111], v[192:195]\n\t"
    "s_waitcnt lgkmcnt(3)\n\t"
    "v_exp_f32 v51, v51\n\t"
    "v_mfma_f32_16x16x32_bf16 v[60:63], a[96:99], a[176:179], v[0:3]\n\t"
    "v_cvt_pk_bf16_f32 v126, v48, v49\n\t"
    "v_cvt_pk_bf16_f32 v127, v50, v51\n\t"
    "v_mfma_f32_16x16x32_bf16 v[60:63], a[100:103], a[180:183], v[60:63]\n\t"
    "v_exp_f32 v52, v52\n\t"
    "v_mfma_f32_16x16x32_bf16 v[60:63], a[104:107], a[184:187], v[60:63]\n\t"
    "v_exp_f32 v53, v53\n\t"
    "v_mfma_f32_16x16x32_bf16 v[60:63], a[108:111], a[188:191], v[60:63]\n\t"
    "v_exp_f32 v54, v54\n\t"
    "v_mfma_f32_16x16x32_bf16 v[196:199], a[144:147], v[64:67], v[196:199]\n\t"
    "v_exp_f32 v55, v55\n\t"
    "v_mfma_f32_16x16x32_bf16 v[196:199], a[148:151], v[76:79], v[196:199]\n\t"
    "s_waitcnt lgkmcnt(0)\n\t"
    "v_cvt_pk_bf16_f32 v130, v52, v53\n\t"
    "v_cvt_pk_bf16_f32 v131, v54, v55\n\t"
    "v_mfma_f32_16x16x32_bf16 v[196:199], a[152:155], v[88:91], v[196:199]\n\t"
    "s_waitcnt vmcnt(20) lgkmcnt(0)\n\t"
    "v_exp_f32 v56, v56\n\t"
    "v_mfma_f32_16x16x32_bf16 v[196:199], a[156:159], v[100:103], v[196:199]\n\t"
    "s_barrier\n\t"
    "ds_read_b128 a[112:115], v12 offset:53248\n\t"
    "v_mfma_f32_16x16x32_bf16 v[40:43], a[96:99], a[192:195], v[4:7]\n\t"
    "s_mov_b32 m0, s85\n\t"
    "ds_read_b128 a[116:119], v13 offset:53248\n\t"
    "v_mfma_f32_16x16x32_bf16 v[40:43], a[100:103], a[196:199], v[40:43]\n\t"
    "v_exp_f32 v57, v57\n\t"
    "v_mfma_f32_16x16x32_bf16 v[40:43], a[104:107], a[200:203], v[40:43]\n\t"
    "ds_read_b128 a[120:123], v14 offset:53248\n\t"
    "v_cvt_pk_bf16_f32 v134, v56, v57\n\t"
    "v_mfma_f32_16x16x32_bf16 v[40:43], a[108:111], a[204:207], v[40:43]\n\t"
    "buffer_load_dwordx4 v20, s[36:39], s70 offen offset:0 nt lds\n\t"
    "v_mfma_f32_16x16x32_bf16 v[200:203], a[144:147], v[68:71], v[200:203]\n\t"
    "ds_read_b128 a[124:127], v15 offset:53248\n\t"
    "ds_read_b64_tr_b16 a[128:129], v16 offset:16384\n\t"
    "v_mfma_f32_16x16x32_bf16 v[200:203], a[148:151], v[80:83], v[200:203]\n\t"
    "v_exp_f32 v58, v58\n\t"
    "v_mfma_f32_16x16x32_bf16 v[200:203], a[152:155], v[92:95], v[200:203]\n\t"
    "v_exp_f32 v59, v59\n\t"
    "v_mfma_f32_16x16x32_bf16 v[200:203], a[156:159], v[104:107], v[200:203]\n\t"
    "ds_read_b64_tr_b16 a[130:131], v16 offset:18432\n\t"
    "v_cvt_pk_bf16_f32 v135, v58, v59\n\t"
    "v_mfma_f32_16x16x32_bf16 v[44:47], a[96:99], a[208:211], v[8:11]\n\t"
    "v_exp_f32 v60, v60\n\t"
    "v_mfma_f32_16x16x32_bf16 v[44:47], a[100:103], a[212:215], v[44:47]\n\t"
    "ds_read_b64_tr_b16 a[132:133], v16 offset:20480\n\t"
    "ds_read_b64_tr_b16 a[134:135], v16 offset:22528\n\t"
    "v_mfma_f32_16x16x32_bf16 v[44:47], a[104:107], a[216:219], v[44:47]\n\t"
    "v_exp_f32 v61, v61\n\t"
    "v_mfma_f32_16x16x32_bf16 v[44:47], a[108:111], a[220:223], v[44:47]\n\t"
    "v_exp_f32 v62, v62\n\t"
    "v_mfma_f32_16x16x32_bf16 v[48:51], a[228:231], a[80:83], 0\n\t"
    "ds_read_b64_tr_b16 a[136:137], v16 offset:24576\n\t"
    "v_cvt_pk_bf16_f32 v136, v60, v61\n\t"
    "v_mfma_f32_16x16x32_bf16 v[48:51], a[232:235], a[80:83], v[48:51]\n\t"
    "buffer_load_dwordx4 v21, s[36:39], s70 offen offset:1024 nt lds\n\t"
    "v_mfma_f32_16x16x32_bf16 v[52:55], a[236:239], a[80:83], 0\n\t"
    "ds_read_b64_tr_b16 a[138:139], v16 offset:26624\n\t"
    "ds_read_b64_tr_b16 a[140:141], v16 offset:28672\n\t"
    "v_mfma_f32_16x16x32_bf16 v[52:55], a[240:243], a[80:83], v[52:55]\n\t"
    "v_exp_f32 v63, v63\n\t"
    "v_mfma_f32_16x16x32_bf16 v[204:207], a[144:147], v[72:75], v[204:207]\n\t"
    "v_cvt_pk_bf16_f32 v137, v62, v63\n\t"
    "v_mfma_f32_16x16x32_bf16 v[204:207], a[148:151], v[84:87], v[204:207]\n\t"
    "v_exp_f32 v40, v40\n\t"
    "v_mfma_f32_16x16x32_bf16 v[204:207], a[152:155], v[96:99], v[204:207]\n\t"
    "ds_read_b64_tr_b16 a[142:143], v16 offset:30720\n\t"
    "v_cvt_pk_bf16_f32 v48, v48, v49\n\t"
    "v_mfma_f32_16x16x32_bf16 v[204:207], a[156:159], v[108:111], v[204:207]\n\t"
    "s_waitcnt lgkmcnt(4)\n\t"
    "v_exp_f32 v41, v41\n\t"
    "v_mfma_f32_16x16x32_bf16 v[56:59], a[112:115], a[176:179], v[0:3]\n\t"
    "ds_read_b128 a[96:99], v12 offset:57344\n\t"
    "v_cvt_pk_bf16_f32 v140, v40, v41\n\t"
    "v_mfma_f32_16x16x32_bf16 v[56:59], a[116:119], a[180:183], v[56:59]\n\t"
    "v_exp_f32 v42, v42\n\t"
    "v_mfma_f32_16x16x32_bf16 v[56:59], a[120:123], a[184:187], v[56:59]\n\t"
    "ds_read_b128 a[100:103], v13 offset:57344\n\t"
    "v_cvt_pk_bf16_f32 v50, v50, v51\n\t"
    "v_mfma_f32_16x16x32_bf16 v[56:59], a[124:127], a[188:191], v[56:59]\n\t"
    "buffer_load_dwordx4 v22, s[36:39], s70 offen offset:2048 nt lds\n\t"
    "v_mfma_f32_16x16x32_bf16 v[208:211], a[128:131], v[64:67], v[208:211]\n\t"
    "v_exp_f32 v43, v43\n\t"
    "v_mfma_f32_16x16x32_bf16 v[208:211], a[132:135], v[76:79], v[208:211]\n\t"
    "s_waitcnt lgkmcnt(3)\n\t"
    "ds_read_b128 a[104:107], v14 offset:57344\n\t"
    "v_cvt_pk_bf16_f32 v141, v42, v43\n\t"
    "v_mfma_f32_16x16x32_bf16 v[208:211], a[136:139], v[88:91], v[208:211]\n\t"
    "s_waitcnt lgkmcnt(3)\n\t"
    "v_exp_f32 v44, v44\n\t"
    "v_mfma_f32_16x16x32_bf16 v[208:211], a[140:143], v[100:103], v[208:211]\n\t"
    "v_exp_f32 v45, v45\n\t"
    "v_mfma_f32_16x16x32_bf16 v[60:63], a[112:115], a[192:195], v[4:7]\n\t"
    "ds_read_b128 a[108:111], v15 offset:57344\n\t"
    "v_cvt_pk_bf16_f32 v144, v44, v45\n\t"
    "v_mfma_f32_16x16x32_bf16 v[60:63], a[116:119], a[196:199], v[60:63]\n\t"
    "v_exp_f32 v46, v46\n\t"
    "v_mfma_f32_16x16x32_bf16 v[60:63], a[120:123], a[200:203], v[60:63]\n\t"
    "ds_read_b64_tr_b16 a[144:145], v17 offset:16384\n\t"
    "v_accvgpr_write_b32 a80, v48\n\t"
    "v_mfma_f32_16x16x32_bf16 v[60:63], a[124:127], a[204:207], v[60:63]\n\t"
    "v_exp_f32 v47, v47\n\t"
    "v_mfma_f32_16x16x32_bf16 v[212:215], a[128:131], v[68:71], v[212:215]\n\t"
    "buffer_load_dwordx4 v23, s[36:39], s70 offen offset:3072 nt lds\n\t"
    "v_mfma_f32_16x16x32_bf16 v[212:215], a[132:135], v[80:83], v[212:215]\n\t"
    "ds_read_b64_tr_b16 a[146:147], v17 offset:18432\n\t"
    "v_cvt_pk_bf16_f32 v145, v46, v47\n\t"
    "v_mfma_f32_16x16x32_bf16 v[212:215], a[136:139], v[92:95], v[212:215]\n\t"
    "v_accvgpr_write_b32 a81, v50\n\t"
    "v_cvt_pk_bf16_f32 v52, v52, v53\n\t"
    "v_mfma_f32_16x16x32_bf16 v[212:215], a[140:143], v[104:107], v[212:215]\n\t"
    "v_cvt_pk_bf16_f32 v54, v54, v55\n\t"
    "v_accvgpr_write_b32 a82, v52\n\t"
    "v_mfma_f32_16x16x32_bf16 a[164:167], a[172:175], v[68:71], a[164:167]\n\t"
    "ds_read_b64_tr_b16 a[148:149], v17 offset:20480\n\t"
    "v_accvgpr_write_b32 a83, v54\n\t"
    "v_mfma_f32_16x16x32_bf16 a[164:167], a[172:175], v[80:83], a[164:167]\n\t"
    "v_exp_f32 v56, v56\n\t"
    "v_mfma_f32_16x16x32_bf16 a[164:167], a[172:175], v[92:95], a[164:167]\n\t"
    "v_exp_f32 v57, v57\n\t"
    "v_mfma_f32_16x16x32_bf16 a[164:167], a[172:175], v[104:107], a[164:167]\n\t"
    "ds_read_b64_tr_b16 a[150:151], v17 offset:22528\n\t"
    "v_cvt_pk_bf16_f32 v138, v56, v57\n\t"
    "v_mfma_f32_16x16x32_bf16 v[40:43], a[112:115], a[208:211], v[8:11]\n\t"
    "s_add_u32 s78, s56, 36864\n\t"
    "v_exp_f32 v58, v58\n\t"
    "v_mfma_f32_16x16x32_bf16 v[40:43], a[116:119], a[212:215], v[40:43]\n\t"
    "s_mov_b32 m0, s75\n\t"
    "ds_read_b64_tr_b16 a[152:153], v17 offset:24576\n\t"
    "ds_read_b64_tr_b16 a[154:155], v17 offset:26624\n\t"
    "v_mfma_f32_16x16x32_bf16 v[40:43], a[120:123], a[216:219], v[40:43]\n\t"
    "buffer_load_dwordx4 v20, s[40:43], s78 offen offset:0 nt lds\n\t"
    "v_mfma_f32_16x16x32_bf16 v[40:43], a[124:127], a[220:223], v[40:43]\n\t"
    "v_exp_f32 v59, v59\n\t"
    "v_mfma_f32_16x16x32_bf16 v[44:47], a[228:231], a[84:87], 0\n\t"
    "v_cvt_pk_bf16_f32 v139, v58, v59\n\t"
    "v_mfma_f32_16x16x32_bf16 v[44:47], a[232:235], a[84:87], v[44:47]\n\t"
    "v_exp_f32 v60, v60\n\t"
    "v_mfma_f32_16x16x32_bf16 v[48:51], a[236:239], a[84:87], 0\n\t"
    "v_exp_f32 v61, v61\n\t"
    "v_mfma_f32_16x16x32_bf16 v[48:51], a[240:243], a[84:87], v[48:51]\n\t"
    "ds_read_b64_tr_b16 a[156:157], v17 offset:28672\n\t"
    "v_cvt_pk_bf16_f32 v142, v60, v61\n\t"
    "v_mfma_f32_16x16x32_bf16 v[216:219], a[128:131], v[72:75], v[216:219]\n\t"
    "v_exp_f32 v62, v62\n\t"
    "v_mfma_f32_16x16x32_bf16 v[216:219], a[132:135], v[84:87], v[216:219]\n\t"
    "v_exp_f32 v63, v63\n\t"
    "v_mfma_f32_16x16x32_bf16 v[216:219], a[136:139], v[96:99], v[216:219]\n\t"
    "ds_read_b64_tr_b16 a[158:159], v17 offset:30720\n\t"
    "v_cvt_pk_bf16_f32 v143, v62, v63\n\t"
    "v_mfma_f32_16x16x32_bf16 v[216:219], a[140:143], v[108:111], v[216:219]\n\t"
    "s_waitcnt lgkmcnt(2)\n\t"
    "v_exp_f32 v40, v40\n\t"
    "v_mfma_f32_16x16x32_bf16 v[52:55], a[96:99], a[176:179], v[0:3]\n\t"
    "ds_read_b128 a[112:115], v12 offset:61440\n\t"
    "v_cvt_pk_bf16_f32 v44, v44, v45\n\t"
    "v_mfma_f32_16x16x32_bf16 v[52:55], a[100:103], a[180:183], v[52:55]\n\t"
    "buffer_load_dwordx4 v21, s[40:43], s78 offen offset:1024 nt lds\n\t"
    "v_mfma_f32_16x16x32_bf16 v[52:55], a[104:107], a[184:187], v[52:55]\n\t"
    "ds_read_b128 a[116:119], v13 offset:61440\n\t"
    "v_cvt_pk_bf16_f32 v46, v46, v47\n\t"
    "v_mfma_f32_16x16x32_bf16 v[52:55], a[108:111], a[188:191], v[52:55]\n\t"
    "v_exp_f32 v41, v41\n\t"
    "v_mfma_f32_16x16x32_bf16 v[220:223], a[144:147], v[64:67], v[220:223]\n\t"
    "ds_read_b128 a[120:123], v14 offset:61440\n\t"
    "v_cvt_pk_bf16_f32 v146, v40, v41\n\t"
    "v_mfma_f32_16x16x32_bf16 v[220:223], a[148:151], v[76:79], v[220:223]\n\t"
    "v_exp_f32 v42, v42\n\t"
    "v_mfma_f32_16x16x32_bf16 v[220:223], a[152:155], v[88:91], v[220:223]\n\t"
    "s_waitcnt lgkmcnt(3)\n\t"
    "v_exp_f32 v43, v43\n\t"
    "v_mfma_f32_16x16x32_bf16 v[220:223], a[156:159], v[100:103], v[220:223]\n\t"
    "ds_read_b128 a[124:127], v15 offset:61440\n\t"
    "v_cvt_pk_bf16_f32 v147, v42, v43\n\t"
    "v_mfma_f32_16x16x32_bf16 v[56:59], a[96:99], a[192:195], v[4:7]\n\t"
    "v_accvgpr_write_b32 a84, v44\n\t"
    "v_accvgpr_write_b32 a85, v46\n\t"
    "v_mfma_f32_16x16x32_bf16 v[56:59], a[100:103], a[196:199], v[56:59]\n\t"
    "ds_read_b64_tr_b16 a[128:129], v18 offset:16384\n\t"
    "v_cvt_pk_bf16_f32 v48, v48, v49\n\t"
    "v_mfma_f32_16x16x32_bf16 v[56:59], a[104:107], a[200:203], v[56:59]\n\t"
    "buffer_load_dwordx4 v22, s[40:43], s78 offen offset:2048 nt lds\n\t"
    "v_mfma_f32_16x16x32_bf16 v[56:59], a[108:111], a[204:207], v[56:59]\n\t"
    "ds_read_b64_tr_b16 a[130:131], v18 offset:18432\n\t"
    "v_cvt_pk_bf16_f32 v50, v50, v51\n\t"
    "v_mfma_f32_16x16x32_bf16 v[224:227], a[144:147], v[68:71], v[224:227]\n\t"
    "v_accvgpr_write_b32 a86, v48\n\t"
    "v_accvgpr_write_b32 a87, v50\n\t"
    "v_mfma_f32_16x16x32_bf16 v[224:227], a[148:151], v[80:83], v[224:227]\n\t"
    "v_exp_f32 v52, v52\n\t"
    "v_mfma_f32_16x16x32_bf16 v[224:227], a[152:155], v[92:95], v[224:227]\n\t"
    "ds_read_b64_tr_b16 a[132:133], v18 offset:20480\n\t"
    "ds_read_b64_tr_b16 a[134:135], v18 offset:22528\n\t"
    "v_mfma_f32_16x16x32_bf16 v[224:227], a[156:159], v[104:107], v[224:227]\n\t"
    "v_exp_f32 v53, v53\n\t"
    "v_mfma_f32_16x16x32_bf16 v[60:63], a[96:99], a[208:211], v[8:11]\n\t"
    "v_exp_f32 v54, v54\n\t"
    "v_mfma_f32_16x16x32_bf16 v[60:63], a[100:103], a[212:215], v[60:63]\n\t"
    "v_exp_f32 v55, v55\n\t"
    "v_mfma_f32_16x16x32_bf16 v[60:63], a[104:107], a[216:219], v[60:63]\n\t"
    "ds_read_b64_tr_b16 a[136:137], v18 offset:24576\n\t"
    "v_cvt_pk_bf16_f32 v148, v52, v53\n\t"
    "v_mfma_f32_16x16x32_bf16 v[60:63], a[108:111], a[220:223], v[60:63]\n\t"
    "v_cvt_pk_bf16_f32 v149, v54, v55\n\t"
    "v_mfma_f32_16x16x32_bf16 v[40:43], a[228:231], a[88:91], 0\n\t"
    "buffer_load_dwordx4 v23, s[40:43], s78 offen offset:3072 nt lds\n\t"
    "v_mfma_f32_16x16x32_bf16 v[40:43], a[232:235], a[88:91], v[40:43]\n\t"
    "ds_read_b64_tr_b16 a[138:139], v18 offset:26624\n\t"
    "ds_read_b64_tr_b16 a[140:141], v18 offset:28672\n\t"
    "v_mfma_f32_16x16x32_bf16 v[44:47], a[236:239], a[88:91], 0\n\t"
    "v_exp_f32 v56, v56\n\t"
    "v_mfma_f32_16x16x32_bf16 v[44:47], a[240:243], a[88:91], v[44:47]\n\t"
    "v_exp_f32 v57, v57\n\t"
    "v_mfma_f32_16x16x32_bf16 v[228:231], a[144:147], v[72:75], v[228:231]\n\t"
    "v_exp_f32 v58, v58\n\t"
    "v_mfma_f32_16x16x32_bf16 v[228:231], a[148:151], v[84:87], v[228:231]\n\t"
    "v_exp_f32 v59, v59\n\t"
    "v_mfma_f32_16x16x32_bf16 v[228:231], a[152:155], v[96:99], v[228:231]\n\t"
    "ds_read_b64_tr_b16 a[142:143], v18 offset:30720\n\t"
    "v_cvt_pk_bf16_f32 v152, v56, v57\n\t"
    "v_mfma_f32_16x16x32_bf16 v[228:231], a[156:159], v[108:111], v[228:231]\n\t"
    "s_waitcnt lgkmcnt(3)\n\t"
    "v_cvt_pk_bf16_f32 v153, v58, v59\n\t"
    "v_cvt_pk_bf16_f32 v40, v40, v41\n\t"
    "v_mfma_f32_16x16x32_bf16 v[48:51], a[112:115], a[176:179], v[0:3]\n\t"
    "s_add_u32 s79, s62, 0\n\t"
    "ds_read_b128 a[96:99], v12 offset:0\n\t"
    "v_cvt_pk_bf16_f32 v42, v42, v43\n\t"
    "v_mfma_f32_16x16x32_bf16 v[48:51], a[116:119], a[180:183], v[48:51]\n\t"
    "s_add_u32 m0, s77, 0\n\t"
    "v_exp_f32 v60, v60\n\t"
    "v_mfma_f32_16x16x32_bf16 v[48:51], a[120:123], a[184:187], v[48:51]\n\t"
    "ds_read_b128 a[100:103], v13 offset:0\n\t"
    "v_accvgpr_write_b32 a88, v40\n\t"
    "v_mfma_f32_16x16x32_bf16 v[48:51], a[124:127], a[188:191], v[48:51]\n\t"
    "buffer_load_dwordx4 v24, s[44:47], s79 offen offset:0 nt lds\n\t"
    "v_mfma_f32_16x16x32_bf16 v[232:235], a[128:131], v[64:67], v[232:235]\n\t"
    "v_exp_f32 v61, v61\n\t"
    "v_mfma_f32_16x16x32_bf16 v[232:235], a[132:135], v[76:79], v[232:235]\n\t"
    "s_waitcnt lgkmcnt(3)\n\t"
    "ds_read_b128 a[104:107], v14 offset:0\n\t"
    "v_cvt_pk_bf16_f32 v156, v60, v61\n\t"
    "v_mfma_f32_16x16x32_bf16 v[232:235], a[136:139], v[88:91], v[232:235]\n\t"
    "s_waitcnt lgkmcnt(3)\n\t"
    "v_exp_f32 v62, v62\n\t"
    "v_mfma_f32_16x16x32_bf16 v[232:235], a[140:143], v[100:103], v[232:235]\n\t"
    "ds_read_b128 a[108:111], v15 offset:0\n\t"
    "v_accvgpr_write_b32 a89, v42\n\t"
    "v_mfma_f32_16x16x32_bf16 v[52:55], a[112:115], a[192:195], v[4:7]\n\t"
    "v_exp_f32 v63, v63\n\t"
    "v_mfma_f32_16x16x32_bf16 v[52:55], a[116:119], a[196:199], v[52:55]\n\t"
    "buffer_load_dwordx4 v25, s[44:47], s79 offen offset:1024 nt lds\n\t"
    "v_mfma_f32_16x16x32_bf16 v[52:55], a[120:123], a[200:203], v[52:55]\n\t"
    "ds_read_b64_tr_b16 a[144:145], v19 offset:16384\n\t"
    "v_cvt_pk_bf16_f32 v157, v62, v63\n\t"
    "v_mfma_f32_16x16x32_bf16 v[52:55], a[124:127], a[204:207], v[52:55]\n\t"
    "v_cvt_pk_bf16_f32 v44, v44, v45\n\t"
    "v_cvt_pk_bf16_f32 v46, v46, v47\n\t"
    "v_mfma_f32_16x16x32_bf16 v[236:239], a[128:131], v[68:71], v[236:239]\n\t"
    "v_accvgpr_write_b32 a90, v44\n\t"
    "v_accvgpr_write_b32 a91, v46\n\t"
    "v_mfma_f32_16x16x32_bf16 v[236:239], a[132:135], v[80:83], v[236:239]\n\t"
    "ds_read_b64_tr_b16 a[146:147], v19 offset:18432\n\t"
    "ds_read_b64_tr_b16 a[148:149], v19 offset:20480\n\t"
    "v_mfma_f32_16x16x32_bf16 v[236:239], a[136:139], v[92:95], v[236:239]\n\t"
    "v_exp_f32 v48, v48\n\t"
    "v_mfma_f32_16x16x32_bf16 v[236:239], a[140:143], v[104:107], v[236:239]\n\t"
    "v_exp_f32 v49, v49\n\t"
    "v_mfma_f32_16x16x32_bf16 a[168:171], a[172:175], v[72:75], a[168:171]\n\t"
    "buffer_load_dwordx4 v26, s[44:47], s79 offen offset:2048 nt lds\n\t"
    "v_mfma_f32_16x16x32_bf16 a[168:171], a[172:175], v[84:87], a[168:171]\n\t"
    "v_exp_f32 v50, v50\n\t"
    "v_mfma_f32_16x16x32_bf16 a[168:171], a[172:175], v[96:99], a[168:171]\n\t"
    "ds_read_b64_tr_b16 a[150:151], v19 offset:22528\n\t"
    "v_cvt_pk_bf16_f32 v150, v48, v49\n\t"
    "v_mfma_f32_16x16x32_bf16 a[168:171], a[172:175], v[108:111], a[168:171]\n\t"
    "v_exp_f32 v51, v51\n\t"
    "v_mfma_f32_16x16x32_bf16 v[56:59], a[112:115], a[208:211], v[8:11]\n\t"
    "v_cvt_pk_bf16_f32 v151, v50, v51\n\t"
    "v_mfma_f32_16x16x32_bf16 v[56:59], a[116:119], a[212:215], v[56:59]\n\t"
    "ds_read_b64_tr_b16 a[152:153], v19 offset:24576\n\t"
    "ds_read_b64_tr_b16 a[154:155], v19 offset:26624\n\t"
    "v_mfma_f32_16x16x32_bf16 v[56:59], a[120:123], a[216:219], v[56:59]\n\t"
    "v_exp_f32 v52, v52\n\t"
    "v_mfma_f32_16x16x32_bf16 v[56:59], a[124:127], a[220:223], v[56:59]\n\t"
    "buffer_load_dwordx4 v27, s[44:47], s79 offen offset:3072 nt lds\n\t"
    "v_mfma_f32_16x16x32_bf16 v[60:63], a[228:231], a[92:95], 0\n\t"
    "v_exp_f32 v53, v53\n\t"
    "v_mfma_f32_16x16x32_bf16 v[60:63], a[232:235], a[92:95], v[60:63]\n\t"
    "v_exp_f32 v54, v54\n\t"
    "v_mfma_f32_16x16x32_bf16 v[40:43], a[236:239], a[92:95], 0\n\t"
    "ds_read_b64_tr_b16 a[156:157], v19 offset:28672\n\t"
    "v_cvt_pk_bf16_f32 v154, v52, v53\n\t"
    "v_mfma_f32_16x16x32_bf16 v[40:43], a[240:243], a[92:95], v[40:43]\n\t"
    "v_exp_f32 v55, v55\n\t"
    "v_mfma_f32_16x16x32_bf16 v[240:243], a[128:131], v[72:75], v[240:243]\n\t"
    "v_cvt_pk_bf16_f32 v155, v54, v55\n\t"
    "v_mfma_f32_16x16x32_bf16 v[240:243], a[132:135], v[84:87], v[240:243]\n\t"
    "ds_read_b64_tr_b16 a[158:159], v19 offset:30720\n\t"
    "v_cvt_pk_bf16_f32 v60, v60, v61\n\t"
    "v_mfma_f32_16x16x32_bf16 v[240:243], a[136:139], v[96:99], v[240:243]\n\t"
    "v_cvt_pk_bf16_f32 v62, v62, v63\n\t"
    "v_accvgpr_write_b32 a92, v60\n\t"
    "v_mfma_f32_16x16x32_bf16 v[240:243], a[140:143], v[108:111], v[240:243]\n\t"
    "s_waitcnt lgkmcnt(2)\n\t"
    "v_accvgpr_write_b32 a93, v62\n\t"
    "v_cvt_pk_bf16_f32 v40, v40, v41\n\t"
    "v_mfma_f32_16x16x32_bf16 v[44:47], a[96:99], a[48:51], 0\n\t"
    "v_cvt_pk_bf16_f32 v42, v42, v43\n\t"
    "v_accvgpr_write_b32 a94, v40\n\t"
    "v_mfma_f32_16x16x32_bf16 v[44:47], a[100:103], a[52:55], v[44:47]\n\t"
    "v_accvgpr_write_b32 a95, v42\n\t"
    "v_mfma_f32_16x16x32_bf16 v[44:47], a[104:107], a[56:59], v[44:47]\n\t"
    "v_exp_f32 v56, v56\n\t"
    "v_mfma_f32_16x16x32_bf16 v[44:47], a[108:111], a[60:63], v[44:47]\n\t"
    "v_exp_f32 v57, v57\n\t"
    "v_mfma_f32_16x16x32_bf16 v[244:247], a[144:147], v[64:67], v[244:247]\n\t"
    "v_exp_f32 v58, v58\n\t"
    "v_mfma_f32_16x16x32_bf16 v[244:247], a[148:151], v[76:79], v[244:247]\n\t"
    "v_exp_f32 v59, v59\n\t"
    "v_mfma_f32_16x16x32_bf16 v[244:247], a[152:155], v[88:91], v[244:247]\n\t"
    "s_waitcnt vmcnt(20) lgkmcnt(0)\n\t"
    "v_cvt_pk_bf16_f32 v158, v56, v57\n\t"
    "v_cvt_pk_bf16_f32 v159, v58, v59\n\t"
    "v_mfma_f32_16x16x32_bf16 v[244:247], a[156:159], v[100:103], v[244:247]\n\t"
    "s_barrier\n\t"
    "ds_read_b128 a[112:115], v12 offset:4096\n\t"
    "v_max3_f32 v0, v44, v45, v46\n\t"
    "v_mfma_f32_16x16x32_bf16 v[48:51], a[96:99], a[64:67], 0\n\t"
    "s_add_u32 s78, s56, 53248\n\t"
    "ds_read_b128 a[116:119], v13 offset:4096\n\t"
    "v_max_f32 v0, v0, v47\n\t"
    "v_mfma_f32_16x16x32_bf16 v[48:51], a[100:103], a[68:71], v[48:51]\n\t"
    "s_mov_b32 m0, s76\n\t"
    "ds_read_b128 a[120:123], v14 offset:4096\n\t"
    "v_mov_b32 v1, v0\n\t"
    "v_mfma_f32_16x16x32_bf16 v[48:51], a[104:107], a[72:75], v[48:51]\n\t"
    "buffer_load_dwordx4 v20, s[40:43], s78 offen offset:0 nt lds\n\t"
    "v_mfma_f32_16x16x32_bf16 v[48:51], a[108:111], a[76:79], v[48:51]\n\t"
    "ds_read_b128 a[124:127], v15 offset:4096\n\t"
    "ds_read_b64_tr_b16 a[128:129], v16 offset:32768\n\t"
    "v_mfma_f32_16x16x32_bf16 v[248:251], a[144:147], v[68:71], v[248:251]\n\t"
    "v_permlane32_swap_b32 v0, v1\n\t"
    "v_mfma_f32_16x16x32_bf16 v[248:251], a[148:151], v[80:83], v[248:251]\n\t"
    "ds_read_b64_tr_b16 a[130:131], v16 offset:34816\n\t"
    "v_max_f32 v0, v0, v1\n\t"
    "v_mfma_f32_16x16x32_bf16 v[248:251], a[152:155], v[92:95], v[248:251]\n\t"
    "v_mov_b32 v1, v0\n\t"
    "v_mfma_f32_16x16x32_bf16 v[248:251], a[156:159], v[104:107], v[248:251]\n\t"
    "ds_read_b64_tr_b16 a[132:133], v16 offset:36864\n\t"
    "v_max3_f32 v4, v48, v49, v50\n\t"
    "v_mfma_f32_16x16x32_bf16 v[52:55], a[96:99], a[80:83], 0\n\t"
    "ds_read_b64_tr_b16 a[134:135], v16 offset:38912\n\t"
    "v_max_f32 v4, v4, v51\n\t"
    "v_mfma_f32_16x16x32_bf16 v[52:55], a[100:103], a[84:87], v[52:55]\n\t"
    "v_permlane16_swap_b32 v0, v1\n\t"
    "v_mfma_f32_16x16x32_bf16 v[52:55], a[104:107], a[88:91], v[52:55]\n\t"
    "ds_read_b64_tr_b16 a[136:137], v16 offset:40960\n\t"
    "v_min_f32 v2, -v0, -v1\n\t"
    "v_mfma_f32_16x16x32_bf16 v[52:55], a[108:111], a[92:95], v[52:55]\n\t"
    "v_mov_b32 v0, v2\n\t"
    "v_mov_b32 v1, v2\n\t"
    "v_mov_b32 v3, v2\n\t"
    "v_mfma_f32_16x16x32_bf16 v[252:255], a[144:147], v[72:75], v[252:255]\n\t"
    "ds_read_b64_tr_b16 a[138:139], v16 offset:43008\n\t"
    "v_mov_b32 v5, v4\n\t"
    "v_mfma_f32_16x16x32_bf16 v[252:255], a[148:151], v[84:87], v[252:255]\n\t"
    "ds_read_b64_tr_b16 a[140:141], v16 offset:45056\n\t"
    "v_add_f32 v44, v44, v0\n\t"
    "v_mfma_f32_16x16x32_bf16 v[252:255], a[152:155], v[96:99], v[252:255]\n\t"
    "ds_read_b64_tr_b16 a[142:143], v16 offset:47104\n\t"
    "v_add_f32 v45, v45, v0\n\t"
    "v_mfma_f32_16x16x32_bf16 v[252:255], a[156:159], v[108:111], v[252:255]\n\t"
    "s_waitcnt lgkmcnt(5)\n\t"
    "v_permlane32_swap_b32 v4, v5\n\t"
    "v_mfma_f32_16x16x32_bf16 v[56:59], a[112:115], a[48:51], v[0:3]\n\t"
    "v_max_f32 v4, v4, v5\n\t"
    "v_mov_b32 v5, v4\n\t"
    "v_mfma_f32_16x16x32_bf16 v[56:59], a[116:119], a[52:55], v[56:59]\n\t"
    "ds_read_b128 a[96:99], v12 offset:8192\n\t"
    "v_max3_f32 v8, v52, v53, v54\n\t"
    "v_max_f32 v8, v8, v55\n\t"
    "v_mfma_f32_16x16x32_bf16 v[56:59], a[120:123], a[56:59], v[56:59]\n\t"
    "ds_read_b128 a[100:103], v13 offset:8192\n\t"
    "v_permlane16_swap_b32 v4, v5\n\t"
    "v_mfma_f32_16x16x32_bf16 v[56:59], a[124:127], a[60:63], v[56:59]\n\t"
    "buffer_load_dwordx4 v21, s[40:43], s78 offen offset:1024 nt lds\n\t"
    "v_min_f32 v6, -v4, -v5\n\t"
    "v_mfma_f32_16x16x32_bf16 v[160:163], a[128:131], v[112:115], v[160:163]\n\t"
    "s_waitcnt lgkmcnt(4)\n\t"
    "v_mov_b32 v4, v6\n\t"
    "v_mov_b32 v5, v6\n\t"
    "v_mov_b32 v7, v6\n\t"
    "v_mfma_f32_16x16x32_bf16 v[160:163], a[132:135], v[124:127], v[160:163]\n\t"
    "ds_read_b128 a[104:107], v14 offset:8192\n\t"
    "v_mov_b32 v9, v8\n\t"
    "v_mfma_f32_16x16x32_bf16 v[160:163], a[136:139], v[136:139], v[160:163]\n\t"
    "s_waitcnt lgkmcnt(3)\n\t"
    "buffer_load_dwordx4 v22, s[40:43], s78 offen offset:2048 nt lds\n\t"
    "v_mfma_f32_16x16x32_bf16 v[160:163], a[140:143], v[148:151], v[160:163]\n\t"
    "v_permlane32_swap_b32 v8, v9\n\t"
    "v_mfma_f32_16x16x32_bf16 v[60:63], a[112:115], a[64:67], v[4:7]\n\t"
    "v_max_f32 v8, v8, v9\n\t"
    "v_mov_b32 v9, v8\n\t"
    "v_mfma_f32_16x16x32_bf16 v[60:63], a[116:119], a[68:71], v[60:63]\n\t"
    "ds_read_b128 a[108:111], v15 offset:8192\n\t"
    "v_add_f32 v46, v46, v0\n\t"
    "v_mfma_f32_16x16x32_bf16 v[60:63], a[120:123], a[72:75], v[60:63]\n\t"
    "ds_read_b64_tr_b16 a[144:145], v17 offset:32768\n\t"
    "v_permlane16_swap_b32 v8, v9\n\t"
    "v_mfma_f32_16x16x32_bf16 v[60:63], a[124:127], a[76:79], v[60:63]\n\t"
    "v_min_f32 v10, -v8, -v9\n\t"
    "v_mov_b32 v8, v10\n\t"
    "v_mfma_f32_16x16x32_bf16 v[164:167], a[128:131], v[116:119], v[164:167]\n\t"
    "v_mov_b32 v9, v10\n\t"
    "v_mov_b32 v11, v10\n\t"
    "v_mfma_f32_16x16x32_bf16 v[164:167], a[132:135], v[128:131], v[164:167]\n\t"
    "ds_read_b64_tr_b16 a[146:147], v17 offset:34816\n\t"
    "v_add_f32 v47, v47, v0\n\t"
    "v_mfma_f32_16x16x32_bf16 v[164:167], a[136:139], v[140:143], v[164:167]\n\t"
    "ds_read_b64_tr_b16 a[148:149], v17 offset:36864\n\t"
    "v_add_f32 v48, v48, v4\n\t"
    "v_mfma_f32_16x16x32_bf16 v[164:167], a[140:143], v[152:155], v[164:167]\n\t"
    "v_exp_f32 v44, v44\n\t"
    "v_mfma_f32_16x16x32_bf16 a[160:163], a[172:175], v[112:115], a[160:163]\n\t"
    "ds_read_b64_tr_b16 a[150:151], v17 offset:38912\n\t"
    "v_add_f32 v49, v49, v4\n\t"
    "v_mfma_f32_16x16x32_bf16 a[160:163], a[172:175], v[124:127], a[160:163]\n\t"
    "buffer_load_dwordx4 v23, s[40:43], s78 offen offset:3072 nt lds\n\t"
    "v_mfma_f32_16x16x32_bf16 a[160:163], a[172:175], v[136:139], a[160:163]\n\t"
    "ds_read_b64_tr_b16 a[152:153], v17 offset:40960\n\t"
    "v_add_f32 v50, v50, v4\n\t"
    "v_mfma_f32_16x16x32_bf16 a[160:163], a[172:175], v[148:151], a[160:163]\n\t"
    "v_exp_f32 v45, v45\n\t"
    "v_mfma_f32_16x16x32_bf16 v[40:43], a[112:115], a[80:83], v[8:11]\n\t"
    "v_exp_f32 v46, v46\n\t"
    "v_mfma_f32_16x16x32_bf16 v[40:43], a[116:119], a[84:87], v[40:43]\n\t"
    "ds_read_b64_tr_b16 a[154:155], v17 offset:43008\n\t"
    "v_cvt_pk_bf16_f32 v64, v44, v45\n\t"
    "v_mfma_f32_16x16x32_bf16 v[40:43], a[120:123], a[88:91], v[40:43]\n\t"
    "v_exp_f32 v47, v47\n\t"
    "v_add_f32 v51, v51, v4\n\t"
    "v_mfma_f32_16x16x32_bf16 v[40:43], a[124:127], a[92:95], v[40:43]\n\t"
    "ds_read_b64_tr_b16 a[156:157], v17 offset:45056\n\t"
    "v_cvt_pk_bf16_f32 v65, v46, v47\n\t"
    "v_mfma_f32_16x16x32_bf16 v[168:171], a[128:131], v[120:123], v[168:171]\n\t"
    "s_add_u32 s79, s62, 128\n\t"
    "v_exp_f32 v48, v48\n\t"
    "v_mfma_f32_16x16x32_bf16 v[168:171], a[132:135], v[132:135], v[168:171]\n\t"
    "s_add_u32 m0, s77, 16384\n\t"
    "v_exp_f32 v49, v49\n\t"
    "v_mfma_f32_16x16x32_bf16 v[168:171], a[136:139], v[144:147], v[168:171]\n\t"
    "ds_read_b64_tr_b16 a[158:159], v17 offset:47104\n\t"
    "v_cvt_pk_bf16_f32 v68, v48, v49\n\t"
    "v_mfma_f32_16x16x32_bf16 v[168:171], a[140:143], v[156:159], v[168:171]\n\t"
    "s_waitcnt lgkmcnt(3)\n\t"
    "buffer_load_dwordx4 v24, s[44:47], s79 offen offset:0 nt lds\n\t"
    "v_mfma_f32_16x16x32_bf16 v[44:47], a[96:99], a[48:51], v[0:3]\n\t"
    "ds_read_b128 a[112:115], v12 offset:12288\n\t"
    "v_accvgpr_read_b32 v35, a160\n\t"
    "v_mfma_f32_16x16x32_bf16 v[44:47], a[100:103], a[52:55], v[44:47]\n\t"
    "v_exp_f32 v50, v50\n\t"
    "v_mfma_f32_16x16x32_bf16 v[44:47], a[104:107], a[56:59], v[44:47]\n\t"
    "ds_read_b128 a[116:119], v13 offset:12288\n\t"
    "v_cmp_nlt_f32 s[86:87], v35, s54\n\t"
    "v_mfma_f32_16x16x32_bf16 v[44:47], a[108:111], a[60:63], v[44:47]\n\t"
    "v_exp_f32 v51, v51\n\t"
    "v_mfma_f32_16x16x32_bf16 v[172:175], a[144:147], v[112:115], v[172:175]\n\t"
    "s_cmp_lg_u64 s[86:87], 0\n\ts_cselect_b32 s81, 1, 0\n\ts_or_b32 s80, s80, s81\n\t"
    "ds_read_b128 a[120:123], v14 offset:12288\n\t"
    "v_cvt_pk_bf16_f32 v69, v50, v51\n\t"
    "v_mfma_f32_16x16x32_bf16 v[172:175], a[148:151], v[124:127], v[172:175]\n\t"
    "s_waitcnt lgkmcnt(4)\n\t"
    "v_cmp_ngt_f32 s[92:93], s99, v35\n\t"
    "v_add_f32 v52, v52, v8\n\t"
    "v_mfma_f32_16x16x32_bf16 v[172:175], a[152:155], v[136:139], v[172:175]\n\t"
    "s_waitcnt lgkmcnt(3)\n\t"
    "v_rcp_f32 v35, v35\n\t"
    "v_add_f32 v53, v53, v8\n\t"
    "v_mfma_f32_16x16x32_bf16 v[172:175], a[156:159], v[148:151], v[172:175]\n\t"
    "s_cmp_lg_u64 s[92:93], 0\n\ts_cselect_b32 s81, 8, 0\n\ts_or_b32 s80, s80, s81\n\t"
    "ds_read_b128 a[124:127], v15 offset:12288\n\t"
    "v_add_f32 v54, v54, v8\n\t"
    "v_mfma_f32_16x16x32_bf16 v[48:51], a[96:99], a[64:67], v[4:7]\n\t"
    "buffer_load_dwordx4 v25, s[44:47], s79 offen offset:1024 nt lds\n\t"
    "v_mfma_f32_16x16x32_bf16 v[48:51], a[100:103], a[68:71], v[48:51]\n\t"
    "ds_read_b64_tr_b16 a[128:129], v18 offset:32768\n\t"
    "v_add_f32 v55, v55, v8\n\t"
    "v_mfma_f32_16x16x32_bf16 v[48:51], a[104:107], a[72:75], v[48:51]\n\t"
    "v_exp_f32 v52, v52\n\t"
    "v_mfma_f32_16x16x32_bf16 v[48:51], a[108:111], a[76:79], v[48:51]\n\t"
    "ds_read_b64_tr_b16 a[130:131], v18 offset:34816\n\t"
    "v_cmp_ngt_f32 s[92:93], s99, v35\n\t"
    "v_mfma_f32_16x16x32_bf16 v[176:179], a[144:147], v[116:119], v[176:179]\n\t"
    "v_exp_f32 v53, v53\n\t"
    "v_mfma_f32_16x16x32_bf16 v[176:179], a[148:151], v[128:131], v[176:179]\n\t"
    "s_cmp_lg_u64 s[92:93], 0\n\ts_cselect_b32 s81, 1, 0\n\ts_or_b32 s80, s80, s81\n\t"
    "v_exp_f32 v54, v54\n\t"
    "v_mfma_f32_16x16x32_bf16 v[176:179], a[152:155], v[140:143], v[176:179]\n\t"
    "ds_read_b64_tr_b16 a[132:133], v18 offset:36864\n\t"
    "v_cvt_pk_bf16_f32 v72, v52, v53\n\t"
    "v_mfma_f32_16x16x32_bf16 v[176:179], a[156:159], v[152:155], v[176:179]\n\t"
    "v_exp_f32 v55, v55\n\t"
    "v_mfma_f32_16x16x32_bf16 a[164:167], a[172:175], v[116:119], a[164:167]\n\t"
    "ds_read_b64_tr_b16 a[134:135], v18 offset:38912\n\t"
    "v_cvt_pk_bf16_f32 v73, v54, v55\n\t"
    "v_mfma_f32_16x16x32_bf16 a[164:167], a[172:175], v[128:131], a[164:167]\n\t"
    "v_exp_f32 v56, v56\n\t"
    "v_mfma_f32_16x16x32_bf16 a[164:167], a[172:175], v[140:143], a[164:167]\n\t"
    "ds_read_b64_tr_b16 a[136:137], v18 offset:40960\n\t"
    "v_exp_f32 v57, v57\n\t"
    "v_mfma_f32_16x16x32_bf16 a[164:167], a[172:175], v[152:155], a[164:167]\n\t"
    "buffer_load_dwordx4 v26, s[44:47], s79 offen offset:2048 nt lds\n\t"
    "v_mfma_f32_16x16x32_bf16 v[52:55], a[96:99], a[80:83], v[8:11]\n\t"
    "v_exp_f32 v58, v58\n\t"
    "v_mfma_f32_16x16x32_bf16 v[52:55], a[100:103], a[84:87], v[52:55]\n\t"
    "ds_read_b64_tr_b16 a[138:139], v18 offset:43008\n\t"
    "v_cvt_pk_bf16_f32 v66, v56, v57\n\t"
    "v_mfma_f32_16x16x32_bf16 v[52:55], a[104:107], a[88:91], v[52:55]\n\t"
    "v_exp_f32 v59, v59\n\t"
    "v_mfma_f32_16x16x32_bf16 v[52:55], a[108:111], a[92:95], v[52:55]\n\t"
    "ds_read_b64_tr_b16 a[140:141], v18 offset:45056\n\t"
    "v_cvt_pk_bf16_f32 v67, v58, v59\n\t"
    "v_mfma_f32_16x16x32_bf16 v[180:183], a[144:147], v[120:123], v[180:183]\n\t"
    "v_exp_f32 v60, v60\n\t"
    "v_mfma_f32_16x16x32_bf16 v[180:183], a[148:151], v[132:135], v[180:183]\n\t"
    "v_exp_f32 v61, v61\n\t"
    "v_mfma_f32_16x16x32_bf16 v[180:183], a[152:155], v[144:147], v[180:183]\n\t"
    "ds_read_b64_tr_b16 a[142:143], v18 offset:47104\n\t"
    "v_cvt_pk_bf16_f32 v70, v60, v61\n\t"
    "v_mfma_f32_16x16x32_bf16 v[180:183], a[156:159], v[156:159], v[180:183]\n\t"
    "s_waitcnt lgkmcnt(3)\n\t"
    "v_exp_f32 v62, v62\n\t"
    "v_mfma_f32_16x16x32_bf16 v[56:59], a[112:115], a[48:51], v[0:3]\n\t"
    "ds_read_b128 a[96:99], v12 offset:16384\n\t"
    "v_accvgpr_read_b32 v36, a164\n\t"
    "v_mfma_f32_16x16x32_bf16 v[56:59], a[116:119], a[52:55], v[56:59]\n\t"
    "v_exp_f32 v63, v63\n\t"
    "v_mfma_f32_16x16x32_bf16 v[56:59], a[120:123], a[56:59], v[56:59]\n\t"
    "ds_read_b128 a[100:103], v13 offset:16384\n\t"
    "v_cvt_pk_bf16_f32 v71, v62, v63\n\t"
    "v_cmp_nlt_f32 s[88:89], v36, s54\n\t"
    "v_mfma_f32_16x16x32_bf16 v[56:59], a[124:127], a[60:63], v[56:59]\n\t"
    "s_waitcnt vmcnt(15)\n\t"
    "buffer_load_dwordx4 v27, s[44:47], s79 offen offset:3072 nt lds\n\t"
    "v_mfma_f32_16x16x32_bf16 v[184:187], a[128:131], v[112:115], v[184:187]\n\t"
    "s_cmp_lg_u64 s[88:89], 0\n\ts_cselect_b32 s81, 1, 0\n\ts_or_b32 s80, s80, s81\n\t"
    "ds_read_b128 a[104:107], v14 offset:16384\n\t"
    "v_cmp_ngt_f32 s[92:93], s99, v36\n\t"
    "v_mfma_f32_16x16x32_bf16 v[184:187], a[132:135], v[124:127], v[184:187]\n\t"
    "s_waitcnt lgkmcnt(4)\n\t"
    "ds_read_b128 a[0:3], v28 offset:0\n\t"
    "v_mul_f32 v160, v35, v160\n\t"
    "v_mfma_f32_16x16x32_bf16 v[184:187], a[136:139], v[136:139], v[184:187]\n\t"
    "s_waitcnt lgkmcnt(4)\n\t"
    "s_cmp_lg_u64 s[92:93], 0\n\ts_cselect_b32 s81, 8, 0\n\ts_or_b32 s80, s80, s81\n\t"
    "ds_read_b128 a[108:111], v15 offset:16384\n\t"
    "v_mul_f32 v161, v35, v161\n\t"
    "v_mfma_f32_16x16x32_bf16 v[184:187], a[140:143], v[148:151], v[184:187]\n\t"
    "v_rcp_f32 v36, v36\n\t"
    "v_mfma_f32_16x16x32_bf16 v[60:63], a[112:115], a[64:67], v[4:7]\n\t"
    "ds_read_b128 a[4:7], v29 offset:0\n\t"
    "v_cmp_ngt_f32 s[92:93], s99, v36\n\t"
    "v_mfma_f32_16x16x32_bf16 v[60:63], a[116:119], a[68:71], v[60:63]\n\t"
    "ds_read_b64_tr_b16 a[144:145], v19 offset:32768\n\t"
    "v_mul_f32 v162, v35, v162\n\t"
    "v_mfma_f32_16x16x32_bf16 v[60:63], a[120:123], a[72:75], v[60:63]\n\t"
    "s_cmp_lg_u64 s[92:93], 0\n\ts_cselect_b32 s81, 1, 0\n\ts_or_b32 s80, s80, s81\n\t"
    "v_exp_f32 v40, v40\n\t"
    "v_mfma_f32_16x16x32_bf16 v[60:63], a[124:127], a[76:79], v[60:63]\n\t"
    "ds_read_b64_tr_b16 a[146:147], v19 offset:34816\n\t"
    "v_exp_f32 v41, v41\n\t"
    "v_mfma_f32_16x16x32_bf16 v[188:191], a[128:131], v[116:119], v[188:191]\n\t"
    "v_exp_f32 v42, v42\n\t"
    "v_mfma_f32_16x16x32_bf16 v[188:191], a[132:135], v[128:131], v[188:191]\n\t"
    "ds_read_b64_tr_b16 a[148:149], v19 offset:36864\n\t"
    "v_cvt_pk_bf16_f32 v74, v40, v41\n\t"
    "v_mfma_f32_16x16x32_bf16 v[188:191], a[136:139], v[140:143], v[188:191]\n\t"
    "v_exp_f32 v43, v43\n\t"
    "v_mfma_f32_16x16x32_bf16 v[188:191], a[140:143], v[152:155], v[188:191]\n\t"
    "ds_read_b64_tr_b16 a[150:151], v19 offset:38912\n\t"
    "v_cvt_pk_bf16_f32 v75, v42, v43\n\t"
    "v_mfma_f32_16x16x32_bf16 a[168:171], a[172:175], v[120:123], a[168:171]\n\t"
    "ds_read_b128 a[8:11], v30 offset:0\n\t"
    "v_mul_f32 v163, v35, v163\n\t"
    "v_mfma_f32_16x16x32_bf16 a[168:171], a[172:175], v[132:135], a[168:171]\n\t"
    "ds_read_b128 a[12:15], v31 offset:0\n\t"
    "v_cvt_pk_bf16_f32 v160, v160, v161\n\t"
    "v_mfma_f32_16x16x32_bf16 a[168:171], a[172:175], v[144:147], a[168:171]\n\t"
    "ds_read_b64_tr_b16 a[152:153], v19 offset:40960\n\t"
    "v_exp_f32 v44, v44\n\t"
    "v_mfma_f32_16x16x32_bf16 a[168:171], a[172:175], v[156:159], a[168:171]\n\t"
    "v_exp_f32 v45, v45\n\t"
    "v_mfma_f32_16x16x32_bf16 v[40:43], a[112:115], a[80:83], v[8:11]\n\t"
    "ds_read_b64_tr_b16 a[154:155], v19 offset:43008\n\t"
    "v_cvt_pk_bf16_f32 v76, v44, v45\n\t"
    "v_mfma_f32_16x16x32_bf16 v[40:43], a[116:119], a[84:87], v[40:43]\n\t"
    "v_exp_f32 v46, v46\n\t"
    "v_mfma_f32_16x16x32_bf16 v[40:43], a[120:123], a[88:91], v[40:43]\n\t"
    "ds_read_b64_tr_b16 a[156:157], v19 offset:45056\n\t"
    "v_cvt_pk_bf16_f32 v161, v162, v163\n\t"
    "v_mfma_f32_16x16x32_bf16 v[40:43], a[124:127], a[92:95], v[40:43]\n\t"
    "v_exp_f32 v47, v47\n\t"
    "v_mul_f32 v172, v35, v172\n\t"
    "v_mfma_f32_16x16x32_bf16 v[192:195], a[128:131], v[120:123], v[192:195]\n\t"
    "v_cvt_pk_bf16_f32 v77, v46, v47\n\t"
    "v_accvgpr_read_b32 v37, a168\n\t"
    "v_mfma_f32_16x16x32_bf16 v[192:195], a[132:135], v[132:135], v[192:195]\n\t"
    "ds_read_b64_tr_b16 a[158:159], v19 offset:47104\n\t"
    "v_cmp_nlt_f32 s[90:91], v37, s54\n\t"
    "v_mfma_f32_16x16x32_bf16 v[192:195], a[136:139], v[144:147], v[192:195]\n\t"
    "v_exp_f32 v48, v48\n\t"
    "v_mfma_f32_16x16x32_bf16 v[192:195], a[140:143], v[156:159], v[192:195]\n\t"
    "s_waitcnt lgkmcnt(3)\n\t"
    "s_cmp_lg_u64 s[90:91], 0\n\ts_cselect_b32 s81, 1, 0\n\ts_or_b32 s80, s80, s81\n\t"
    "v_exp_f32 v49, v49\n\t"
    "v_cmp_ngt_f32 s[92:93], s99, v37\n\t"
    "v_mfma_f32_16x16x32_bf16 v[44:47], a[96:99], a[48:51], v[0:3]\n\t"
    "v_exp_f32 v50, v50\n\t"
    "v_mfma_f32_16x16x32_bf16 v[44:47], a[100:103], a[52:55], v[44:47]\n\t"
    "s_cmp_lg_u64 s[92:93], 0\n\ts_cselect_b32 s81, 8, 0\n\ts_or_b32 s80, s80, s81\n\t"
    "v_exp_f32 v51, v51\n\t"
    "v_mfma_f32_16x16x32_bf16 v[44:47], a[104:107], a[56:59], v[44:47]\n\t"
    "v_cvt_pk_bf16_f32 v80, v48, v49\n\t"
    "v_cvt_pk_bf16_f32 v81, v50, v51\n\t"
    "v_mfma_f32_16x16x32_bf16 v[44:47], a[108:111], a[60:63], v[44:47]\n\t"
    "v_exp_f32 v52, v52\n\t"
    "v_mul_f32 v173, v35, v173\n\t"
    "v_mfma_f32_16x16x32_bf16 v[196:199], a[144:147], v[112:115], v[196:199]\n\t"
    "v_exp_f32 v53, v53\n\t"
    "v_mfma_f32_16x16x32_bf16 v[196:199], a[148:151], v[124:127], v[196:199]\n\t"
    "s_waitcnt lgkmcnt(0)\n\t"
    "v_exp_f32 v54, v54\n\t"
    "v_mfma_f32_16x16x32_bf16 v[196:199], a[152:155], v[136:139], v[196:199]\n\t"
    "s_waitcnt vmcnt(20) lgkmcnt(0)\n\t"
    "v_exp_f32 v55, v55\n\t"
    "v_mfma_f32_16x16x32_bf16 v[196:199], a[156:159], v[148:151], v[196:199]\n\t"
    "s_barrier\n\t"
    "ds_read_b128 a[112:115], v12 offset:20480\n\t"
    "v_cvt_pk_bf16_f32 v84, v52, v53\n\t"
    "v_cvt_pk_bf16_f32 v85, v54, v55\n\t"
    "v_mfma_f32_16x16x32_bf16 v[48:51], a[96:99], a[64:67], v[4:7]\n\t"
    "s_mov_b32 m0, s85\n\t"
    "ds_read_b128 a[116:119], v13 offset:20480\n\t"
    "v_mul_f32 v174, v35, v174\n\t"
    "v_mfma_f32_16x16x32_bf16 v[48:51], a[100:103], a[68:71], v[48:51]\n\t"
    "v_rcp_f32 v37, v37\n\t"
    "v_mfma_f32_16x16x32_bf16 v[48:51], a[104:107], a[72:75], v[48:51]\n\t"
    "ds_read_b128 a[120:123], v14 offset:20480\n\t"
    "v_exp_f32 v56, v56\n\t"
    "v_mfma_f32_16x16x32_bf16 v[48:51], a[108:111], a[76:79], v[48:51]\n\t"
    "buffer_load_dwordx4 v20, s[36:39], s71 offen offset:0 nt lds\n\t"
    "v_mfma_f32_16x16x32_bf16 v[200:203], a[144:147], v[116:119], v[200:203]\n\t"
    "ds_read_b128 a[124:127], v15 offset:20480\n\t"
    "v_exp_f32 v57, v57\n\t"
    "v_mfma_f32_16x16x32_bf16 v[200:203], a[148:151], v[128:131], v[200:203]\n\t"
    "ds_read_b64_tr_b16 a[128:129], v16 offset:49152\n\t"
    "v_cvt_pk_bf16_f32 v78, v56, v57\n\t"
    "v_mfma_f32_16x16x32_bf16 v[200:203], a[152:155], v[140:143], v[200:203]\n\t"
    "v_exp_f32 v58, v58\n\t"
    "v_mfma_f32_16x16x32_bf16 v[200:203], a[156:159], v[152:155], v[200:203]\n\t"
    "ds_read_b64_tr_b16 a[130:131], v16 offset:51200\n\t"
    "v_exp_f32 v59, v59\n\t"
    "v_mfma_f32_16x16x32_bf16 v[52:55], a[96:99], a[80:83], v[8:11]\n\t"
    "v_cvt_pk_bf16_f32 v79, v58, v59\n\t"
    "v_cmp_ngt_f32 s[92:93], s99, v37\n\t"
    "v_mfma_f32_16x16x32_bf16 v[52:55], a[100:103], a[84:87], v[52:55]\n\t"
    "ds_read_b64_tr_b16 a[132:133], v16 offset:53248\n\t"
    "v_mul_f32 v175, v35, v175\n\t"
    "v_mfma_f32_16x16x32_bf16 v[52:55], a[104:107], a[88:91], v[52:55]\n\t"
    "s_cmp_lg_u64 s[92:93], 0\n\ts_cselect_b32 s81, 1, 0\n\ts_or_b32 s80, s80, s81\n\t"
    "v_exp_f32 v60, v60\n\t"
    "v_cvt_pk_bf16_f32 v172, v172, v173\n\t"
    "v_mfma_f32_16x16x32_bf16 v[52:55], a[108:111], a[92:95], v[52:55]\n\t"
    "ds_read_b64_tr_b16 a[134:135], v16 offset:55296\n\t"
    "v_exp_f32 v61, v61\n\t"
    "v_mfma_f32_16x16x32_bf16 v[56:59], a[228:231], a[0:3], 0\n\t"
    "v_exp_f32 v62, v62\n\t"
    "v_exp_f32 v63, v63\n\t"
    "v_mfma_f32_16x16x32_bf16 v[56:59], a[232:235], a[0:3], v[56:59]\n\t"
    "v_cvt_pk_bf16_f32 v82, v60, v61\n\t"
    "v_cvt_pk_bf16_f32 v83, v62, v63\n\t"
    "v_mfma_f32_16x16x32_bf16 v[60:63], a[236:239], a[0:3], 0\n\t"
    "ds_read_b64_tr_b16 a[136:137], v16 offset:57344\n\t"
    "v_exp_f32 v40, v40\n\t"
    "v_mfma_f32_16x16x32_bf16 v[60:63], a[240:243], a[0:3], v[60:63]\n\t"
    "ds_read_b64_tr_b16 a[138:139], v16 offset:59392\n\t"
    "v_cvt_pk_bf16_f32 v173, v174, v175\n\t"
    "v_mfma_f32_16x16x32_bf16 v[204:207], a[144:147], v[120:123], v[204:207]\n\t"
    "ds_read_b64_tr_b16 a[140:141], v16 offset:61440\n\t"
    "v_exp_f32 v41, v41\n\t"
    "v_mfma_f32_16x16x32_bf16 v[204:207], a[148:151], v[132:135], v[204:207]\n\t"
    "v_exp_f32 v42, v42\n\t"
    "v_mfma_f32_16x16x32_bf16 v[204:207], a[152:155], v[144:147], v[204:207]\n\t"
    "ds_read_b64_tr_b16 a[142:143], v16 offset:63488\n\t"
    "v_exp_f32 v43, v43\n\t"
    "v_mfma_f32_16x16x32_bf16 v[204:207], a[156:159], v[156:159], v[204:207]\n\t"
    "s_waitcnt lgkmcnt(4)\n\t"
    "v_cvt_pk_bf16_f32 v86, v40, v41\n\t"
    "v_cvt_pk_bf16_f32 v87, v42, v43\n\t"
    "v_cvt_pk_bf16_f32 v56, v56, v57\n\t"
    "v_mfma_f32_16x16x32_bf16 v[40:43], a[112:115], a[48:51], v[0:3]\n\t"
    "buffer_load_dwordx4 v21, s[36:39], s71 offen offset:1024 nt lds\n\t"
    "v_mfma_f32_16x16x32_bf16 v[40:43], a[116:119], a[52:55], v[40:43]\n\t"
    "ds_read_b128 a[96:99], v12 offset:24576\n\t"
    "v_cvt_pk_bf16_f32 v58, v58, v59\n\t"
    "v_mfma_f32_16x16x32_bf16 v[40:43], a[120:123], a[56:59], v[40:43]\n\t"
    "ds_read_b128 a[100:103], v13 offset:24576\n\t"
    "v_exp_f32 v44, v44\n\t"
    "v_mfma_f32_16x16x32_bf16 v[40:43], a[124:127], a[60:63], v[40:43]\n\t"
    "v_exp_f32 v45, v45\n\t"
    "v_mfma_f32_16x16x32_bf16 v[208:211], a[128:131], v[112:115], v[208:211]\n\t"
    "ds_read_b128 a[104:107], v14 offset:24576\n\t"
    "v_exp_f32 v46, v46\n\t"
    "v_mfma_f32_16x16x32_bf16 v[208:211], a[132:135], v[124:127], v[208:211]\n\t"
    "s_waitcnt lgkmcnt(4)\n\t"
    "v_exp_f32 v47, v47\n\t"
    "v_mfma_f32_16x16x32_bf16 v[208:211], a[136:139], v[136:139], v[208:211]\n\t"
    "s_waitcnt lgkmcnt(3)\n\t"
    "v_cvt_pk_bf16_f32 v88, v44, v45\n\t"
    "v_cvt_pk_bf16_f32 v89, v46, v47\n\t"
    "v_mfma_f32_16x16x32_bf16 v[208:211], a[140:143], v[148:151], v[208:211]\n\t"
    "ds_read_b128 a[108:111], v15 offset:24576\n\t"
    "v_exp_f32 v48, v48\n\t"
    "v_mfma_f32_16x16x32_bf16 v[44:47], a[112:115], a[64:67], v[4:7]\n\t"
    "buffer_load_dwordx4 v22, s[36:39], s71 offen offset:2048 nt lds\n\t"
    "v_mfma_f32_16x16x32_bf16 v[44:47], a[116:119], a[68:71], v[44:47]\n\t"
    "ds_read_b64_tr_b16 a[144:145], v17 offset:49152\n\t"
    "v_exp_f32 v49, v49\n\t"
    "v_mfma_f32_16x16x32_bf16 v[44:47], a[120:123], a[72:75], v[44:47]\n\t"
    "ds_read_b64_tr_b16 a[146:147], v17 offset:51200\n\t"
    "v_cvt_pk_bf16_f32 v92, v48, v49\n\t"
    "v_mfma_f32_16x16x32_bf16 v[44:47], a[124:127], a[76:79], v[44:47]\n\t"
    "v_exp_f32 v50, v50\n\t"
    "v_accvgpr_write_b32 a0, v56\n\t"
    "v_mfma_f32_16x16x32_bf16 v[212:215], a[128:131], v[116:119], v[212:215]\n\t"
    "ds_read_b64_tr_b16 a[148:149], v17 offset:53248\n\t"
    "v_accvgpr_write_b32 a1, v58\n\t"
    "v_mfma_f32_16x16x32_bf16 v[212:215], a[132:135], v[128:131], v[212:215]\n\t"
    "v_exp_f32 v51, v51\n\t"
    "v_cvt_pk_bf16_f32 v60, v60, v61\n\t"
    "v_mfma_f32_16x16x32_bf16 v[212:215], a[136:139], v[140:143], v[212:215]\n\t"
    "ds_read_b64_tr_b16 a[150:151], v17 offset:55296\n\t"
    "v_cvt_pk_bf16_f32 v93, v50, v51\n\t"
    "v_mfma_f32_16x16x32_bf16 v[212:215], a[140:143], v[152:155], v[212:215]\n\t"
    "buffer_load_dwordx4 v23, s[36:39], s71 offen offset:3072 nt lds\n\t"
    "v_cvt_pk_bf16_f32 v62, v62, v63\n\t"
    "v_mfma_f32_16x16x32_bf16 v[48:51], a[112:115], a[80:83], v[8:11]\n\t"
    "ds_read_b64_tr_b16 a[152:153], v17 offset:57344\n\t"
    "v_accvgpr_write_b32 a2, v60\n\t"
    "v_mfma_f32_16x16x32_bf16 v[48:51], a[116:119], a[84:87], v[48:51]\n\t"
    "v_exp_f32 v52, v52\n\t"
    "v_accvgpr_write_b32 a3, v62\n\t"
    "v_mfma_f32_16x16x32_bf16 v[48:51], a[120:123], a[88:91], v[48:51]\n\t"
    "ds_read_b64_tr_b16 a[154:155], v17 offset:59392\n\t"
    "v_mul_f32 v184, v35, v184\n\t"
    "v_mfma_f32_16x16x32_bf16 v[48:51], a[124:127], a[92:95], v[48:51]\n\t"
    "v_exp_f32 v53, v53\n\t"
    "v_mul_f32 v185, v35, v185\n\t"
    "v_mfma_f32_16x16x32_bf16 v[216:219], a[128:131], v[120:123], v[216:219]\n\t"
    "s_add_u32 s78, s56, 69632\n\t"
    "ds_read_b64_tr_b16 a[156:157], v17 offset:61440\n\t"
    "v_cvt_pk_bf16_f32 v96, v52, v53\n\t"
    "v_mfma_f32_16x16x32_bf16 v[216:219], a[132:135], v[132:135], v[216:219]\n\t"
    "s_mov_b32 m0, s73\n\t"
    "v_exp_f32 v54, v54\n\t"
    "v_mul_f32 v186, v35, v186\n\t"
    "v_mfma_f32_16x16x32_bf16 v[216:219], a[136:139], v[144:147], v[216:219]\n\t"
    "ds_read_b64_tr_b16 a[158:159], v17 offset:63488\n\t"
    "v_exp_f32 v55, v55\n\t"
    "v_mfma_f32_16x16x32_bf16 v[216:219], a[140:143], v[156:159], v[216:219]\n\t"
    "s_waitcnt lgkmcnt(4)\n\t"
    "v_cvt_pk_bf16_f32 v97, v54, v55\n\t"
    "v_mul_f32 v187, v35, v187\n\t"
    "v_mfma_f32_16x16x32_bf16 v[52:55], a[96:99], a[48:51], v[0:3]\n\t"
    "ds_read_b128 a[112:115], v12 offset:28672\n\t"
    "v_exp_f32 v40, v40\n\t"
    "v_mfma_f32_16x16x32_bf16 v[52:55], a[100:103], a[52:55], v[52:55]\n\t"
    "buffer_load_dwordx4 v20, s[40:43], s78 offen offset:0 nt lds\n\t"
    "v_mfma_f32_16x16x32_bf16 v[52:55], a[104:107], a[56:59], v[52:55]\n\t"
    "ds_read_b128 a[116:119], v13 offset:28672\n\t"
    "v_exp_f32 v41, v41\n\t"
    "v_mfma_f32_16x16x32_bf16 v[52:55], a[108:111], a[60:63], v[52:55]\n\t"
    "v_exp_f32 v42, v42\n\t"
    "v_cvt_pk_bf16_f32 v90, v40, v41\n\t"
    "v_mfma_f32_16x16x32_bf16 v[220:223], a[144:147], v[112:115], v[220:223]\n\t"
    "ds_read_b128 a[120:123], v14 offset:28672\n\t"
    "v_cvt_pk_bf16_f32 v184, v184, v185\n\t"
    "v_mfma_f32_16x16x32_bf16 v[220:223], a[148:151], v[124:127], v[220:223]\n\t"
    "s_waitcnt lgkmcnt(4)\n\t"
    "v_exp_f32 v43, v43\n\t"
    "v_cvt_pk_bf16_f32 v185, v186, v187\n\t"
    "v_mfma_f32_16x16x32_bf16 v[220:223], a[152:155], v[136:139], v[220:223]\n\t"
    "s_waitcnt lgkmcnt(3)\n\t"
    "v_cvt_pk_bf16_f32 v91, v42, v43\n\t"
    "v_mul_f32 v196, v35, v196\n\t"
    "v_mfma_f32_16x16x32_bf16 v[220:223], a[156:159], v[148:151], v[220:223]\n\t"
    "ds_read_b128 a[124:127], v15 offset:28672\n\t"
    "v_exp_f32 v44, v44\n\t"
    "v_mfma_f32_16x16x32_bf16 v[56:59], a[96:99], a[64:67], v[4:7]\n\t"
    "buffer_load_dwordx4 v21, s[40:43], s78 offen offset:1024 nt lds\n\t"
    "v_mfma_f32_16x16x32_bf16 v[56:59], a[100:103], a[68:71], v[56:59]\n\t"
    "ds_read_b64_tr_b16 a[128:129], v18 offset:49152\n\t"
    "v_exp_f32 v45, v45\n\t"
    "v_mfma_f32_16x16x32_bf16 v[56:59], a[104:107], a[72:75], v[56:59]\n\t"
    "v_exp_f32 v46, v46\n\t"
    "v_cvt_pk_bf16_f32 v94, v44, v45\n\t"
    "v_mfma_f32_16x16x32_bf16 v[56:59], a[108:111], a[76:79], v[56:59]\n\t"
    "ds_read_b64_tr_b16 a[130:131], v18 offset:51200\n\t"
    "v_mul_f32 v197, v35, v197\n\t"
    "v_mfma_f32_16x16x32_bf16 v[224:227], a[144:147], v[116:119], v[224:227]\n\t"
    "v_exp_f32 v47, v47\n\t"
    "v_mul_f32 v198, v35, v198\n\t"
    "v_mfma_f32_16x16x32_bf16 v[224:227], a[148:151], v[128:131], v[224:227]\n\t"
    "v_cvt_pk_bf16_f32 v95, v46, v47\n\t"
    "v_mul_f32 v199, v35, v199\n\t"
    "v_mfma_f32_16x16x32_bf16 v[224:227], a[152:155], v[140:143], v[224:227]\n\t"
    "ds_read_b64_tr_b16 a[132:133], v18 offset:53248\n\t"
    "v_exp_f32 v48, v48\n\t"
    "v_mfma_f32_16x16x32_bf16 v[224:227], a[156:159], v[152:155], v[224:227]\n\t"
    "v_exp_f32 v49, v49\n\t"
    "v_mfma_f32_16x16x32_bf16 v[60:63], a[96:99], a[80:83], v[8:11]\n\t"
    "ds_read_b64_tr_b16 a[134:135], v18 offset:55296\n\t"
    "v_exp_f32 v50, v50\n\t"
    "v_mfma_f32_16x16x32_bf16 v[60:63], a[100:103], a[84:87], v[60:63]\n\t"
    "v_exp_f32 v51, v51\n\t"
    "v_cvt_pk_bf16_f32 v98, v48, v49\n\t"
    "v_mfma_f32_16x16x32_bf16 v[60:63], a[104:107], a[88:91], v[60:63]\n\t"
    "ds_read_b64_tr_b16 a[136:137], v18 offset:57344\n\t"
    "v_cvt_pk_bf16_f32 v99, v50, v51\n\t"
    "v_mfma_f32_16x16x32_bf16 v[60:63], a[108:111], a[92:95], v[60:63]\n\t"
    "buffer_load_dwordx4 v22, s[40:43], s78 offen offset:2048 nt lds\n\t"
    "v_cvt_pk_bf16_f32 v196, v196, v197\n\t"
    "v_mfma_f32_16x16x32_bf16 v[40:43], a[228:231], a[4:7], 0\n\t"
    "v_exp_f32 v52, v52\n\t"
    "v_mfma_f32_16x16x32_bf16 v[40:43], a[232:235], a[4:7], v[40:43]\n\t"
    "ds_read_b64_tr_b16 a[138:139], v18 offset:59392\n\t"
    "v_exp_f32 v53, v53\n\t"
    "v_mfma_f32_16x16x32_bf16 v[44:47], a[236:239], a[4:7], 0\n\t"
    "v_exp_f32 v54, v54\n\t"
    "v_mfma_f32_16x16x32_bf16 v[44:47], a[240:243], a[4:7], v[44:47]\n\t"
    "ds_read_b64_tr_b16 a[140:141], v18 offset:61440\n\t"
    "v_exp_f32 v55, v55\n\t"
    "v_mfma_f32_16x16x32_bf16 v[228:231], a[144:147], v[120:123], v[228:231]\n\t"
    "ds_write_b64 v32, v[160:161] offset:4096\n\t"
    "v_mfma_f32_16x16x32_bf16 v[228:231], a[148:151], v[132:135], v[228:231]\n\t"
    "v_cvt_pk_bf16_f32 v100, v52, v53\n\t"
    "v_cvt_pk_bf16_f32 v101, v54, v55\n\t"
    "v_mfma_f32_16x16x32_bf16 v[228:231], a[152:155], v[144:147], v[228:231]\n\t"
    "ds_read_b64_tr_b16 a[142:143], v18 offset:63488\n\t"
    "v_exp_f32 v56, v56\n\t"
    "v_mfma_f32_16x16x32_bf16 v[228:231], a[156:159], v[156:159], v[228:231]\n\t"
    "s_waitcnt lgkmcnt(4)\n\t"
    "v_exp_f32 v57, v57\n\t"
    "v_mfma_f32_16x16x32_bf16 v[48:51], a[112:115], a[48:51], v[0:3]\n\t"
    "ds_read_b128 a[96:99], v12 offset:32768\n\t"
    "v_exp_f32 v58, v58\n\t"
    "v_mfma_f32_16x16x32_bf16 v[48:51], a[116:119], a[52:55], v[48:51]\n\t"
    "ds_read_b128 a[100:103], v13 offset:32768\n\t"
    "v_cvt_pk_bf16_f32 v104, v56, v57\n\t"
    "v_mfma_f32_16x16x32_bf16 v[48:51], a[120:123], a[56:59], v[48:51]\n\t"
    "buffer_load_dwordx4 v23, s[40:43], s78 offen offset:3072 nt lds\n\t"
    "v_cvt_pk_bf16_f32 v197, v198, v199\n\t"
    "v_mfma_f32_16x16x32_bf16 v[48:51], a[124:127], a[60:63], v[48:51]\n\t"
    "ds_read_b128 a[104:107], v14 offset:32768\n\t"
    "v_cvt_pk_bf16_f32 v40, v40, v41\n\t"
    "v_mfma_f32_16x16x32_bf16 v[232:235], a[128:131], v[112:115], v[232:235]\n\t"
    "v_exp_f32 v59, v59\n\t"
    "v_cvt_pk_bf16_f32 v42, v42, v43\n\t"
    "v_mfma_f32_16x16x32_bf16 v[232:235], a[132:135], v[124:127], v[232:235]\n\t"
    "s_waitcnt lgkmcnt(4)\n\t"
    "ds_read_b128 a[108:111], v15 offset:32768\n\t"
    "v_cvt_pk_bf16_f32 v105, v58, v59\n\t"
    "v_accvgpr_write_b32 a4, v40\n\t"
    "v_mfma_f32_16x16x32_bf16 v[232:235], a[136:139], v[136:139], v[232:235]\n\t"
    "s_waitcnt lgkmcnt(4)\n\t"
    "ds_write_b64 v32, v[172:173] offset:4128\n\t"
    "v_mfma_f32_16x16x32_bf16 v[232:235], a[140:143], v[148:151], v[232:235]\n\t"
    "v_exp_f32 v60, v60\n\t"
    "v_mfma_f32_16x16x32_bf16 v[52:55], a[112:115], a[64:67], v[4:7]\n\t"
    "s_add_u32 s79, s62, 32768\n\t"
    "ds_read_b64_tr_b16 a[144:145], v19 offset:49152\n\t"
    "v_accvgpr_write_b32 a5, v42\n\t"
    "v_mfma_f32_16x16x32_bf16 v[52:55], a[116:119], a[68:71], v[52:55]\n\t"
    "s_add_u32 m0, s77, 32768\n\t"
    "ds_read_b64_tr_b16 a[146:147], v19 offset:51200\n\t"
    "v_exp_f32 v61, v61\n\t"
    "v_mfma_f32_16x16x32_bf16 v[52:55], a[120:123], a[72:75], v[52:55]\n\t"
    "buffer_load_dwordx4 v24, s[44:47], s79 offen offset:0 nt lds\n\t"
    "v_mfma_f32_16x16x32_bf16 v[52:55], a[124:127], a[76:79], v[52:55]\n\t"
    "ds_read_b64_tr_b16 a[148:149], v19 offset:53248\n\t"
    "v_exp_f32 v62, v62\n\t"
    "v_mfma_f32_16x16x32_bf16 v[236:239], a[128:131], v[116:119], v[236:239]\n\t"
    "v_exp_f32 v63, v63\n\t"
    "v_cvt_pk_bf16_f32 v108, v60, v61\n\t"
    "v_mfma_f32_16x16x32_bf16 v[236:239], a[132:135], v[128:131], v[236:239]\n\t"
    "ds_read_b64_tr_b16 a[150:151], v19 offset:55296\n\t"
    "v_cvt_pk_bf16_f32 v109, v62, v63\n\t"
    "v_mfma_f32_16x16x32_bf16 v[236:239], a[136:139], v[140:143], v[236:239]\n\t"
    "ds_write_b64 v32, v[184:185] offset:4160\n\t"
    "v_mfma_f32_16x16x32_bf16 v[236:239], a[140:143], v[152:155], v[236:239]\n\t"
    "v_mul_f32 v164, v36, v164\n\t"
    "v_mul_f32 v165, v36, v165\n\t"
    "v_mfma_f32_16x16x32_bf16 v[56:59], a[112:115], a[80:83], v[8:11]\n\t"
    "ds_read_b64_tr_b16 a[152:153], v19 offset:57344\n\t"
    "v_mul_f32 v166, v36, v166\n\t"
    "v_mul_f32 v167, v36, v167\n\t"
    "v_mfma_f32_16x16x32_bf16 v[56:59], a[116:119], a[84:87], v[56:59]\n\t"
    "ds_read_b64_tr_b16 a[154:155], v19 offset:59392\n\t"
    "v_cvt_pk_bf16_f32 v164, v164, v165\n\t"
    "v_mfma_f32_16x16x32_bf16 v[56:59], a[120:123], a[88:91], v[56:59]\n\t"
    "buffer_load_dwordx4 v25, s[44:47], s79 offen offset:1024 nt lds\n\t"
    "v_cvt_pk_bf16_f32 v165, v166, v167\n\t"
    "v_mfma_f32_16x16x32_bf16 v[56:59], a[124:127], a[92:95], v[56:59]\n\t"
    "ds_read_b64_tr_b16 a[156:157], v19 offset:61440\n\t"
    "v_cvt_pk_bf16_f32 v44, v44, v45\n\t"
    "v_cvt_pk_bf16_f32 v46, v46, v47\n\t"
    "v_mfma_f32_16x16x32_bf16 v[240:243], a[128:131], v[120:123], v[240:243]\n\t"
    "v_accvgpr_write_b32 a6, v44\n\t"
    "v_accvgpr_write_b32 a7, v46\n\t"
    "v_mfma_f32_16x16x32_bf16 v[240:243], a[132:135], v[132:135], v[240:243]\n\t"
    "ds_read_b64_tr_b16 a[158:159], v19 offset:63488\n\t"
    "v_exp_f32 v48, v48\n\t"
    "v_mfma_f32_16x16x32_bf16 v[240:243], a[136:139], v[144:147], v[240:243]\n\t"
    "ds_write_b64 v32, v[196:197] offset:4192\n\t"
    "v_mfma_f32_16x16x32_bf16 v[240:243], a[140:143], v[156:159], v[240:243]\n\t"
    "s_waitcnt lgkmcnt(5)\n\t"
    "v_exp_f32 v49, v49\n\t"
    "v_mfma_f32_16x16x32_bf16 v[60:63], a[96:99], a[48:51], v[0:3]\n\t"
    "ds_read_b128 a[176:179], v34 offset:4096\n\t"
    "v_exp_f32 v50, v50\n\t"
    "v_mfma_f32_16x16x32_bf16 v[60:63], a[100:103], a[52:55], v[60:63]\n\t"
    "ds_read_b128 a[180:183], v34 offset:5248\n\t"
    "v_cvt_pk_bf16_f32 v102, v48, v49\n\t"
    "v_mfma_f32_16x16x32_bf16 v[60:63], a[104:107], a[56:59], v[60:63]\n\t"
    "buffer_load_dwordx4 v26, s[44:47], s79 offen offset:2048 nt lds\n\t"
    "v_mul_f32 v176, v36, v176\n\t"
    "v_mfma_f32_16x16x32_bf16 v[60:63], a[108:111], a[60:63], v[60:63]\n\t"
    "v_exp_f32 v51, v51\n\t"
    "v_mul_f32 v177, v36, v177\n\t"
    "v_mfma_f32_16x16x32_bf16 v[244:247], a[144:147], v[112:115], v[244:247]\n\t"
    "v_cvt_pk_bf16_f32 v103, v50, v51\n\t"
    "v_mul_f32 v178, v36, v178\n\t"
    "v_mfma_f32_16x16x32_bf16 v[244:247], a[148:151], v[124:127], v[244:247]\n\t"
    "s_waitcnt lgkmcnt(3)\n\t"
    "v_mul_f32 v179, v36, v179\n\t"
    "v_exp_f32 v52, v52\n\t"
    "v_mfma_f32_16x16x32_bf16 v[244:247], a[152:155], v[136:139], v[244:247]\n\t"
    "s_waitcnt vmcnt(19) lgkmcnt(3)\n\t"
    "ds_write_b64 v32, v[164:165] offset:4096\n\t"
    "v_mfma_f32_16x16x32_bf16 v[244:247], a[156:159], v[148:151], v[244:247]\n\t"
    "s_barrier\n\t"
    "v_exp_f32 v53, v53\n\t"
    "v_mfma_f32_16x16x32_bf16 v[40:43], a[96:99], a[64:67], v[4:7]\n\t"
    "ds_read_b128 a[112:115], v12 offset:36864\n\t"
    "v_exp_f32 v54, v54\n\t"
    "v_mfma_f32_16x16x32_bf16 v[40:43], a[100:103], a[68:71], v[40:43]\n\t"
    "ds_read_b128 a[116:119], v13 offset:36864\n\t"
    "v_exp_f32 v55, v55\n\t"
    "v_mfma_f32_16x16x32_bf16 v[40:43], a[104:107], a[72:75], v[40:43]\n\t"
    "ds_read_b128 a[120:123], v14 offset:36864\n\t"
    "v_cvt_pk_bf16_f32 v176, v176, v177\n\t"
    "v_mfma_f32_16x16x32_bf16 v[40:43], a[108:111], a[76:79], v[40:43]\n\t"
    "buffer_load_dwordx4 v27, s[44:47], s79 offen offset:3072 nt lds\n\t"
    "v_cvt_pk_bf16_f32 v177, v178, v179\n\t"
    "v_mfma_f32_16x16x32_bf16 v[248:251], a[144:147], v[116:119], v[248:251]\n\t"
    "s_add_u32 s78, s56, 86016\n\t"
    "ds_read_b128 a[124:127], v15 offset:36864\n\t"
    "v_cvt_pk_bf16_f32 v106, v52, v53\n\t"
    "v_cvt_pk_bf16_f32 v107, v54, v55\n\t"
    "v_mfma_f32_16x16x32_bf16 v[248:251], a[148:151], v[128:131], v[248:251]\n\t"
    "s_mov_b32 m0, s74\n\t"
    "ds_read_b64_tr_b16 a[128:129], v16 offset:0\n\t"
    "v_mul_f32 v188, v36, v188\n\t"
    "v_mfma_f32_16x16x32_bf16 v[248:251], a[152:155], v[140:143], v[248:251]\n\t"
    "buffer_load_dwordx4 v20, s[40:43], s78 offen offset:0 nt lds\n\t"
    "v_mul_f32 v189, v36, v189\n\t"
    "v_mfma_f32_16x16x32_bf16 v[248:251], a[156:159], v[152:155], v[248:251]\n\t"
    "ds_read_b64_tr_b16 a[130:131], v16 offset:2048\n\t"
    "v_mul_f32 v190, v36, v190\n\t"
    "v_mfma_f32_16x16x32_bf16 v[44:47], a[96:99], a[80:83], v[8:11]\n\t"
    "v_exp_f32 v56, v56\n\t"
    "v_mul_f32 v191, v36, v191\n\t"
    "v_mfma_f32_16x16x32_bf16 v[44:47], a[100:103], a[84:87], v[44:47]\n\t"
    "ds_read_b64_tr_b16 a[132:133], v16 offset:4096\n\t"
    "v_exp_f32 v57, v57\n\t"
    "v_mfma_f32_16x16x32_bf16 v[44:47], a[104:107], a[88:91], v[44:47]\n\t"
    "ds_write_b64 v32, v[176:177] offset:4128\n\t"
    "v_mfma_f32_16x16x32_bf16 v[44:47], a[108:111], a[92:95], v[44:47]\n\t"
    "v_exp_f32 v58, v58\n\t"
    "v_mfma_f32_16x16x32_bf16 v[48:51], a[228:231], a[8:11], 0\n\t"
    "ds_read_b64_tr_b16 a[134:135], v16 offset:6144\n\t"
    "v_exp_f32 v59, v59\n\t"
    "v_mfma_f32_16x16x32_bf16 v[48:51], a[232:235], a[8:11], v[48:51]\n\t"
    "ds_read_b64_tr_b16 a[136:137], v16 offset:8192\n\t"
    "v_cvt_pk_bf16_f32 v110, v56, v57\n\t"
    "v_mfma_f32_16x16x32_bf16 v[52:55], a[236:239], a[8:11], 0\n\t"
    "ds_read_b64_tr_b16 a[138:139], v16 offset:10240\n\t"
    "v_cvt_pk_bf16_f32 v111, v58, v59\n\t"
    "v_cvt_pk_bf16_f32 v188, v188, v189\n\t"
    "v_mfma_f32_16x16x32_bf16 v[52:55], a[240:243], a[8:11], v[52:55]\n\t"
    "v_cvt_pk_bf16_f32 v189, v190, v191\n\t"
    "v_exp_f32 v60, v60\n\t"
    "v_mfma_f32_16x16x32_bf16 v[252:255], a[144:147], v[120:123], v[252:255]\n\t"
    "ds_read_b64_tr_b16 a[140:141], v16 offset:12288\n\t"
    "v_mul_f32 v200, v36, v200\n\t"
    "v_mfma_f32_16x16x32_bf16 v[252:255], a[148:151], v[132:135], v[252:255]\n\t"
    "buffer_load_dwordx4 v21, s[40:43], s78 offen offset:1024 nt lds\n\t"
    "v_mul_f32 v201, v36, v201\n\t"
    "v_mfma_f32_16x16x32_bf16 v[252:255], a[152:155], v[144:147], v[252:255]\n\t"
    "ds_read_b64_tr_b16 a[142:143], v16 offset:14336\n\t"
    "v_exp_f32 v61, v61\n\t"
    "v_mfma_f32_16x16x32_bf16 v[252:255], a[156:159], v[156:159], v[252:255]\n\t"
    "s_waitcnt lgkmcnt(5)\n\t"
    "v_exp_f32 v62, v62\n\t"
    "v_mfma_f32_16x16x32_bf16 v[56:59], a[112:115], a[48:51], v[0:3]\n\t"
    "ds_read_b128 a[96:99], v12 offset:40960\n\t"
    "v_exp_f32 v63, v63\n\t"
    "v_mfma_f32_16x16x32_bf16 v[56:59], a[116:119], a[52:55], v[56:59]\n\t"
    "ds_write_b64 v32, v[188:189] offset:4160\n\t"
    "v_mfma_f32_16x16x32_bf16 v[56:59], a[120:123], a[56:59], v[56:59]\n\t"
    "v_cvt_pk_bf16_f32 v112, v60, v61\n\t"
    "v_cvt_pk_bf16_f32 v113, v62, v63\n\t"
    "v_mul_f32 v202, v36, v202\n\t"
    "v_mfma_f32_16x16x32_bf16 v[56:59], a[124:127], a[60:63], v[56:59]\n\t"
    "ds_read_b128 a[100:103], v13 offset:40960\n\t"
    "v_mul_f32 v203, v36, v203\n\t"
    "v_mfma_f32_16x16x32_bf16 v[160:163], a[128:131], v[64:67], 0\n\t"
    "s_waitcnt lgkmcnt(4)\n\t"
    "ds_read_b128 a[104:107], v14 offset:40960\n\t"
    "v_cvt_pk_bf16_f32 v200, v200, v201\n\t"
    "v_cvt_pk_bf16_f32 v201, v202, v203\n\t"
    "v_mfma_f32_16x16x32_bf16 v[160:163], a[132:135], v[76:79], v[160:163]\n\t"
    "v_exp_f32 v40, v40\n\t"
    "v_mul_f32 v168, v37, v168\n\t"
    "v_mfma_f32_16x16x32_bf16 v[160:163], a[136:139], v[88:91], v[160:163]\n\t"
    "s_waitcnt lgkmcnt(4)\n\t"
    "ds_read_b128 a[108:111], v15 offset:40960\n\t"
    "v_exp_f32 v41, v41\n\t"
    "v_mfma_f32_16x16x32_bf16 v[160:163], a[140:143], v[100:103], v[160:163]\n\t"
    "buffer_load_dwordx4 v22, s[40:43], s78 offen offset:2048 nt lds\n\t"
    "v_mfma_f32_16x16x32_bf16 v[60:63], a[112:115], a[64:67], v[4:7]\n\t"
    "ds_read_b64_tr_b16 a[144:145], v17 offset:0\n\t"
    "v_exp_f32 v42, v42\n\t"
    "v_mfma_f32_16x16x32_bf16 v[60:63], a[116:119], a[68:71], v[60:63]\n\t"
    "v_exp_f32 v43, v43\n\t"
    "v_cvt_pk_bf16_f32 v116, v40, v41\n\t"
    "v_mfma_f32_16x16x32_bf16 v[60:63], a[120:123], a[72:75], v[60:63]\n\t"
    "ds_write_b64 v32, v[200:201] offset:4192\n\t"
    "v_mfma_f32_16x16x32_bf16 v[60:63], a[124:127], a[76:79], v[60:63]\n\t"
    "v_cvt_pk_bf16_f32 v117, v42, v43\n\t"
    "v_mul_f32 v169, v37, v169\n\t"
    "v_mul_f32 v170, v37, v170\n\t"
    "v_mfma_f32_16x16x32_bf16 v[164:167], a[128:131], v[68:71], 0\n\t"
    "ds_read_b64_tr_b16 a[146:147], v17 offset:2048\n\t"
    "v_mul_f32 v171, v37, v171\n\t"
    "v_cvt_pk_bf16_f32 v168, v168, v169\n\t"
    "v_mfma_f32_16x16x32_bf16 v[164:167], a[132:135], v[80:83], v[164:167]\n\t"
    "ds_read_b64_tr_b16 a[148:149], v17 offset:4096\n\t"
    "v_cvt_pk_bf16_f32 v169, v170, v171\n\t"
    "v_mfma_f32_16x16x32_bf16 v[164:167], a[136:139], v[92:95], v[164:167]\n\t"
    "ds_read_b64_tr_b16 a[150:151], v17 offset:6144\n\t"
    "v_exp_f32 v44, v44\n\t"
    "v_mfma_f32_16x16x32_bf16 v[164:167], a[140:143], v[104:107], v[164:167]\n\t"
    "ds_read_b128 a[184:187], v34 offset:4096\n\t"
    "v_exp_f32 v45, v45\n\t"
    "v_mfma_f32_16x16x32_bf16 v[40:43], a[112:115], a[80:83], v[8:11]\n\t"
    "ds_read_b128 a[188:191], v34 offset:5248\n\t"
    "v_exp_f32 v46, v46\n\t"
    "v_mfma_f32_16x16x32_bf16 v[40:43], a[116:119], a[84:87], v[40:43]\n\t"
    "ds_read_b64_tr_b16 a[152:153], v17 offset:8192\n\t"
    "v_exp_f32 v47, v47\n\t"
    "v_mfma_f32_16x16x32_bf16 v[40:43], a[120:123], a[88:91], v[40:43]\n\t"
    "ds_write_b64 v32, v[168:169] offset:4096\n\t"
    "v_mfma_f32_16x16x32_bf16 v[40:43], a[124:127], a[92:95], v[40:43]\n\t"
    "v_cvt_pk_bf16_f32 v120, v44, v45\n\t"
    "v_cvt_pk_bf16_f32 v121, v46, v47\n\t"
    "v_mfma_f32_16x16x32_bf16 v[168:171], a[128:131], v[72:75], 0\n\t"
    "ds_read_b64_tr_b16 a[154:155], v17 offset:10240\n\t"
    "v_cvt_pk_bf16_f32 v48, v48, v49\n\t"
    "v_cvt_pk_bf16_f32 v50, v50, v51\n\t"
    "v_mfma_f32_16x16x32_bf16 v[168:171], a[132:135], v[84:87], v[168:171]\n\t"
    "ds_read_b64_tr_b16 a[156:157], v17 offset:12288\n\t"
    "v_accvgpr_write_b32 a8, v48\n\t"
    "v_accvgpr_write_b32 a9, v50\n\t"
    "v_mfma_f32_16x16x32_bf16 v[168:171], a[136:139], v[96:99], v[168:171]\n\t"
    "ds_read_b64_tr_b16 a[158:159], v17 offset:14336\n\t"
    "v_mul_f32 v180, v37, v180\n\t"
    "v_mul_f32 v181, v37, v181\n\t"
    "v_mfma_f32_16x16x32_bf16 v[168:171], a[140:143], v[108:111], v[168:171]\n\t"
    "s_waitcnt lgkmcnt(6)\n\t"
    "buffer_load_dwordx4 v23, s[40:43], s78 offen offset:3072 nt lds\n\t"
    "v_mul_f32 v182, v37, v182\n\t"
    "v_mfma_f32_16x16x32_bf16 v[44:47], a[96:99], a[48:51], v[0:3]\n\t"
    "s_add_u32 s79, s62, 32896\n\t"
    "ds_read_b128 a[112:115], v12 offset:45056\n\t"
    "v_mul_f32 v183, v37, v183\n\t"
    "v_mfma_f32_16x16x32_bf16 v[44:47], a[100:103], a[52:55], v[44:47]\n\t"
    "s_add_u32 m0, s77, 49152\n\t"
    "v_cvt_pk_bf16_f32 v180, v180, v181\n\t"
    "v_cvt_pk_bf16_f32 v181, v182, v183\n\t"
    "v_cvt_pk_bf16_f32 v52, v52, v53\n\t"
    "v_mfma_f32_16x16x32_bf16 v[44:47], a[104:107], a[56:59], v[44:47]\n\t"
    "ds_read_b128 a[116:119], v13 offset:45056\n\t"
    "v_cvt_pk_bf16_f32 v54, v54, v55\n\t"
    "v_accvgpr_write_b32 a10, v52\n\t"
    "v_mfma_f32_16x16x32_bf16 v[44:47], a[108:111], a[60:63], v[44:47]\n\t"
    "buffer_load_dwordx4 v24, s[44:47], s79 offen offset:0 nt lds\n\t"
    "v_accvgpr_write_b32 a11, v54\n\t"
    "v_mfma_f32_16x16x32_bf16 v[172:175], a[144:147], v[64:67], 0\n\t"
    "v_exp_f32 v56, v56\n\t"
    "v_mul_f32 v192, v37, v192\n\t"
    "v_mfma_f32_16x16x32_bf16 v[172:175], a[148:151], v[76:79], v[172:175]\n\t"
    "s_waitcnt lgkmcnt(3)\n\t"
    "ds_read_b128 a[120:123], v14 offset:45056\n\t"
    "v_exp_f32 v57, v57\n\t"
    "v_mfma_f32_16x16x32_bf16 v[172:175], a[152:155], v[88:91], v[172:175]\n\t"
    "s_waitcnt lgkmcnt(3)\n\t"
    "v_exp_f32 v58, v58\n\t"
    "v_mfma_f32_16x16x32_bf16 v[172:175], a[156:159], v[100:103], v[172:175]\n\t"
    "ds_write_b64 v32, v[180:181] offset:4128\n\t"
    "v_mfma_f32_16x16x32_bf16 v[48:51], a[96:99], a[64:67], v[4:7]\n\t"
    "v_exp_f32 v59, v59\n\t"
    "v_cvt_pk_bf16_f32 v114, v56, v57\n\t"
    "v_mfma_f32_16x16x32_bf16 v[48:51], a[100:103], a[68:71], v[48:51]\n\t"
    "ds_read_b128 a[124:127], v15 offset:45056\n\t"
    "v_cvt_pk_bf16_f32 v115, v58, v59\n\t"
    "v_mul_f32 v193, v37, v193\n\t"
    "v_mfma_f32_16x16x32_bf16 v[48:51], a[104:107], a[72:75], v[48:51]\n\t"
    "ds_read_b64_tr_b16 a[128:129], v18 offset:0\n\t"
    "v_mul_f32 v194, v37, v194\n\t"
    "v_mul_f32 v195, v37, v195\n\t"
    "v_mfma_f32_16x16x32_bf16 v[48:51], a[108:111], a[76:79], v[48:51]\n\t"
    "v_exp_f32 v60, v60\n\t"
    "v_mfma_f32_16x16x32_bf16 v[176:179], a[144:147], v[68:71], 0\n\t"
    "buffer_load_dwordx4 v25, s[44:47], s79 offen offset:1024 nt lds\n\t"
    "v_cvt_pk_bf16_f32 v192, v192, v193\n\t"
    "v_mfma_f32_16x16x32_bf16 v[176:179], a[148:151], v[80:83], v[176:179]\n\t"
    "ds_read_b64_tr_b16 a[130:131], v18 offset:2048\n\t"
    "v_exp_f32 v61, v61\n\t"
    "v_mfma_f32_16x16x32_bf16 v[176:179], a[152:155], v[92:95], v[176:179]\n\t"
    "v_exp_f32 v62, v62\n\t"
    "v_cvt_pk_bf16_f32 v193, v194, v195\n\t"
    "v_mfma_f32_16x16x32_bf16 v[176:179], a[156:159], v[104:107], v[176:179]\n\t"
    "v_exp_f32 v63, v63\n\t"
    "v_cvt_pk_bf16_f32 v118, v60, v61\n\t"
    "v_mfma_f32_16x16x32_bf16 a[160:163], a[172:175], v[64:67], 0\n\t"
    "ds_read_b64_tr_b16 a[132:133], v18 offset:4096\n\t"
    "v_cvt_pk_bf16_f32 v119, v62, v63\n\t"
    "v_mul_f32 v204, v37, v204\n\t"
    "v_mfma_f32_16x16x32_bf16 a[160:163], a[172:175], v[76:79], a[160:163]\n\t"
    "ds_write_b64 v32, v[192:193] offset:4160\n\t"
    "v_mfma_f32_16x16x32_bf16 a[160:163], a[172:175], v[88:91], a[160:163]\n\t"
    "v_exp_f32 v40, v40\n\t"
    "v_mfma_f32_16x16x32_bf16 a[160:163], a[172:175], v[100:103], a[160:163]\n\t"
    "ds_read_b64_tr_b16 a[134:135], v18 offset:6144\n\t"
    "v_exp_f32 v41, v41\n\t"
    "v_mfma_f32_16x16x32_bf16 v[52:55], a[96:99], a[80:83], v[8:11]\n\t"
    "v_exp_f32 v42, v42\n\t"
    "v_cvt_pk_bf16_f32 v122, v40, v41\n\t"
    "v_mfma_f32_16x16x32_bf16 v[52:55], a[100:103], a[84:87], v[52:55]\n\t"
    "ds_read_b64_tr_b16 a[136:137], v18 offset:8192\n\t"
    "v_exp_f32 v43, v43\n\t"
    "v_mfma_f32_16x16x32_bf16 v[52:55], a[104:107], a[88:91], v[52:55]\n\t"
    "buffer_load_dwordx4 v26, s[44:47], s79 offen offset:2048 nt lds\n\t"
    "v_mfma_f32_16x16x32_bf16 v[52:55], a[108:111], a[92:95], v[52:55]\n\t"
    "v_cvt_pk_bf16_f32 v123, v42, v43\n\t"
    "v_mul_f32 v205, v37, v205\n\t"
    "v_mul_f32 v206, v37, v206\n\t"
    "v_mfma_f32_16x16x32_bf16 v[56:59], a[228:231], a[12:15], 0\n\t"
    "ds_read_b64_tr_b16 a[138:139], v18 offset:10240\n\t"
    "v_mul_f32 v207, v37, v207\n\t"
    "v_cvt_pk_bf16_f32 v204, v204, v205\n\t"
    "v_mfma_f32_16x16x32_bf16 v[56:59], a[232:235], a[12:15], v[56:59]\n\t"
    "v_cvt_pk_bf16_f32 v205, v206, v207\n\t"
    "v_exp_f32 v44, v44\n\t"
    "v_mfma_f32_16x16x32_bf16 v[60:63], a[236:239], a[12:15], 0\n\t"
    "ds_write_b64 v32, v[204:205] offset:4192\n\t"
    "v_mfma_f32_16x16x32_bf16 v[60:63], a[240:243], a[12:15], v[60:63]\n\t"
    "v_exp_f32 v45, v45\n\t"
    "v_mul_f32 v208, v35, v208\n\t"
    "v_mfma_f32_16x16x32_bf16 v[180:183], a[144:147], v[72:75], 0\n\t"
    "ds_read_b64_tr_b16 a[140:141], v18 offset:12288\n\t"
    "v_cvt_pk_bf16_f32 v124, v44, v45\n\t"
    "v_mfma_f32_16x16x32_bf16 v[180:183], a[148:151], v[84:87], v[180:183]\n\t"
    "v_exp_f32 v46, v46\n\t"
    "v_mul_f32 v209, v35, v209\n\t"
    "v_mfma_f32_16x16x32_bf16 v[180:183], a[152:155], v[96:99], v[180:183]\n\t"
    "ds_read_b64_tr_b16 a[142:143], v18 offset:14336\n\t"
    "v_exp_f32 v47, v47\n\t"
    "v_mfma_f32_16x16x32_bf16 v[180:183], a[156:159], v[108:111], v[180:183]\n\t"
    "s_waitcnt vmcnt(15) lgkmcnt(4)\n\t"
    "v_cvt_pk_bf16_f32 v125, v46, v47\n\t"
    "v_mul_f32 v210, v35, v210\n\t"
    "v_mul_f32 v211, v35, v211\n\t"
    "v_mfma_f32_16x16x32_bf16 v[40:43], a[112:115], a[48:51], v[0:3]\n\t"
    "ds_read_b128 a[96:99], v12 offset:49152\n\t"
    "v_cvt_pk_bf16_f32 v208, v208, v209\n\t"
    "v_cvt_pk_bf16_f32 v209, v210, v211\n\t"
    "v_mfma_f32_16x16x32_bf16 v[40:43], a[116:119], a[52:55], v[40:43]\n\t"
    "ds_read_b128 a[100:103], v13 offset:49152\n\t"
    "v_mul_f32 v220, v35, v220\n\t"
    "v_mfma_f32_16x16x32_bf16 v[40:43], a[120:123], a[56:59], v[40:43]\n\t"
    "buffer_load_dwordx4 v27, s[44:47], s79 offen offset:3072 nt lds\n\t"
    "v_mul_f32 v221, v35, v221\n\t"
    "v_mfma_f32_16x16x32_bf16 v[40:43], a[124:127], a[60:63], v[40:43]\n\t"
    "ds_read_b128 a[104:107], v14 offset:49152\n\t"
    "v_exp_f32 v48, v48\n\t"
    "v_mfma_f32_16x16x32_bf16 v[184:187], a[128:131], v[64:67], 0\n\t"
    "ds_read_b128 a[16:19], v28 offset:0\n\t"
    "v_exp_f32 v49, v49\n\t"
    "v_mfma_f32_16x16x32_bf16 v[184:187], a[132:135], v[76:79], v[184:187]\n\t"
    "s_waitcnt lgkmcnt(5)\n\t"
    "ds_read_b128 a[108:111], v15 offset:49152\n\t"
    "v_exp_f32 v50, v50\n\t"
    "v_mfma_f32_16x16x32_bf16 v[184:187], a[136:139], v[88:91], v[184:187]\n\t"
    "s_waitcnt lgkmcnt(5)\n\t"
    "ds_read_b128 a[192:195], v34 offset:4096\n\t"
    "v_exp_f32 v51, v51\n\t"
    "v_mfma_f32_16x16x32_bf16 v[184:187], a[140:143], v[100:103], v[184:187]\n\t"
    "ds_read_b64_tr_b16 a[144:145], v19 offset:0\n\t"
    "v_cvt_pk_bf16_f32 v128, v48, v49\n\t"
    "v_mfma_f32_16x16x32_bf16 v[44:47], a[112:115], a[64:67], v[4:7]\n\t"
    "ds_read_b128 a[196:199], v34 offset:5248\n\t"
    "v_cvt_pk_bf16_f32 v129, v50, v51\n\t"
    "v_mul_f32 v222, v35, v222\n\t"
    "v_mfma_f32_16x16x32_bf16 v[44:47], a[116:119], a[68:71], v[44:47]\n\t"
    "ds_write_b64 v32, v[208:209] offset:4096\n\t"
    "v_mfma_f32_16x16x32_bf16 v[44:47], a[120:123], a[72:75], v[44:47]\n\t"
    "v_mul_f32 v223, v35, v223\n\t"
    "v_cvt_pk_bf16_f32 v220, v220, v221\n\t"
    "v_mul_f32 v232, v35, v232\n\t"
    "v_mfma_f32_16x16x32_bf16 v[44:47], a[124:127], a[76:79], v[44:47]\n\t"
    "ds_read_b64_tr_b16 a[146:147], v19 offset:2048\n\t"
    "v_cvt_pk_bf16_f32 v221, v222, v223\n\t"
    "v_mul_f32 v233, v35, v233\n\t"
    "v_mfma_f32_16x16x32_bf16 v[188:191], a[128:131], v[68:71], 0\n\t"
    "ds_read_b128 a[20:23], v29 offset:0\n\t"
    "v_mul_f32 v234, v35, v234\n\t"
    "v_mfma_f32_16x16x32_bf16 v[188:191], a[132:135], v[80:83], v[188:191]\n\t"
    "ds_read_b64_tr_b16 a[148:149], v19 offset:4096\n\t"
    "v_exp_f32 v52, v52\n\t"
    "v_mfma_f32_16x16x32_bf16 v[188:191], a[136:139], v[92:95], v[188:191]\n\t"
    "ds_read_b64_tr_b16 a[150:151], v19 offset:6144\n\t"
    "v_exp_f32 v53, v53\n\t"
    "v_mfma_f32_16x16x32_bf16 v[188:191], a[140:143], v[104:107], v[188:191]\n\t"
    "ds_read_b128 a[24:27], v30 offset:0\n\t"
    "v_exp_f32 v54, v54\n\t"
    "v_mfma_f32_16x16x32_bf16 v[48:51], a[112:115], a[80:83], v[8:11]\n\t"
    "ds_read_b64_tr_b16 a[152:153], v19 offset:8192\n\t"
    "v_exp_f32 v55, v55\n\t"
    "v_mfma_f32_16x16x32_bf16 v[48:51], a[116:119], a[84:87], v[48:51]\n\t"
    "ds_read_b128 a[28:31], v31 offset:0\n\t"
    "v_cvt_pk_bf16_f32 v132, v52, v53\n\t"
    "v_mfma_f32_16x16x32_bf16 v[48:51], a[120:123], a[88:91], v[48:51]\n\t"
    "ds_write_b64 v32, v[220:221] offset:4128\n\t"
    "v_mfma_f32_16x16x32_bf16 v[48:51], a[124:127], a[92:95], v[48:51]\n\t"
    "ds_read_b64_tr_b16 a[154:155], v19 offset:10240\n\t"
    "v_cvt_pk_bf16_f32 v133, v54, v55\n\t"
    "v_mul_f32 v235, v35, v235\n\t"
    "v_mfma_f32_16x16x32_bf16 v[192:195], a[128:131], v[72:75], 0\n\t"
    "ds_read_b64_tr_b16 a[156:157], v19 offset:12288\n\t"
    "v_cvt_pk_bf16_f32 v232, v232, v233\n\t"
    "v_cvt_pk_bf16_f32 v233, v234, v235\n\t"
    "v_mfma_f32_16x16x32_bf16 v[192:195], a[132:135], v[84:87], v[192:195]\n\t"
    "ds_read_b64_tr_b16 a[158:159], v19 offset:14336\n\t"
    "v_cvt_pk_bf16_f32 v56, v56, v57\n\t"
    "v_cvt_pk_bf16_f32 v58, v58, v59\n\t"
    "v_mfma_f32_16x16x32_bf16 v[192:195], a[136:139], v[96:99], v[192:195]\n\t"
    "v_accvgpr_write_b32 a12, v56\n\t"
    "v_accvgpr_write_b32 a13, v58\n\t"
    "v_mul_f32 v244, v35, v244\n\t"
    "v_mfma_f32_16x16x32_bf16 v[192:195], a[140:143], v[108:111], v[192:195]\n\t"
    "s_waitcnt lgkmcnt(6)\n\t"
    "v_mul_f32 v245, v35, v245\n\t"
    "v_mul_f32 v246, v35, v246\n\t"
    "v_mul_f32 v247, v35, v247\n\t"
    "v_mfma_f32_16x16x32_bf16 v[52:55], a[96:99], a[48:51], v[0:3]\n\t"
    "v_cvt_pk_bf16_f32 v244, v244, v245\n\t"
    "v_cvt_pk_bf16_f32 v245, v246, v247\n\t"
    "v_mfma_f32_16x16x32_bf16 v[52:55], a[100:103], a[52:55], v[52:55]\n\t"
    "v_cvt_pk_bf16_f32 v60, v60, v61\n\t"
    "v_cvt_pk_bf16_f32 v62, v62, v63\n\t"
    "v_accvgpr_write_b32 a14, v60\n\t"
    "v_mfma_f32_16x16x32_bf16 v[52:55], a[104:107], a[56:59], v[52:55]\n\t"
    "ds_write_b64 v32, v[232:233] offset:4160\n\t"
    "v_mfma_f32_16x16x32_bf16 v[52:55], a[108:111], a[60:63], v[52:55]\n\t"
    "v_accvgpr_write_b32 a15, v62\n\t"
    "v_mul_f32 v212, v36, v212\n\t"
    "v_mul_f32 v213, v36, v213\n\t"
    "v_mfma_f32_16x16x32_bf16 v[196:199], a[144:147], v[64:67], 0\n\t"
    "v_mul_f32 v214, v36, v214\n\t"
    "v_mul_f32 v215, v36, v215\n\t"
    "v_cvt_pk_bf16_f32 v212, v212, v213\n\t"
    "v_mfma_f32_16x16x32_bf16 v[196:199], a[148:151], v[76:79], v[196:199]\n\t"
    "s_waitcnt lgkmcnt(1)\n\t"
    "v_cvt_pk_bf16_f32 v213, v214, v215\n\t"
    "v_exp_f32 v40, v40\n\t"
    "v_mfma_f32_16x16x32_bf16 v[196:199], a[152:155], v[88:91], v[196:199]\n\t"
    "s_waitcnt vmcnt(20) lgkmcnt(1)\n\t"
    "v_exp_f32 v41, v41\n\t"
    "v_mfma_f32_16x16x32_bf16 v[196:199], a[156:159], v[100:103], v[196:199]\n\t"
    "s_barrier\n\t"
    "ds_read_b128 a[112:115], v12 offset:53248\n\t"
    "v_exp_f32 v42, v42\n\t"
    "v_mfma_f32_16x16x32_bf16 v[56:59], a[96:99], a[64:67], v[4:7]\n\t"
    "s_mov_b32 m0, s85\n\t"
    "ds_read_b128 a[116:119], v13 offset:53248\n\t"
    "v_exp_f32 v43, v43\n\t"
    "v_mfma_f32_16x16x32_bf16 v[56:59], a[100:103], a[68:71], v[56:59]\n\t"
    "v_cvt_pk_bf16_f32 v126, v40, v41\n\t"
    "v_cvt_pk_bf16_f32 v127, v42, v43\n\t"
    "v_mul_f32 v224, v36, v224\n\t"
    "v_mfma_f32_16x16x32_bf16 v[56:59], a[104:107], a[72:75], v[56:59]\n\t"
    "ds_write_b64 v32, v[244:245] offset:4192\n\t"
    "v_mfma_f32_16x16x32_bf16 v[56:59], a[108:111], a[76:79], v[56:59]\n\t"
    "v_exp_f32 v44, v44\n\t"
    "v_mul_f32 v225, v36, v225\n\t"
    "v_mfma_f32_16x16x32_bf16 v[200:203], a[144:147], v[68:71], 0\n\t"
    "ds_read_b128 a[120:123], v14 offset:53248\n\t"
    "v_mul_f32 v226, v36, v226\n\t"
    "v_mfma_f32_16x16x32_bf16 v[200:203], a[148:151], v[80:83], v[200:203]\n\t"
    "ds_read_b128 a[124:127], v15 offset:53248\n\t"
    "v_exp_f32 v45, v45\n\t"
    "v_mfma_f32_16x16x32_bf16 v[200:203], a[152:155], v[92:95], v[200:203]\n\t"
    "ds_read_b64_tr_b16 a[128:129], v16 offset:16384\n\t"
    "v_exp_f32 v46, v46\n\t"
    "v_mfma_f32_16x16x32_bf16 v[200:203], a[156:159], v[104:107], v[200:203]\n\t"
    "ds_read_b64_tr_b16 a[130:131], v16 offset:18432\n\t"
    "v_exp_f32 v47, v47\n\t"
    "v_mfma_f32_16x16x32_bf16 v[60:63], a[96:99], a[80:83], v[8:11]\n\t"
    "ds_read_b128 a[200:203], v34 offset:4096\n\t"
    "v_cvt_pk_bf16_f32 v130, v44, v45\n\t"
    "v_cvt_pk_bf16_f32 v131, v46, v47\n\t"
    "v_mfma_f32_16x16x32_bf16 v[60:63], a[100:103], a[84:87], v[60:63]\n\t"
    "ds_read_b128 a[204:207], v34 offset:5248\n\t"
    "v_mul_f32 v227, v36, v227\n\t"
    "v_cvt_pk_bf16_f32 v224, v224, v225\n\t"
    "v_mfma_f32_16x16x32_bf16 v[60:63], a[104:107], a[88:91], v[60:63]\n\t"
    "buffer_load_dwordx4 v20, s[36:39], s72 offen offset:0 nt lds\n\t"
    "ds_write_b64 v32, v[212:213] offset:4096\n\t"
    "v_mfma_f32_16x16x32_bf16 v[60:63], a[108:111], a[92:95], v[60:63]\n\t"
    "ds_read_b64_tr_b16 a[132:133], v16 offset:20480\n\t"
    "v_cvt_pk_bf16_f32 v225, v226, v227\n\t"
    "v_mfma_f32_16x16x32_bf16 v[40:43], a[228:231], a[16:19], 0\n\t"
    "ds_read_b64_tr_b16 a[134:135], v16 offset:22528\n\t"
    "v_exp_f32 v48, v48\n\t"
    "v_mfma_f32_16x16x32_bf16 v[40:43], a[232:235], a[16:19], v[40:43]\n\t"
    "ds_read_b64_tr_b16 a[136:137], v16 offset:24576\n\t"
    "v_exp_f32 v49, v49\n\t"
    "v_mfma_f32_16x16x32_bf16 v[44:47], a[236:239], a[16:19], 0\n\t"
    "ds_read_b64_tr_b16 a[138:139], v16 offset:26624\n\t"
    "v_cvt_pk_bf16_f32 v134, v48, v49\n\t"
    "v_mfma_f32_16x16x32_bf16 v[44:47], a[240:243], a[16:19], v[44:47]\n\t"
    "v_exp_f32 v50, v50\n\t"
    "v_mul_f32 v236, v36, v236\n\t"
    "v_mfma_f32_16x16x32_bf16 v[204:207], a[144:147], v[72:75], 0\n\t"
    "ds_read_b64_tr_b16 a[140:141], v16 offset:28672\n\t"
    "v_exp_f32 v51, v51\n\t"
    "v_mfma_f32_16x16x32_bf16 v[204:207], a[148:151], v[84:87], v[204:207]\n\t"
    "v_cvt_pk_bf16_f32 v135, v50, v51\n\t"
    "v_mul_f32 v237, v36, v237\n\t"
    "v_mfma_f32_16x16x32_bf16 v[204:207], a[152:155], v[96:99], v[204:207]\n\t"
    "ds_read_b64_tr_b16 a[142:143], v16 offset:30720\n\t"
    "ds_write_b64 v32, v[224:225] offset:4128\n\t"
    "v_mfma_f32_16x16x32_bf16 v[204:207], a[156:159], v[108:111], v[204:207]\n\t"
    "s_waitcnt lgkmcnt(6)\n\t"
    "v_mul_f32 v238, v36, v238\n\t"
    "v_mul_f32 v239, v36, v239\n\t"
    "v_cvt_pk_bf16_f32 v236, v236, v237\n\t"
    "v_mfma_f32_16x16x32_bf16 v[48:51], a[112:115], a[48:51], v[0:3]\n\t"
    "buffer_load_dwordx4 v21, s[36:39], s72 offen offset:1024 nt lds\n\t"
    "v_mfma_f32_16x16x32_bf16 v[48:51], a[116:119], a[52:55], v[48:51]\n\t"
    "ds_read_b128 a[96:99], v12 offset:57344\n\t"
    "v_cvt_pk_bf16_f32 v237, v238, v239\n\t"
    "v_mul_f32 v248, v36, v248\n\t"
    "v_mfma_f32_16x16x32_bf16 v[48:51], a[120:123], a[56:59], v[48:51]\n\t"
    "ds_read_b128 a[100:103], v13 offset:57344\n\t"
    "v_exp_f32 v52, v52\n\t"
    "v_mfma_f32_16x16x32_bf16 v[48:51], a[124:127], a[60:63], v[48:51]\n\t"
    "v_exp_f32 v53, v53\n\t"
    "v_mfma_f32_16x16x32_bf16 v[208:211], a[128:131], v[64:67], 0\n\t"
    "s_waitcnt lgkmcnt(4)\n\t"
    "ds_read_b128 a[104:107], v14 offset:57344\n\t"
    "v_exp_f32 v54, v54\n\t"
    "v_mfma_f32_16x16x32_bf16 v[208:211], a[132:135], v[76:79], v[208:211]\n\t"
    "v_exp_f32 v55, v55\n\t"
    "v_mfma_f32_16x16x32_bf16 v[208:211], a[136:139], v[88:91], v[208:211]\n\t"
    "s_waitcnt lgkmcnt(3)\n\t"
    "ds_write_b64 v32, v[236:237] offset:4160\n\t"
    "v_mfma_f32_16x16x32_bf16 v[208:211], a[140:143], v[100:103], v[208:211]\n\t"
    "v_cvt_pk_bf16_f32 v136, v52, v53\n\t"
    "v_cvt_pk_bf16_f32 v137, v54, v55\n\t"
    "v_mul_f32 v249, v36, v249\n\t"
    "v_mfma_f32_16x16x32_bf16 v[52:55], a[112:115], a[64:67], v[4:7]\n\t"
    "ds_read_b128 a[108:111], v15 offset:57344\n\t"
    "v_mul_f32 v250, v36, v250\n\t"
    "v_mul_f32 v251, v36, v251\n\t"
    "v_mfma_f32_16x16x32_bf16 v[52:55], a[116:119], a[68:71], v[52:55]\n\t"
    "ds_read_b64_tr_b16 a[144:145], v17 offset:16384\n\t"
    "v_cvt_pk_bf16_f32 v248, v248, v249\n\t"
    "v_mfma_f32_16x16x32_bf16 v[52:55], a[120:123], a[72:75], v[52:55]\n\t"
    "buffer_load_dwordx4 v22, s[36:39], s72 offen offset:2048 nt lds\n\t"
    "v_cvt_pk_bf16_f32 v249, v250, v251\n\t"
    "v_mfma_f32_16x16x32_bf16 v[52:55], a[124:127], a[76:79], v[52:55]\n\t"
    "ds_read_b64_tr_b16 a[146:147], v17 offset:18432\n\t"
    "v_mul_f32 v216, v37, v216\n\t"
    "v_mfma_f32_16x16x32_bf16 v[212:215], a[128:131], v[68:71], 0\n\t"
    "v_exp_f32 v56, v56\n\t"
    "v_mul_f32 v217, v37, v217\n\t"
    "v_mfma_f32_16x16x32_bf16 v[212:215], a[132:135], v[80:83], v[212:215]\n\t"
    "v_exp_f32 v57, v57\n\t"
    "v_mfma_f32_16x16x32_bf16 v[212:215], a[136:139], v[92:95], v[212:215]\n\t"
    "ds_write_b64 v32, v[248:249] offset:4192\n\t"
    "v_mfma_f32_16x16x32_bf16 v[212:215], a[140:143], v[104:107], v[212:215]\n\t"
    "v_exp_f32 v58, v58\n\t"
    "v_cvt_pk_bf16_f32 v140, v56, v57\n\t"
    "v_mfma_f32_16x16x32_bf16 a[164:167], a[172:175], v[68:71], 0\n\t"
    "ds_read_b64_tr_b16 a[148:149], v17 offset:20480\n\t"
    "v_exp_f32 v59, v59\n\t"
    "v_mfma_f32_16x16x32_bf16 a[164:167], a[172:175], v[80:83], a[164:167]\n\t"
    "ds_read_b64_tr_b16 a[150:151], v17 offset:22528\n\t"
    "v_cvt_pk_bf16_f32 v141, v58, v59\n\t"
    "v_mfma_f32_16x16x32_bf16 a[164:167], a[172:175], v[92:95], a[164:167]\n\t"
    "buffer_load_dwordx4 v23, s[36:39], s72 offen offset:3072 nt lds\n\t"
    "v_mul_f32 v218, v37, v218\n\t"
    "v_mfma_f32_16x16x32_bf16 a[164:167], a[172:175], v[104:107], a[164:167]\n\t"
    "ds_read_b64_tr_b16 a[152:153], v17 offset:24576\n\t"
    "v_mul_f32 v219, v37, v219\n\t"
    "v_mfma_f32_16x16x32_bf16 v[56:59], a[112:115], a[80:83], v[8:11]\n\t"
    "s_add_u32 s78, s56, 102400\n\t"
    "ds_read_b128 a[208:211], v34 offset:4096\n\t"
    "v_cvt_pk_bf16_f32 v216, v216, v217\n\t"
    "v_cvt_pk_bf16_f32 v217, v218, v219\n\t"
    "v_mfma_f32_16x16x32_bf16 v[56:59], a[116:119], a[84:87], v[56:59]\n\t"
    "s_mov_b32 m0, s75\n\t"
    "ds_read_b128 a[212:215], v34 offset:5248\n\t"
    "v_cvt_pk_bf16_f32 v40, v40, v41\n\t"
    "v_mfma_f32_16x16x32_bf16 v[56:59], a[120:123], a[88:91], v[56:59]\n\t"
    "ds_write_b64 v32, v[216:217] offset:4096\n\t"
    "v_mfma_f32_16x16x32_bf16 v[56:59], a[124:127], a[92:95], v[56:59]\n\t"
    "ds_read_b64_tr_b16 a[154:155], v17 offset:26624\n\t"
    "v_exp_f32 v60, v60\n\t"
    "v_mfma_f32_16x16x32_bf16 v[216:219], a[128:131], v[72:75], 0\n\t"
    "ds_read_b64_tr_b16 a[156:157], v17 offset:28672\n\t"
    "v_exp_f32 v61, v61\n\t"
    "v_mfma_f32_16x16x32_bf16 v[216:219], a[132:135], v[84:87], v[216:219]\n\t"
    "v_exp_f32 v62, v62\n\t"
    "v_mfma_f32_16x16x32_bf16 v[216:219], a[136:139], v[96:99], v[216:219]\n\t"
    "ds_read_b64_tr_b16 a[158:159], v17 offset:30720\n\t"
    "v_exp_f32 v63, v63\n\t"
    "v_mfma_f32_16x16x32_bf16 v[216:219], a[140:143], v[108:111], v[216:219]\n\t"
    "s_waitcnt lgkmcnt(6)\n\t"
    "v_cvt_pk_bf16_f32 v144, v60, v61\n\t"
    "v_cvt_pk_bf16_f32 v145, v62, v63\n\t"
    "v_mfma_f32_16x16x32_bf16 v[60:63], a[96:99], a[48:51], v[0:3]\n\t"
    "buffer_load_dwordx4 v20, s[40:43], s78 offen offset:0 nt lds\n\t"
    "v_cvt_pk_bf16_f32 v42, v42, v43\n\t"
    "v_mfma_f32_16x16x32_bf16 v[60:63], a[100:103], a[52:55], v[60:63]\n\t"
    "ds_read_b128 a[112:115], v12 offset:61440\n\t"
    "v_accvgpr_write_b32 a16, v40\n\t"
    "v_mfma_f32_16x16x32_bf16 v[60:63], a[104:107], a[56:59], v[60:63]\n\t"
    "ds_read_b128 a[116:119], v13 offset:61440\n\t"
    "v_accvgpr_write_b32 a17, v42\n\t"
    "v_mfma_f32_16x16x32_bf16 v[60:63], a[108:111], a[60:63], v[60:63]\n\t"
    "v_cvt_pk_bf16_f32 v44, v44, v45\n\t"
    "v_cvt_pk_bf16_f32 v46, v46, v47\n\t"
    "v_accvgpr_write_b32 a18, v44\n\t"
    "v_mfma_f32_16x16x32_bf16 v[220:223], a[144:147], v[64:67], 0\n\t"
    "ds_read_b128 a[120:123], v14 offset:61440\n\t"
    "v_accvgpr_write_b32 a19, v46\n\t"
    "v_mfma_f32_16x16x32_bf16 v[220:223], a[148:151], v[76:79], v[220:223]\n\t"
    "s_waitcnt lgkmcnt(4)\n\t"
    "buffer_load_dwordx4 v21, s[40:43], s78 offen offset:1024 nt lds\n\t"
    "v_mfma_f32_16x16x32_bf16 v[220:223], a[152:155], v[88:91], v[220:223]\n\t"
    "s_waitcnt lgkmcnt(3)\n\t"
    "v_exp_f32 v48, v48\n\t"
    "v_mul_f32 v228, v37, v228\n\t"
    "v_mfma_f32_16x16x32_bf16 v[220:223], a[156:159], v[100:103], v[220:223]\n\t"
    "ds_read_b128 a[124:127], v15 offset:61440\n\t"
    "v_mul_f32 v229, v37, v229\n\t"
    "v_mfma_f32_16x16x32_bf16 v[40:43], a[96:99], a[64:67], v[4:7]\n\t"
    "v_exp_f32 v49, v49\n\t"
    "v_mfma_f32_16x16x32_bf16 v[40:43], a[100:103], a[68:71], v[40:43]\n\t"
    "ds_read_b64_tr_b16 a[128:129], v18 offset:16384\n\t"
    "v_exp_f32 v50, v50\n\t"
    "v_mfma_f32_16x16x32_bf16 v[40:43], a[104:107], a[72:75], v[40:43]\n\t"
    "v_exp_f32 v51, v51\n\t"
    "v_mfma_f32_16x16x32_bf16 v[40:43], a[108:111], a[76:79], v[40:43]\n\t"
    "ds_read_b64_tr_b16 a[130:131], v18 offset:18432\n\t"
    "v_cvt_pk_bf16_f32 v138, v48, v49\n\t"
    "v_mfma_f32_16x16x32_bf16 v[224:227], a[144:147], v[68:71], 0\n\t"
    "v_cvt_pk_bf16_f32 v139, v50, v51\n\t"
    "v_mul_f32 v230, v37, v230\n\t"
    "v_mul_f32 v231, v37, v231\n\t"
    "v_mfma_f32_16x16x32_bf16 v[224:227], a[148:151], v[80:83], v[224:227]\n\t"
    "v_exp_f32 v52, v52\n\t"
    "v_mfma_f32_16x16x32_bf16 v[224:227], a[152:155], v[92:95], v[224:227]\n\t"
    "ds_read_b64_tr_b16 a[132:133], v18 offset:20480\n\t"
    "v_cvt_pk_bf16_f32 v228, v228, v229\n\t"
    "v_mfma_f32_16x16x32_bf16 v[224:227], a[156:159], v[104:107], v[224:227]\n\t"
    "v_exp_f32 v53, v53\n\t"
    "v_cvt_pk_bf16_f32 v229, v230, v231\n\t"
    "v_mfma_f32_16x16x32_bf16 v[44:47], a[96:99], a[80:83], v[8:11]\n\t"
    "ds_read_b64_tr_b16 a[134:135], v18 offset:22528\n\t"
    "v_cvt_pk_bf16_f32 v142, v52, v53\n\t"
    "v_mfma_f32_16x16x32_bf16 v[44:47], a[100:103], a[84:87], v[44:47]\n\t"
    "v_exp_f32 v54, v54\n\t"
    "v_mfma_f32_16x16x32_bf16 v[44:47], a[104:107], a[88:91], v[44:47]\n\t"
    "ds_read_b64_tr_b16 a[136:137], v18 offset:24576\n\t"
    "v_exp_f32 v55, v55\n\t"
    "v_mfma_f32_16x16x32_bf16 v[44:47], a[108:111], a[92:95], v[44:47]\n\t"
    "v_cvt_pk_bf16_f32 v143, v54, v55\n\t"
    "v_mul_f32 v240, v37, v240\n\t"
    "v_mfma_f32_16x16x32_bf16 v[48:51], a[228:231], a[20:23], 0\n\t"
    "buffer_load_dwordx4 v22, s[40:43], s78 offen offset:2048 nt lds\n\t"
    "v_mfma_f32_16x16x32_bf16 v[48:51], a[232:235], a[20:23], v[48:51]\n\t"
    "ds_read_b64_tr_b16 a[138:139], v18 offset:26624\n\t"
    "v_exp_f32 v56, v56\n\t"
    "v_mfma_f32_16x16x32_bf16 v[52:55], a[236:239], a[20:23], 0\n\t"
    "ds_write_b64 v32, v[228:229] offset:4128\n\t"
    "v_mfma_f32_16x16x32_bf16 v[52:55], a[240:243], a[20:23], v[52:55]\n\t"
    "v_exp_f32 v57, v57\n\t"
    "v_mfma_f32_16x16x32_bf16 v[228:231], a[144:147], v[72:75], 0\n\t"
    "ds_read_b64_tr_b16 a[140:141], v18 offset:28672\n\t"
    "v_cvt_pk_bf16_f32 v146, v56, v57\n\t"
    "v_mfma_f32_16x16x32_bf16 v[228:231], a[148:151], v[84:87], v[228:231]\n\t"
    "v_exp_f32 v58, v58\n\t"
    "v_mul_f32 v241, v37, v241\n\t"
    "v_mfma_f32_16x16x32_bf16 v[228:231], a[152:155], v[96:99], v[228:231]\n\t"
    "ds_read_b64_tr_b16 a[142:143], v18 offset:30720\n\t"
    "v_exp_f32 v59, v59\n\t"
    "v_mfma_f32_16x16x32_bf16 v[228:231], a[156:159], v[108:111], v[228:231]\n\t"
    "s_waitcnt lgkmcnt(4)\n\t"
    "v_cvt_pk_bf16_f32 v147, v58, v59\n\t"
    "v_mul_f32 v242, v37, v242\n\t"
    "v_mfma_f32_16x16x32_bf16 v[56:59], a[112:115], a[48:51], v[0:3]\n\t"
    "ds_read_b128 a[96:99], v12 offset:0\n\t"
    "v_exp_f32 v60, v60\n\t"
    "v_mfma_f32_16x16x32_bf16 v[56:59], a[116:119], a[52:55], v[56:59]\n\t"
    "buffer_store_dwordx4 a[176:179], v33, s[48:51], s64 offen offset:0 nt\n\t"
    "v_mfma_f32_16x16x32_bf16 v[56:59], a[120:123], a[56:59], v[56:59]\n\t"
    "buffer_load_dwordx4 v23, s[40:43], s78 offen offset:3072 nt lds\n\t"
    "v_mfma_f32_16x16x32_bf16 v[56:59], a[124:127], a[60:63], v[56:59]\n\t"
    "ds_read_b128 a[100:103], v13 offset:0\n\t"
    "v_mul_f32 v243, v37, v243\n\t"
    "v_mfma_f32_16x16x32_bf16 v[232:235], a[128:131], v[64:67], 0\n\t"
    "s_add_u32 s79, s62, 65536\n\t"
    "ds_read_b128 a[104:107], v14 offset:0\n\t"
    "v_cvt_pk_bf16_f32 v240, v240, v241\n\t"
    "v_mfma_f32_16x16x32_bf16 v[232:235], a[132:135], v[76:79], v[232:235]\n\t"
    "s_waitcnt lgkmcnt(4)\n\t"
    "s_add_u32 m0, s77, 0\n\t"
    "v_exp_f32 v61, v61\n\t"
    "v_mfma_f32_16x16x32_bf16 v[232:235], a[136:139], v[88:91], v[232:235]\n\t"
    "s_waitcnt lgkmcnt(3)\n\t"
    "v_exp_f32 v62, v62\n\t"
    "v_exp_f32 v63, v63\n\t"
    "v_mfma_f32_16x16x32_bf16 v[232:235], a[140:143], v[100:103], v[232:235]\n\t"
    "v_cvt_pk_bf16_f32 v148, v60, v61\n\t"
    "v_cvt_pk_bf16_f32 v149, v62, v63\n\t"
    "v_mfma_f32_16x16x32_bf16 v[60:63], a[112:115], a[64:67], v[4:7]\n\t"
    "ds_read_b128 a[108:111], v15 offset:0\n\t"
    "v_cvt_pk_bf16_f32 v241, v242, v243\n\t"
    "v_mfma_f32_16x16x32_bf16 v[60:63], a[116:119], a[68:71], v[60:63]\n\t"
    "ds_read_b64_tr_b16 a[144:145], v19 offset:16384\n\t"
    "v_exp_f32 v40, v40\n\t"
    "v_mfma_f32_16x16x32_bf16 v[60:63], a[120:123], a[72:75], v[60:63]\n\t"
    "buffer_load_dwordx4 v24, s[44:47], s79 offen offset:0 nt lds\n\t"
    "v_mfma_f32_16x16x32_bf16 v[60:63], a[124:127], a[76:79], v[60:63]\n\t"
    "ds_read_b64_tr_b16 a[146:147], v19 offset:18432\n\t"
    "v_cvt_pk_bf16_f32 v48, v48, v49\n\t"
    "v_mfma_f32_16x16x32_bf16 v[236:239], a[128:131], v[68:71], 0\n\t"
    "v_exp_f32 v41, v41\n\t"
    "v_mfma_f32_16x16x32_bf16 v[236:239], a[132:135], v[80:83], v[236:239]\n\t"
    "ds_read_b64_tr_b16 a[148:149], v19 offset:20480\n\t"
    "v_exp_f32 v42, v42\n\t"
    "v_mfma_f32_16x16x32_bf16 v[236:239], a[136:139], v[92:95], v[236:239]\n\t"
    "v_exp_f32 v43, v43\n\t"
    "v_mfma_f32_16x16x32_bf16 v[236:239], a[140:143], v[104:107], v[236:239]\n\t"
    "ds_read_b64_tr_b16 a[150:151], v19 offset:22528\n\t"
    "v_cvt_pk_bf16_f32 v152, v40, v41\n\t"
    "v_mfma_f32_16x16x32_bf16 a[168:171], a[172:175], v[72:75], 0\n\t"
    "v_cvt_pk_bf16_f32 v153, v42, v43\n\t"
    "v_cvt_pk_bf16_f32 v50, v50, v51\n\t"
    "v_mfma_f32_16x16x32_bf16 a[168:171], a[172:175], v[84:87], a[168:171]\n\t"
    "buffer_load_dwordx4 v25, s[44:47], s79 offen offset:1024 nt lds\n\t"
    "v_accvgpr_write_b32 a20, v48\n\t"
    "v_mfma_f32_16x16x32_bf16 a[168:171], a[172:175], v[96:99], a[168:171]\n\t"
    "ds_read_b64_tr_b16 a[152:153], v19 offset:24576\n\t"
    "v_accvgpr_write_b32 a21, v50\n\t"
    "v_mfma_f32_16x16x32_bf16 a[168:171], a[172:175], v[108:111], a[168:171]\n\t"
    "v_exp_f32 v44, v44\n\t"
    "v_mfma_f32_16x16x32_bf16 v[40:43], a[112:115], a[80:83], v[8:11]\n\t"
    "ds_read_b64_tr_b16 a[154:155], v19 offset:26624\n\t"
    "v_cvt_pk_bf16_f32 v52, v52, v53\n\t"
    "v_mfma_f32_16x16x32_bf16 v[40:43], a[116:119], a[84:87], v[40:43]\n\t"
    "ds_write_b64 v32, v[240:241] offset:4160\n\t"
    "v_mfma_f32_16x16x32_bf16 v[40:43], a[120:123], a[88:91], v[40:43]\n\t"
    "v_exp_f32 v45, v45\n\t"
    "v_mfma_f32_16x16x32_bf16 v[40:43], a[124:127], a[92:95], v[40:43]\n\t"
    "ds_read_b64_tr_b16 a[156:157], v19 offset:28672\n\t"
    "v_cvt_pk_bf16_f32 v156, v44, v45\n\t"
    "v_mfma_f32_16x16x32_bf16 v[240:243], a[128:131], v[72:75], 0\n\t"
    "v_exp_f32 v46, v46\n\t"
    "v_mfma_f32_16x16x32_bf16 v[240:243], a[132:135], v[84:87], v[240:243]\n\t"
    "ds_read_b64_tr_b16 a[158:159], v19 offset:30720\n\t"
    "v_exp_f32 v47, v47\n\t"
    "v_mfma_f32_16x16x32_bf16 v[240:243], a[136:139], v[96:99], v[240:243]\n\t"
    "v_cvt_pk_bf16_f32 v157, v46, v47\n\t"
    "v_cvt_pk_bf16_f32 v54, v54, v55\n\t"
    "v_mfma_f32_16x16x32_bf16 v[240:243], a[140:143], v[108:111], v[240:243]\n\t"
    "s_waitcnt lgkmcnt(4)\n\t"
    "buffer_load_dwordx4 v26, s[44:47], s79 offen offset:2048 nt lds\n\t"
    "v_mfma_f32_16x16x32_bf16 v[44:47], a[96:99], a[48:51], v[0:3]\n\t"
    "buffer_load_dwordx4 v27, s[44:47], s79 offen offset:3072 nt lds\n\t"
    "v_mfma_f32_16x16x32_bf16 v[44:47], a[100:103], a[52:55], v[44:47]\n\t"
    "v_accvgpr_write_b32 a22, v52\n\t"
    "v_accvgpr_write_b32 a23, v54\n\t"
    "v_mul_f32 v252, v37, v252\n\t"
    "v_mfma_f32_16x16x32_bf16 v[44:47], a[104:107], a[56:59], v[44:47]\n\t"
    "v_exp_f32 v56, v56\n\t"
    "v_mfma_f32_16x16x32_bf16 v[44:47], a[108:111], a[60:63], v[44:47]\n\t"
    "v_exp_f32 v57, v57\n\t"
    "v_mfma_f32_16x16x32_bf16 v[244:247], a[144:147], v[64:67], 0\n\t"
    "v_exp_f32 v58, v58\n\t"
    "v_cvt_pk_bf16_f32 v150, v56, v57\n\t"
    "v_mfma_f32_16x16x32_bf16 v[244:247], a[148:151], v[76:79], v[244:247]\n\t"
    "s_waitcnt lgkmcnt(0)\n\t"
    "v_exp_f32 v59, v59\n\t"
    "v_mfma_f32_16x16x32_bf16 v[244:247], a[152:155], v[88:91], v[244:247]\n\t"
    "s_waitcnt vmcnt(21) lgkmcnt(0)\n\t"
    "v_cvt_pk_bf16_f32 v151, v58, v59\n\t"
    "v_mul_f32 v253, v37, v253\n\t"
    "v_mfma_f32_16x16x32_bf16 v[244:247], a[156:159], v[100:103], v[244:247]\n\t"
    "s_barrier\n\t"
    "ds_read_b128 a[112:115], v12 offset:4096\n\t"
    "v_mul_f32 v254, v37, v254\n\t"
    "v_mfma_f32_16x16x32_bf16 v[48:51], a[96:99], a[64:67], v[4:7]\n\t"
    "s_mov_b32 s78, s59\n\t"
    "ds_read_b128 a[116:119], v13 offset:4096\n\t"
    "v_mul_f32 v255, v37, v255\n\t"
    "v_mfma_f32_16x16x32_bf16 v[48:51], a[100:103], a[68:71], v[48:51]\n\t"
    "s_mov_b32 m0, s76\n\t"
    "v_exp_f32 v60, v60\n\t"
    "v_cvt_pk_bf16_f32 v252, v252, v253\n\t"
    "v_mfma_f32_16x16x32_bf16 v[48:51], a[104:107], a[72:75], v[48:51]\n\t"
    "ds_read_b128 a[120:123], v14 offset:4096\n\t"
    "v_cvt_pk_bf16_f32 v253, v254, v255\n\t"
    "v_mfma_f32_16x16x32_bf16 v[48:51], a[108:111], a[76:79], v[48:51]\n\t"
    "buffer_load_dwordx4 v20, s[40:43], s78 offen offset:0 nt lds\n\t"
    "v_mfma_f32_16x16x32_bf16 v[248:251], a[144:147], v[68:71], 0\n\t"
    "ds_read_b128 a[124:127], v15 offset:4096\n\t"
    "v_exp_f32 v61, v61\n\t"
    "v_mfma_f32_16x16x32_bf16 v[248:251], a[148:151], v[80:83], v[248:251]\n\t"
    "ds_read_b64_tr_b16 a[128:129], v16 offset:32768\n\t"
    "v_cvt_pk_bf16_f32 v154, v60, v61\n\t"
    "v_mfma_f32_16x16x32_bf16 v[248:251], a[152:155], v[92:95], v[248:251]\n\t"
    "v_exp_f32 v62, v62\n\t"
    "v_mfma_f32_16x16x32_bf16 v[248:251], a[156:159], v[104:107], v[248:251]\n\t"
    "ds_read_b64_tr_b16 a[130:131], v16 offset:34816\n\t"
    "ds_read_b64_tr_b16 a[132:133], v16 offset:36864\n\t"
    "v_mfma_f32_16x16x32_bf16 v[52:55], a[96:99], a[80:83], v[8:11]\n\t"
    "v_exp_f32 v63, v63\n\t"
    "v_mfma_f32_16x16x32_bf16 v[52:55], a[100:103], a[84:87], v[52:55]\n\t"
    "buffer_store_dwordx4 a[180:183], v33, s[48:51], s64 offen offset:2048 nt\n\t"
    "v_mfma_f32_16x16x32_bf16 v[52:55], a[104:107], a[88:91], v[52:55]\n\t"
    "v_cvt_pk_bf16_f32 v155, v62, v63\n\t"
    "v_mfma_f32_16x16x32_bf16 v[52:55], a[108:111], a[92:95], v[52:55]\n\t"
    "ds_read_b64_tr_b16 a[134:135], v16 offset:38912\n\t"
    "ds_read_b64_tr_b16 a[136:137], v16 offset:40960\n\t"
    "v_mfma_f32_16x16x32_bf16 v[56:59], a[228:231], a[24:27], 0\n\t"
    "v_exp_f32 v40, v40\n\t"
    "v_mfma_f32_16x16x32_bf16 v[56:59], a[232:235], a[24:27], v[56:59]\n\t"
    "ds_write_b64 v32, v[252:253] offset:4192\n\t"
    "v_mfma_f32_16x16x32_bf16 v[60:63], a[236:239], a[24:27], 0\n\t"
    "v_exp_f32 v41, v41\n\t"
    "v_mfma_f32_16x16x32_bf16 v[60:63], a[240:243], a[24:27], v[60:63]\n\t"
    "ds_read_b64_tr_b16 a[138:139], v16 offset:43008\n\t"
    "v_cvt_pk_bf16_f32 v158, v40, v41\n\t"
    "v_mfma_f32_16x16x32_bf16 v[252:255], a[144:147], v[72:75], 0\n\t"
    "ds_read_b128 a[216:219], v34 offset:4096\n\t"
    "ds_read_b128 a[220:223], v34 offset:5248\n\t"
    "v_mfma_f32_16x16x32_bf16 v[252:255], a[148:151], v[84:87], v[252:255]\n\t"
    "ds_read_b64_tr_b16 a[140:141], v16 offset:45056\n\t"
    "v_exp_f32 v42, v42\n\t"
    "v_mfma_f32_16x16x32_bf16 v[252:255], a[152:155], v[96:99], v[252:255]\n\t"
    "ds_read_b64_tr_b16 a[142:143], v16 offset:47104\n\t"
    "v_exp_f32 v43, v43\n\t"
    "v_mfma_f32_16x16x32_bf16 v[252:255], a[156:159], v[108:111], v[252:255]\n\t"
    "s_waitcnt lgkmcnt(6)\n\t"
    "v_cvt_pk_bf16_f32 v159, v42, v43\n\t"
    "v_cvt_pk_bf16_f32 v56, v56, v57\n\t"
    "v_mfma_f32_16x16x32_bf16 v[40:43], a[112:115], a[48:51], v[0:3]\n\t"
    "buffer_load_dwordx4 v21, s[40:43], s78 offen offset:1024 nt lds\n\t"
    "v_mfma_f32_16x16x32_bf16 v[40:43], a[116:119], a[52:55], v[40:43]\n\t"
    "ds_read_b128 a[96:99], v12 offset:8192\n\t"
    "v_cvt_pk_bf16_f32 v58, v58, v59\n\t"
    "v_mfma_f32_16x16x32_bf16 v[40:43], a[120:123], a[56:59], v[40:43]\n\t"
    "ds_read_b128 a[100:103], v13 offset:8192\n\t"
    "v_accvgpr_write_b32 a24, v56\n\t"
    "v_mfma_f32_16x16x32_bf16 v[40:43], a[124:127], a[60:63], v[40:43]\n\t"
    "v_exp_f32 v44, v44\n\t"
    "v_mfma_f32_16x16x32_bf16 v[160:163], a[128:131], v[112:115], v[160:163]\n\t"
    "ds_read_b128 a[104:107], v14 offset:8192\n\t"
    "v_exp_f32 v45, v45\n\t"
    "v_mfma_f32_16x16x32_bf16 v[160:163], a[132:135], v[124:127], v[160:163]\n\t"
    "s_waitcnt lgkmcnt(4)\n\t"
    "v_exp_f32 v46, v46\n\t"
    "v_mfma_f32_16x16x32_bf16 v[160:163], a[136:139], v[136:139], v[160:163]\n\t"
    "s_waitcnt lgkmcnt(3)\n\t"
    "v_exp_f32 v47, v47\n\t"
    "v_mfma_f32_16x16x32_bf16 v[160:163], a[140:143], v[148:151], v[160:163]\n\t"
    "v_cvt_pk_bf16_f32 v64, v44, v45\n\t"
    "v_cvt_pk_bf16_f32 v65, v46, v47\n\t"
    "v_mfma_f32_16x16x32_bf16 v[44:47], a[112:115], a[64:67], v[4:7]\n\t"
    "ds_read_b128 a[108:111], v15 offset:8192\n\t"
    "v_accvgpr_write_b32 a25, v58\n\t"
    "v_mfma_f32_16x16x32_bf16 v[44:47], a[116:119], a[68:71], v[44:47]\n\t"
    "buffer_load_dwordx4 v22, s[40:43], s78 offen offset:2048 nt lds\n\t"
    "v_mfma_f32_16x16x32_bf16 v[44:47], a[120:123], a[72:75], v[44:47]\n\t"
    "ds_read_b64_tr_b16 a[144:145], v17 offset:32768\n\t"
    "v_cvt_pk_bf16_f32 v60, v60, v61\n\t"
    "v_mfma_f32_16x16x32_bf16 v[44:47], a[124:127], a[76:79], v[44:47]\n\t"
    "ds_read_b64_tr_b16 a[146:147], v17 offset:34816\n\t"
    "v_exp_f32 v48, v48\n\t"
    "v_mfma_f32_16x16x32_bf16 v[164:167], a[128:131], v[116:119], v[164:167]\n\t"
    "ds_read_b64_tr_b16 a[148:149], v17 offset:36864\n\t"
    "v_cvt_pk_bf16_f32 v62, v62, v63\n\t"
    "v_mfma_f32_16x16x32_bf16 v[164:167], a[132:135], v[128:131], v[164:167]\n\t"
    "v_exp_f32 v49, v49\n\t"
    "v_accvgpr_write_b32 a26, v60\n\t"
    "v_mfma_f32_16x16x32_bf16 v[164:167], a[136:139], v[140:143], v[164:167]\n\t"
    "v_exp_f32 v50, v50\n\t"
    "v_exp_f32 v51, v51\n\t"
    "v_mfma_f32_16x16x32_bf16 v[164:167], a[140:143], v[152:155], v[164:167]\n\t"
    "v_cvt_pk_bf16_f32 v68, v48, v49\n\t"
    "v_cvt_pk_bf16_f32 v69, v50, v51\n\t"
    "v_mfma_f32_16x16x32_bf16 v[48:51], a[112:115], a[80:83], v[8:11]\n\t"
    "ds_read_b64_tr_b16 a[150:151], v17 offset:38912\n\t"
    "v_accvgpr_write_b32 a27, v62\n\t"
    "v_mfma_f32_16x16x32_bf16 v[48:51], a[116:119], a[84:87], v[48:51]\n\t"
    "ds_read_b64_tr_b16 a[152:153], v17 offset:40960\n\t"
    "v_exp_f32 v52, v52\n\t"
    "v_mfma_f32_16x16x32_bf16 v[48:51], a[120:123], a[88:91], v[48:51]\n\t"
    "buffer_load_dwordx4 v23, s[40:43], s78 offen offset:3072 nt lds\n\t"
    "v_mfma_f32_16x16x32_bf16 v[48:51], a[124:127], a[92:95], v[48:51]\n\t"
    "ds_read_b64_tr_b16 a[154:155], v17 offset:43008\n\t"
    "ds_read_b64_tr_b16 a[156:157], v17 offset:45056\n\t"
    "v_mfma_f32_16x16x32_bf16 v[168:171], a[128:131], v[120:123], v[168:171]\n\t"
    "s_add_u32 s79, s62, 65664\n\t"
    "v_exp_f32 v53, v53\n\t"
    "v_mfma_f32_16x16x32_bf16 v[168:171], a[132:135], v[132:135], v[168:171]\n\t"
    "s_add_u32 m0, s77, 16384\n\t"
    "v_exp_f32 v54, v54\n\t"
    "v_cvt_pk_bf16_f32 v72, v52, v53\n\t"
    "v_mfma_f32_16x16x32_bf16 v[168:171], a[136:139], v[144:147], v[168:171]\n\t"
    "ds_read_b64_tr_b16 a[158:159], v17 offset:47104\n\t"
    "v_exp_f32 v55, v55\n\t"
    "v_mfma_f32_16x16x32_bf16 v[168:171], a[140:143], v[156:159], v[168:171]\n\t"
    "s_waitcnt lgkmcnt(5)\n\t"
    "v_cvt_pk_bf16_f32 v73, v54, v55\n\t"
    "v_mfma_f32_16x16x32_bf16 v[52:55], a[96:99], a[48:51], v[0:3]\n\t"
    "ds_read_b128 a[112:115], v12 offset:12288\n\t"
    "v_mfma_f32_16x16x32_bf16 v[52:55], a[100:103], a[52:55], v[52:55]\n\t"
    "buffer_load_dwordx4 v24, s[44:47], s79 offen offset:0 nt lds\n\t"
    "v_mfma_f32_16x16x32_bf16 v[52:55], a[104:107], a[56:59], v[52:55]\n\t"
    "ds_read_b128 a[116:119], v13 offset:12288\n\t"
    "v_mfma_f32_16x16x32_bf16 v[52:55], a[108:111], a[60:63], v[52:55]\n\t"
    "buffer_store_dwordx4 a[184:187], v33, s[48:51], s65 offen offset:0 nt\n\t"
    "v_mfma_f32_16x16x32_bf16 v[172:175], a[144:147], v[112:115], v[172:175]\n\t"
    "s_waitcnt lgkmcnt(3)\n\t"
    "v_exp_f32 v40, v40\n\t"
    "v_mfma_f32_16x16x32_bf16 v[172:175], a[148:151], v[124:127], v[172:175]\n\t"
    "ds_read_b128 a[120:123], v14 offset:12288\n\t"
    "v_mfma_f32_16x16x32_bf16 v[172:175], a[152:155], v[136:139], v[172:175]\n\t"
    "s_waitcnt lgkmcnt(3)\n\t"
    "v_exp_f32 v41, v41\n\t"
    "v_mfma_f32_16x16x32_bf16 v[172:175], a[156:159], v[148:151], v[172:175]\n\t"
    "v_exp_f32 v42, v42\n\t"
    "v_mfma_f32_16x16x32_bf16 v[56:59], a[96:99], a[64:67], v[4:7]\n\t"
    "ds_read_b128 a[124:127], v15 offset:12288\n\t"
    "v_cvt_pk_bf16_f32 v66, v40, v41\n\t"
    "v_mfma_f32_16x16x32_bf16 v[56:59], a[100:103], a[68:71], v[56:59]\n\t"
    "buffer_load_dwordx4 v25, s[44:47], s79 offen offset:1024 nt lds\n\t"
    "v_mfma_f32_16x16x32_bf16 v[56:59], a[104:107], a[72:75], v[56:59]\n\t"
    "ds_read_b64_tr_b16 a[128:129], v18 offset:32768\n\t"
    "ds_read_b64_tr_b16 a[130:131], v18 offset:34816\n\t"
    "v_mfma_f32_16x16x32_bf16 v[56:59], a[108:111], a[76:79], v[56:59]\n\t"
    "v_exp_f32 v43, v43\n\t"
    "v_mfma_f32_16x16x32_bf16 v[176:179], a[144:147], v[116:119], v[176:179]\n\t"
    "v_cvt_pk_bf16_f32 v67, v42, v43\n\t"
    "v_mfma_f32_16x16x32_bf16 v[176:179], a[148:151], v[128:131], v[176:179]\n\t"
    "v_exp_f32 v44, v44\n\t"
    "v_mfma_f32_16x16x32_bf16 v[176:179], a[152:155], v[140:143], v[176:179]\n\t"
    "v_exp_f32 v45, v45\n\t"
    "v_mfma_f32_16x16x32_bf16 v[176:179], a[156:159], v[152:155], v[176:179]\n\t"
    "v_exp_f32 v46, v46\n\t"
    "v_mfma_f32_16x16x32_bf16 a[160:163], a[172:175], v[112:115], a[160:163]\n\t"
    "ds_read_b64_tr_b16 a[132:133], v18 offset:36864\n\t"
    "v_cvt_pk_bf16_f32 v70, v44, v45\n\t"
    "v_mfma_f32_16x16x32_bf16 a[160:163], a[172:175], v[124:127], a[160:163]\n\t"
    "v_exp_f32 v47, v47\n\t"
    "v_mfma_f32_16x16x32_bf16 a[160:163], a[172:175], v[136:139], a[160:163]\n\t"
    "v_cvt_pk_bf16_f32 v71, v46, v47\n\t"
    "v_mfma_f32_16x16x32_bf16 a[160:163], a[172:175], v[148:151], a[160:163]\n\t"
    "ds_read_b64_tr_b16 a[134:135], v18 offset:38912\n\t"
    "ds_read_b64_tr_b16 a[136:137], v18 offset:40960\n\t"
    "v_mfma_f32_16x16x32_bf16 v[60:63], a[96:99], a[80:83], v[8:11]\n\t"
    "buffer_load_dwordx4 v26, s[44:47], s79 offen offset:2048 nt lds\n\t"
    "v_mfma_f32_16x16x32_bf16 v[60:63], a[100:103], a[84:87], v[60:63]\n\t"
    "v_exp_f32 v48, v48\n\t"
    "v_mfma_f32_16x16x32_bf16 v[60:63], a[104:107], a[88:91], v[60:63]\n\t"
    "v_exp_f32 v49, v49\n\t"
    "v_mfma_f32_16x16x32_bf16 v[60:63], a[108:111], a[92:95], v[60:63]\n\t"
    "v_exp_f32 v50, v50\n\t"
    "v_mfma_f32_16x16x32_bf16 v[40:43], a[228:231], a[28:31], 0\n\t"
    "ds_read_b64_tr_b16 a[138:139], v18 offset:43008\n\t"
    "v_cvt_pk_bf16_f32 v74, v48, v49\n\t"
    "v_mfma_f32_16x16x32_bf16 v[40:43], a[232:235], a[28:31], v[40:43]\n\t"
    "v_exp_f32 v51, v51\n\t"
    "v_mfma_f32_16x16x32_bf16 v[44:47], a[236:239], a[28:31], 0\n\t"
    "v_cvt_pk_bf16_f32 v75, v50, v51\n\t"
    "v_mfma_f32_16x16x32_bf16 v[44:47], a[240:243], a[28:31], v[44:47]\n\t"
    "ds_read_b64_tr_b16 a[140:141], v18 offset:45056\n\t"
    "ds_read_b64_tr_b16 a[142:143], v18 offset:47104\n\t"
    "v_mfma_f32_16x16x32_bf16 v[180:183], a[144:147], v[120:123], v[180:183]\n\t"
    "buffer_load_dwordx4 v27, s[44:47], s79 offen offset:3072 nt lds\n\t"
    "v_mfma_f32_16x16x32_bf16 v[180:183], a[148:151], v[132:135], v[180:183]\n\t"
    "v_exp_f32 v52, v52\n\t"
    "v_mfma_f32_16x16x32_bf16 v[180:183], a[152:155], v[144:147], v[180:183]\n\t"
    "v_exp_f32 v53, v53\n\t"
    "v_mfma_f32_16x16x32_bf16 v[180:183], a[156:159], v[156:159], v[180:183]\n\t"
    "s_waitcnt vmcnt(19) lgkmcnt(3)\n\t"
    "v_exp_f32 v54, v54\n\t"
    "v_mfma_f32_16x16x32_bf16 v[48:51], a[112:115], a[48:51], v[0:3]\n\t"
    "ds_read_b128 a[96:99], v12 offset:16384\n\t"
    "v_cvt_pk_bf16_f32 v76, v52, v53\n\t"
    "v_mfma_f32_16x16x32_bf16 v[48:51], a[116:119], a[52:55], v[48:51]\n\t"
    "ds_read_b128 a[100:103], v13 offset:16384\n\t"
    "v_cvt_pk_bf16_f32 v40, v40, v41\n\t"
    "v_mfma_f32_16x16x32_bf16 v[48:51], a[120:123], a[56:59], v[48:51]\n\t"
    "v_exp_f32 v55, v55\n\t"
    "v_mfma_f32_16x16x32_bf16 v[48:51], a[124:127], a[60:63], v[48:51]\n\t"
    "ds_read_b128 a[104:107], v14 offset:16384\n\t"
    "v_cvt_pk_bf16_f32 v77, v54, v55\n\t"
    "v_mfma_f32_16x16x32_bf16 v[184:187], a[128:131], v[112:115], v[184:187]\n\t"
    "ds_read_b128 a[32:35], v28 offset:0\n\t"
    "v_cvt_pk_bf16_f32 v42, v42, v43\n\t"
    "v_mfma_f32_16x16x32_bf16 v[184:187], a[132:135], v[124:127], v[184:187]\n\t"
    "s_waitcnt lgkmcnt(4)\n\t"
    "ds_read_b128 a[108:111], v15 offset:16384\n\t"
    "v_accvgpr_write_b32 a28, v40\n\t"
    "v_mfma_f32_16x16x32_bf16 v[184:187], a[136:139], v[136:139], v[184:187]\n\t"
    "ds_read_b128 a[36:39], v29 offset:0\n\t"
    "v_accvgpr_write_b32 a29, v42\n\t"
    "v_mfma_f32_16x16x32_bf16 v[184:187], a[140:143], v[148:151], v[184:187]\n\t"
    "ds_read_b64_tr_b16 a[144:145], v19 offset:32768\n\t"
    "v_cvt_pk_bf16_f32 v44, v44, v45\n\t"
    "v_mfma_f32_16x16x32_bf16 v[52:55], a[112:115], a[64:67], v[4:7]\n\t"
    "v_exp_f32 v56, v56\n\t"
    "v_mfma_f32_16x16x32_bf16 v[52:55], a[116:119], a[68:71], v[52:55]\n\t"
    "ds_read_b64_tr_b16 a[146:147], v19 offset:34816\n\t"
    "v_cvt_pk_bf16_f32 v46, v46, v47\n\t"
    "v_mfma_f32_16x16x32_bf16 v[52:55], a[120:123], a[72:75], v[52:55]\n\t"
    "v_exp_f32 v57, v57\n\t"
    "v_mfma_f32_16x16x32_bf16 v[52:55], a[124:127], a[76:79], v[52:55]\n\t"
    "ds_read_b64_tr_b16 a[148:149], v19 offset:36864\n\t"
    "v_cvt_pk_bf16_f32 v80, v56, v57\n\t"
    "v_mfma_f32_16x16x32_bf16 v[188:191], a[128:131], v[116:119], v[188:191]\n\t"
    "v_exp_f32 v58, v58\n\t"
    "v_mfma_f32_16x16x32_bf16 v[188:191], a[132:135], v[128:131], v[188:191]\n\t"
    "v_exp_f32 v59, v59\n\t"
    "v_mfma_f32_16x16x32_bf16 v[188:191], a[136:139], v[140:143], v[188:191]\n\t"
    "ds_read_b64_tr_b16 a[150:151], v19 offset:38912\n\t"
    "v_cvt_pk_bf16_f32 v81, v58, v59\n\t"
    "v_mfma_f32_16x16x32_bf16 v[188:191], a[140:143], v[152:155], v[188:191]\n\t"
    "ds_read_b128 a[40:43], v30 offset:0\n\t"
    "v_accvgpr_write_b32 a30, v44\n\t"
    "v_mfma_f32_16x16x32_bf16 v[56:59], a[112:115], a[80:83], v[8:11]\n\t"
    "ds_read_b64_tr_b16 a[152:153], v19 offset:40960\n\t"
    "v_accvgpr_write_b32 a31, v46\n\t"
    "v_mfma_f32_16x16x32_bf16 v[56:59], a[116:119], a[84:87], v[56:59]\n\t"
    "ds_read_b128 a[44:47], v31 offset:0\n\t"
    "ds_read_b64_tr_b16 a[154:155], v19 offset:43008\n\t"
    "v_mfma_f32_16x16x32_bf16 v[56:59], a[120:123], a[88:91], v[56:59]\n\t"
    "v_exp_f32 v60, v60\n\t"
    "v_mfma_f32_16x16x32_bf16 v[56:59], a[124:127], a[92:95], v[56:59]\n\t"
    "ds_read_b64_tr_b16 a[156:157], v19 offset:45056\n\t"
    "ds_read_b64_tr_b16 a[158:159], v19 offset:47104\n\t"
    "v_mfma_f32_16x16x32_bf16 v[192:195], a[128:131], v[120:123], v[192:195]\n\t"
    "v_exp_f32 v61, v61\n\t"
    "v_mfma_f32_16x16x32_bf16 v[192:195], a[132:135], v[132:135], v[192:195]\n\t"
    "v_exp_f32 v62, v62\n\t"
    "v_mfma_f32_16x16x32_bf16 v[192:195], a[136:139], v[144:147], v[192:195]\n\t"
    "v_exp_f32 v63, v63\n\t"
    "v_mfma_f32_16x16x32_bf16 v[192:195], a[140:143], v[156:159], v[192:195]\n\t"
    "s_waitcnt lgkmcnt(5)\n\t"
    "v_cvt_pk_bf16_f32 v84, v60, v61\n\t"
    "v_cvt_pk_bf16_f32 v85, v62, v63\n\t"
    "v_mfma_f32_16x16x32_bf16 v[60:63], a[96:99], a[48:51], v[0:3]\n\t"
    "buffer_store_dwordx4 a[188:191], v33, s[48:51], s65 offen offset:2048 nt\n\t"
    "v_mfma_f32_16x16x32_bf16 v[60:63], a[100:103], a[52:55], v[60:63]\n\t"
    "v_exp_f32 v48, v48\n\t"
    "v_mfma_f32_16x16x32_bf16 v[60:63], a[104:107], a[56:59], v[60:63]\n\t"
    "v_exp_f32 v49, v49\n\t"
    "v_mfma_f32_16x16x32_bf16 v[60:63], a[108:111], a[60:63], v[60:63]\n\t"
    "v_exp_f32 v50, v50\n\t"
    "v_mfma_f32_16x16x32_bf16 v[196:199], a[144:147], v[112:115], v[196:199]\n\t"
    "v_exp_f32 v51, v51\n\t"
    "v_mfma_f32_16x16x32_bf16 v[196:199], a[148:151], v[124:127], v[196:199]\n\t"
    "s_waitcnt lgkmcnt(0)\n\t"
    "v_cvt_pk_bf16_f32 v78, v48, v49\n\t"
    "v_cvt_pk_bf16_f32 v79, v50, v51\n\t"
    "v_mfma_f32_16x16x32_bf16 v[196:199], a[152:155], v[136:139], v[196:199]\n\t"
    "s_waitcnt vmcnt(24) lgkmcnt(0)\n\t"
    "v_exp_f32 v52, v52\n\t"
    "v_mfma_f32_16x16x32_bf16 v[196:199], a[156:159], v[148:151], v[196:199]\n\t"
    "s_barrier\n\t"
    "ds_read_b128 a[112:115], v12 offset:20480\n\t"
    "v_mfma_f32_16x16x32_bf16 v[40:43], a[96:99], a[64:67], v[4:7]\n\t"
    "s_add_u32 s78, s57, 4096\n\t"
    "ds_read_b128 a[116:119], v13 offset:20480\n\t"
    "v_mfma_f32_16x16x32_bf16 v[40:43], a[100:103], a[68:71], v[40:43]\n\t"
    "s_mov_b32 m0, s73\n\t"
    "v_exp_f32 v53, v53\n\t"
    "v_mfma_f32_16x16x32_bf16 v[40:43], a[104:107], a[72:75], v[40:43]\n\t"
    "ds_read_b128 a[120:123], v14 offset:20480\n\t"
    "v_cvt_pk_bf16_f32 v82, v52, v53\n\t"
    "v_mfma_f32_16x16x32_bf16 v[40:43], a[108:111], a[76:79], v[40:43]\n\t"
    "buffer_load_dwordx4 v20, s[40:43], s78 offen offset:0 nt lds\n\t"
    "v_mfma_f32_16x16x32_bf16 v[200:203], a[144:147], v[116:119], v[200:203]\n\t"
    "ds_read_b128 a[124:127], v15 offset:20480\n\t"
    "ds_read_b64_tr_b16 a[128:129], v16 offset:49152\n\t"
    "v_mfma_f32_16x16x32_bf16 v[200:203], a[148:151], v[128:131], v[200:203]\n\t"
    "v_exp_f32 v54, v54\n\t"
    "v_mfma_f32_16x16x32_bf16 v[200:203], a[152:155], v[140:143], v[200:203]\n\t"
    "v_exp_f32 v55, v55\n\t"
    "v_mfma_f32_16x16x32_bf16 v[200:203], a[156:159], v[152:155], v[200:203]\n\t"
    "ds_read_b64_tr_b16 a[130:131], v16 offset:51200\n\t"
    "v_cvt_pk_bf16_f32 v83, v54, v55\n\t"
    "v_mfma_f32_16x16x32_bf16 v[44:47], a[96:99], a[80:83], v[8:11]\n\t"
    "v_exp_f32 v56, v56\n\t"
    "v_mfma_f32_16x16x32_bf16 v[44:47], a[100:103], a[84:87], v[44:47]\n\t"
    "ds_read_b64_tr_b16 a[132:133], v16 offset:53248\n\t"
    "ds_read_b64_tr_b16 a[134:135], v16 offset:55296\n\t"
    "v_mfma_f32_16x16x32_bf16 v[44:47], a[104:107], a[88:91], v[44:47]\n\t"
    "v_exp_f32 v57, v57\n\t"
    "v_mfma_f32_16x16x32_bf16 v[44:47], a[108:111], a[92:95], v[44:47]\n\t"
    "v_exp_f32 v58, v58\n\t"
    "v_mfma_f32_16x16x32_bf16 v[48:51], a[228:231], a[32:35], 0\n\t"
    "ds_read_b64_tr_b16 a[136:137], v16 offset:57344\n\t"
    "v_cvt_pk_bf16_f32 v86, v56, v57\n\t"
    "v_mfma_f32_16x16x32_bf16 v[48:51], a[232:235], a[32:35], v[48:51]\n\t"
    "v_exp_f32 v59, v59\n\t"
    "v_mfma_f32_16x16x32_bf16 v[52:55], a[236:239], a[32:35], 0\n\t"
    "ds_read_b64_tr_b16 a[138:139], v16 offset:59392\n\t"
    "v_cvt_pk_bf16_f32 v87, v58, v59\n\t"
    "v_mfma_f32_16x16x32_bf16 v[52:55], a[240:243], a[32:35], v[52:55]\n\t"
    "buffer_load_dwordx4 v21, s[40:43], s78 offen offset:1024 nt lds\n\t"
    "v_mfma_f32_16x16x32_bf16 v[204:207], a[144:147], v[120:123], v[204:207]\n\t"
    "ds_read_b64_tr_b16 a[140:141], v16 offset:61440\n\t"
    "ds_read_b64_tr_b16 a[142:143], v16 offset:63488\n\t"
    "v_mfma_f32_16x16x32_bf16 v[204:207], a[148:151], v[132:135], v[204:207]\n\t"
    "v_exp_f32 v60, v60\n\t"
    "v_mfma_f32_16x16x32_bf16 v[204:207], a[152:155], v[144:147], v[204:207]\n\t"
    "v_exp_f32 v61, v61\n\t"
    "v_mfma_f32_16x16x32_bf16 v[204:207], a[156:159], v[156:159], v[204:207]\n\t"
    "s_waitcnt lgkmcnt(4)\n\t"
    "v_exp_f32 v62, v62\n\t"
    "v_mfma_f32_16x16x32_bf16 v[56:59], a[112:115], a[48:51], v[0:3]\n\t"
    "ds_read_b128 a[96:99], v12 offset:24576\n\t"
    "v_cvt_pk_bf16_f32 v88, v60, v61\n\t"
    "v_mfma_f32_16x16x32_bf16 v[56:59], a[116:119], a[52:55], v[56:59]\n\t"
    "v_exp_f32 v63, v63\n\t"
    "v_mfma_f32_16x16x32_bf16 v[56:59], a[120:123], a[56:59], v[56:59]\n\t"
    "ds_read_b128 a[100:103], v13 offset:24576\n\t"
    "v_cvt_pk_bf16_f32 v89, v62, v63\n\t"
    "v_mfma_f32_16x16x32_bf16 v[56:59], a[124:127], a[60:63], v[56:59]\n\t"
    "buffer_load_dwordx4 v22, s[40:43], s78 offen offset:2048 nt lds\n\t"
    "v_mfma_f32_16x16x32_bf16 v[208:211], a[128:131], v[112:115], v[208:211]\n\t"
    "ds_read_b128 a[104:107], v14 offset:24576\n\t"
    "v_cvt_pk_bf16_f32 v48, v48, v49\n\t"
    "v_mfma_f32_16x16x32_bf16 v[208:211], a[132:135], v[124:127], v[208:211]\n\t"
    "s_waitcnt lgkmcnt(3)\n\t"
    "v_exp_f32 v40, v40\n\t"
    "v_mfma_f32_16x16x32_bf16 v[208:211], a[136:139], v[136:139], v[208:211]\n\t"
    "v_exp_f32 v41, v41\n\t"
    "v_mfma_f32_16x16x32_bf16 v[208:211], a[140:143], v[148:151], v[208:211]\n\t"
    "ds_read_b128 a[108:111], v15 offset:24576\n\t"
    "v_cvt_pk_bf16_f32 v92, v40, v41\n\t"
    "v_mfma_f32_16x16x32_bf16 v[60:63], a[112:115], a[64:67], v[4:7]\n\t"
    "v_exp_f32 v42, v42\n\t"
    "v_mfma_f32_16x16x32_bf16 v[60:63], a[116:119], a[68:71], v[60:63]\n\t"
    "ds_read_b64_tr_b16 a[144:145], v17 offset:49152\n\t"
    "v_cvt_pk_bf16_f32 v50, v50, v51\n\t"
    "v_mfma_f32_16x16x32_bf16 v[60:63], a[120:123], a[72:75], v[60:63]\n\t"
    "v_exp_f32 v43, v43\n\t"
    "v_mfma_f32_16x16x32_bf16 v[60:63], a[124:127], a[76:79], v[60:63]\n\t"
    "ds_read_b64_tr_b16 a[146:147], v17 offset:51200\n\t"
    "v_cvt_pk_bf16_f32 v93, v42, v43\n\t"
    "v_mfma_f32_16x16x32_bf16 v[212:215], a[128:131], v[116:119], v[212:215]\n\t"
    "buffer_store_dwordx4 a[192:195], v33, s[48:51], s66 offen offset:0 nt\n\t"
    "v_mfma_f32_16x16x32_bf16 v[212:215], a[132:135], v[128:131], v[212:215]\n\t"
    "buffer_load_dwordx4 v23, s[40:43], s78 offen offset:3072 nt lds\n\t"
    "v_mfma_f32_16x16x32_bf16 v[212:215], a[136:139], v[140:143], v[212:215]\n\t"
    "ds_read_b64_tr_b16 a[148:149], v17 offset:53248\n\t"
    "v_accvgpr_write_b32 a32, v48\n\t"
    "v_mfma_f32_16x16x32_bf16 v[212:215], a[140:143], v[152:155], v[212:215]\n\t"
    "v_exp_f32 v44, v44\n\t"
    "v_mfma_f32_16x16x32_bf16 a[164:167], a[172:175], v[116:119], a[164:167]\n\t"
    "ds_read_b64_tr_b16 a[150:151], v17 offset:55296\n\t"
    "v_accvgpr_write_b32 a33, v50\n\t"
    "v_mfma_f32_16x16x32_bf16 a[164:167], a[172:175], v[128:131], a[164:167]\n\t"
    "v_exp_f32 v45, v45\n\t"
    "v_mfma_f32_16x16x32_bf16 a[164:167], a[172:175], v[140:143], a[164:167]\n\t"
    "ds_read_b64_tr_b16 a[152:153], v17 offset:57344\n\t"
    "v_cvt_pk_bf16_f32 v96, v44, v45\n\t"
    "v_mfma_f32_16x16x32_bf16 a[164:167], a[172:175], v[152:155], a[164:167]\n\t"
    "v_exp_f32 v46, v46\n\t"
    "v_mfma_f32_16x16x32_bf16 v[40:43], a[112:115], a[80:83], v[8:11]\n\t"
    "s_add_u32 s79, s62, 98304\n\t"
    "v_exp_f32 v47, v47\n\t"
    "v_mfma_f32_16x16x32_bf16 v[40:43], a[116:119], a[84:87], v[40:43]\n\t"
    "s_add_u32 m0, s77, 32768\n\t"
    "ds_read_b64_tr_b16 a[154:155], v17 offset:59392\n\t"
    "v_cvt_pk_bf16_f32 v97, v46, v47\n\t"
    "v_mfma_f32_16x16x32_bf16 v[40:43], a[120:123], a[88:91], v[40:43]\n\t"
    "buffer_load_dwordx4 v24, s[44:47], s79 offen offset:0 nt lds\n\t"
    "v_mfma_f32_16x16x32_bf16 v[40:43], a[124:127], a[92:95], v[40:43]\n\t"
    "ds_read_b64_tr_b16 a[156:157], v17 offset:61440\n\t"
    "v_cvt_pk_bf16_f32 v52, v52, v53\n\t"
    "v_mfma_f32_16x16x32_bf16 v[216:219], a[128:131], v[120:123], v[216:219]\n\t"
    "v_cvt_pk_bf16_f32 v54, v54, v55\n\t"
    "v_accvgpr_write_b32 a34, v52\n\t"
    "v_mfma_f32_16x16x32_bf16 v[216:219], a[132:135], v[132:135], v[216:219]\n\t"
    "v_accvgpr_write_b32 a35, v54\n\t"
    "v_mfma_f32_16x16x32_bf16 v[216:219], a[136:139], v[144:147], v[216:219]\n\t"
    "ds_read_b64_tr_b16 a[158:159], v17 offset:63488\n\t"
    "v_mfma_f32_16x16x32_bf16 v[216:219], a[140:143], v[156:159], v[216:219]\n\t"
    "s_waitcnt lgkmcnt(3)\n\t"
    "v_exp_f32 v56, v56\n\t"
    "v_mfma_f32_16x16x32_bf16 v[44:47], a[96:99], a[48:51], v[0:3]\n\t"
    "ds_read_b128 a[112:115], v12 offset:28672\n\t"
    "v_mfma_f32_16x16x32_bf16 v[44:47], a[100:103], a[52:55], v[44:47]\n\t"
    "v_exp_f32 v57, v57\n\t"
    "v_mfma_f32_16x16x32_bf16 v[44:47], a[104:107], a[56:59], v[44:47]\n\t"
    "ds_read_b128 a[116:119], v13 offset:28672\n\t"
    "v_cvt_pk_bf16_f32 v90, v56, v57\n\t"
    "v_mfma_f32_16x16x32_bf16 v[44:47], a[108:111], a[60:63], v[44:47]\n\t"
    "v_exp_f32 v58, v58\n\t"
    "v_mfma_f32_16x16x32_bf16 v[220:223], a[144:147], v[112:115], v[220:223]\n\t"
    "ds_read_b128 a[120:123], v14 offset:28672\n\t"
    "v_mfma_f32_16x16x32_bf16 v[220:223], a[148:151], v[124:127], v[220:223]\n\t"
    "s_waitcnt lgkmcnt(4)\n\t"
    "buffer_load_dwordx4 v25, s[44:47], s79 offen offset:1024 nt lds\n\t"
    "v_mfma_f32_16x16x32_bf16 v[220:223], a[152:155], v[136:139], v[220:223]\n\t"
    "s_waitcnt lgkmcnt(3)\n\t"
    "v_exp_f32 v59, v59\n\t"
    "v_mfma_f32_16x16x32_bf16 v[220:223], a[156:159], v[148:151], v[220:223]\n\t"
    "ds_read_b128 a[124:127], v15 offset:28672\n\t"
    "v_cvt_pk_bf16_f32 v91, v58, v59\n\t"
    "v_mfma_f32_16x16x32_bf16 v[48:51], a[96:99], a[64:67], v[4:7]\n\t"
    "v_exp_f32 v60, v60\n\t"
    "v_mfma_f32_16x16x32_bf16 v[48:51], a[100:103], a[68:71], v[48:51]\n\t"
    "ds_read_b64_tr_b16 a[128:129], v18 offset:49152\n\t"
    "ds_read_b64_tr_b16 a[130:131], v18 offset:51200\n\t"
    "v_mfma_f32_16x16x32_bf16 v[48:51], a[104:107], a[72:75], v[48:51]\n\t"
    "v_exp_f32 v61, v61\n\t"
    "v_mfma_f32_16x16x32_bf16 v[48:51], a[108:111], a[76:79], v[48:51]\n\t"
    "v_exp_f32 v62, v62\n\t"
    "v_mfma_f32_16x16x32_bf16 v[224:227], a[144:147], v[116:119], v[224:227]\n\t"
    "v_exp_f32 v63, v63\n\t"
    "v_mfma_f32_16x16x32_bf16 v[224:227], a[148:151], v[128:131], v[224:227]\n\t"
    "v_cvt_pk_bf16_f32 v94, v60, v61\n\t"
    "v_cvt_pk_bf16_f32 v95, v62, v63\n\t"
    "v_mfma_f32_16x16x32_bf16 v[224:227], a[152:155], v[140:143], v[224:227]\n\t"
    "ds_read_b64_tr_b16 a[132:133], v18 offset:53248\n\t"
    "ds_read_b64_tr_b16 a[134:135], v18 offset:55296\n\t"
    "v_mfma_f32_16x16x32_bf16 v[224:227], a[156:159], v[152:155], v[224:227]\n\t"
    "buffer_load_dwordx4 v26, s[44:47], s79 offen offset:2048 nt lds\n\t"
    "v_mfma_f32_16x16x32_bf16 v[52:55], a[96:99], a[80:83], v[8:11]\n\t"
    "v_exp_f32 v40, v40\n\t"
    "v_mfma_f32_16x16x32_bf16 v[52:55], a[100:103], a[84:87], v[52:55]\n\t"
    "v_exp_f32 v41, v41\n\t"
    "v_mfma_f32_16x16x32_bf16 v[52:55], a[104:107], a[88:91], v[52:55]\n\t"
    "ds_read_b64_tr_b16 a[136:137], v18 offset:57344\n\t"
    "v_cvt_pk_bf16_f32 v98, v40, v41\n\t"
    "v_mfma_f32_16x16x32_bf16 v[52:55], a[108:111], a[92:95], v[52:55]\n\t"
    "v_exp_f32 v42, v42\n\t"
    "v_mfma_f32_16x16x32_bf16 v[56:59], a[228:231], a[36:39], 0\n\t"
    "v_exp_f32 v43, v43\n\t"
    "v_mfma_f32_16x16x32_bf16 v[56:59], a[232:235], a[36:39], v[56:59]\n\t"
    "ds_read_b64_tr_b16 a[138:139], v18 offset:59392\n\t"
    "v_cvt_pk_bf16_f32 v99, v42, v43\n\t"
    "v_mfma_f32_16x16x32_bf16 v[60:63], a[236:239], a[36:39], 0\n\t"
    "v_exp_f32 v44, v44\n\t"
    "v_mfma_f32_16x16x32_bf16 v[60:63], a[240:243], a[36:39], v[60:63]\n\t"
    "ds_read_b64_tr_b16 a[140:141], v18 offset:61440\n\t"
    "ds_read_b64_tr_b16 a[142:143], v18 offset:63488\n\t"
    "v_mfma_f32_16x16x32_bf16 v[228:231], a[144:147], v[120:123], v[228:231]\n\t"
    "buffer_load_dwordx4 v27, s[44:47], s79 offen offset:3072 nt lds\n\t"
    "v_mfma_f32_16x16x32_bf16 v[228:231], a[148:151], v[132:135], v[228:231]\n\t"
    "v_exp_f32 v45, v45\n\t"
    "v_mfma_f32_16x16x32_bf16 v[228:231], a[152:155], v[144:147], v[228:231]\n\t"
    "v_exp_f32 v46, v46\n\t"
    "v_mfma_f32_16x16x32_bf16 v[228:231], a[156:159], v[156:159], v[228:231]\n\t"
    "s_waitcnt lgkmcnt(3)\n\t"
    "v_exp_f32 v47, v47\n\t"
    "v_mfma_f32_16x16x32_bf16 v[40:43], a[112:115], a[48:51], v[0:3]\n\t"
    "ds_read_b128 a[96:99], v12 offset:32768\n\t"
    "v_cvt_pk_bf16_f32 v100, v44, v45\n\t"
    "v_mfma_f32_16x16x32_bf16 v[40:43], a[116:119], a[52:55], v[40:43]\n\t"
    "v_cvt_pk_bf16_f32 v101, v46, v47\n\t"
    "v_cvt_pk_bf16_f32 v56, v56, v57\n\t"
    "v_mfma_f32_16x16x32_bf16 v[40:43], a[120:123], a[56:59], v[40:43]\n\t"
    "ds_read_b128 a[100:103], v13 offset:32768\n\t"
    "v_cvt_pk_bf16_f32 v58, v58, v59\n\t"
    "v_mfma_f32_16x16x32_bf16 v[40:43], a[124:127], a[60:63], v[40:43]\n\t"
    "buffer_store_dwordx4 a[196:199], v33, s[48:51], s66 offen offset:2048 nt\n\t"
    "v_mfma_f32_16x16x32_bf16 v[232:235], a[128:131], v[112:115], v[232:235]\n\t"
    "ds_read_b128 a[104:107], v14 offset:32768\n\t"
    "v_accvgpr_write_b32 a36, v56\n\t"
    "v_mfma_f32_16x16x32_bf16 v[232:235], a[132:135], v[124:127], v[232:235]\n\t"
    "s_waitcnt lgkmcnt(3)\n\t"
    "v_exp_f32 v48, v48\n\t"
    "v_mfma_f32_16x16x32_bf16 v[232:235], a[136:139], v[136:139], v[232:235]\n\t"
    "ds_read_b128 a[108:111], v15 offset:32768\n\t"
    "v_accvgpr_write_b32 a37, v58\n\t"
    "v_mfma_f32_16x16x32_bf16 v[232:235], a[140:143], v[148:151], v[232:235]\n\t"
    "v_exp_f32 v49, v49\n\t"
    "v_mfma_f32_16x16x32_bf16 v[44:47], a[112:115], a[64:67], v[4:7]\n\t"
    "v_exp_f32 v50, v50\n\t"
    "v_mfma_f32_16x16x32_bf16 v[44:47], a[116:119], a[68:71], v[44:47]\n\t"
    "ds_read_b64_tr_b16 a[144:145], v19 offset:49152\n\t"
    "v_cvt_pk_bf16_f32 v104, v48, v49\n\t"
    "v_mfma_f32_16x16x32_bf16 v[44:47], a[120:123], a[72:75], v[44:47]\n\t"
    "v_exp_f32 v51, v51\n\t"
    "v_mfma_f32_16x16x32_bf16 v[44:47], a[124:127], a[76:79], v[44:47]\n\t"
    "ds_read_b64_tr_b16 a[146:147], v19 offset:51200\n\t"
    "v_cvt_pk_bf16_f32 v105, v50, v51\n\t"
    "v_mfma_f32_16x16x32_bf16 v[236:239], a[128:131], v[116:119], v[236:239]\n\t"
    "v_exp_f32 v52, v52\n\t"
    "v_mfma_f32_16x16x32_bf16 v[236:239], a[132:135], v[128:131], v[236:239]\n\t"
    "ds_read_b64_tr_b16 a[148:149], v19 offset:53248\n\t"
    "v_cvt_pk_bf16_f32 v60, v60, v61\n\t"
    "v_mfma_f32_16x16x32_bf16 v[236:239], a[136:139], v[140:143], v[236:239]\n\t"
    "v_exp_f32 v53, v53\n\t"
    "v_mfma_f32_16x16x32_bf16 v[236:239], a[140:143], v[152:155], v[236:239]\n\t"
    "ds_read_b64_tr_b16 a[150:151], v19 offset:55296\n\t"
    "v_cvt_pk_bf16_f32 v108, v52, v53\n\t"
    "v_mfma_f32_16x16x32_bf16 a[168:171], a[172:175], v[120:123], a[168:171]\n\t"
    "v_exp_f32 v54, v54\n\t"
    "v_mfma_f32_16x16x32_bf16 a[168:171], a[172:175], v[132:135], a[168:171]\n\t"
    "v_exp_f32 v55, v55\n\t"
    "v_mfma_f32_16x16x32_bf16 a[168:171], a[172:175], v[144:147], a[168:171]\n\t"
    "ds_read_b64_tr_b16 a[152:153], v19 offset:57344\n\t"
    "v_cvt_pk_bf16_f32 v109, v54, v55\n\t"
    "v_mfma_f32_16x16x32_bf16 a[168:171], a[172:175], v[156:159], a[168:171]\n\t"
    "v_cvt_pk_bf16_f32 v62, v62, v63\n\t"
    "v_accvgpr_write_b32 a38, v60\n\t"
    "v_mfma_f32_16x16x32_bf16 v[48:51], a[112:115], a[80:83], v[8:11]\n\t"
    "ds_read_b64_tr_b16 a[154:155], v19 offset:59392\n\t"
    "v_accvgpr_write_b32 a39, v62\n\t"
    "v_mfma_f32_16x16x32_bf16 v[48:51], a[116:119], a[84:87], v[48:51]\n\t"
    "v_exp_f32 v40, v40\n\t"
    "v_mfma_f32_16x16x32_bf16 v[48:51], a[120:123], a[88:91], v[48:51]\n\t"
    "ds_read_b64_tr_b16 a[156:157], v19 offset:61440\n\t"
    "ds_read_b64_tr_b16 a[158:159], v19 offset:63488\n\t"
    "v_mfma_f32_16x16x32_bf16 v[48:51], a[124:127], a[92:95], v[48:51]\n\t"
    "v_exp_f32 v41, v41\n\t"
    "v_mfma_f32_16x16x32_bf16 v[240:243], a[128:131], v[120:123], v[240:243]\n\t"
    "v_exp_f32 v42, v42\n\t"
    "v_mfma_f32_16x16x32_bf16 v[240:243], a[132:135], v[132:135], v[240:243]\n\t"
    "v_exp_f32 v43, v43\n\t"
    "v_mfma_f32_16x16x32_bf16 v[240:243], a[136:139], v[144:147], v[240:243]\n\t"
    "v_cvt_pk_bf16_f32 v102, v40, v41\n\t"
    "v_cvt_pk_bf16_f32 v103, v42, v43\n\t"
    "v_mfma_f32_16x16x32_bf16 v[240:243], a[140:143], v[156:159], v[240:243]\n\t"
    "s_waitcnt lgkmcnt(3)\n\t"
    "v_exp_f32 v44, v44\n\t"
    "v_mfma_f32_16x16x32_bf16 v[52:55], a[96:99], a[48:51], v[0:3]\n\t"
    "v_exp_f32 v45, v45\n\t"
    "v_mfma_f32_16x16x32_bf16 v[52:55], a[100:103], a[52:55], v[52:55]\n\t"
    "v_exp_f32 v46, v46\n\t"
    "v_mfma_f32_16x16x32_bf16 v[52:55], a[104:107], a[56:59], v[52:55]\n\t"
    "v_exp_f32 v47, v47\n\t"
    "v_mfma_f32_16x16x32_bf16 v[52:55], a[108:111], a[60:63], v[52:55]\n\t"
    "v_cvt_pk_bf16_f32 v106, v44, v45\n\t"
    "v_cvt_pk_bf16_f32 v107, v46, v47\n\t"
    "v_mfma_f32_16x16x32_bf16 v[244:247], a[144:147], v[112:115], v[244:247]\n\t"
    "v_exp_f32 v48, v48\n\t"
    "v_mfma_f32_16x16x32_bf16 v[244:247], a[148:151], v[124:127], v[244:247]\n\t"
    "s_waitcnt lgkmcnt(0)\n\t"
    "v_exp_f32 v49, v49\n\t"
    "v_mfma_f32_16x16x32_bf16 v[244:247], a[152:155], v[136:139], v[244:247]\n\t"
    "s_waitcnt vmcnt(21) lgkmcnt(0)\n\t"
    "v_exp_f32 v50, v50\n\t"
    "v_mfma_f32_16x16x32_bf16 v[244:247], a[156:159], v[148:151], v[244:247]\n\t"
    "s_barrier\n\t"
    "ds_read_b128 a[112:115], v12 offset:36864\n\t"
    "v_cvt_pk_bf16_f32 v110, v48, v49\n\t"
    "v_mfma_f32_16x16x32_bf16 v[56:59], a[96:99], a[64:67], v[4:7]\n\t"
    "s_add_u32 s78, s57, 20480\n\t"
    "ds_read_b128 a[116:119], v13 offset:36864\n\t"
    "v_mfma_f32_16x16x32_bf16 v[56:59], a[100:103], a[68:71], v[56:59]\n\t"
    "s_mov_b32 m0, s74\n\t"
    "v_exp_f32 v51, v51\n\t"
    "v_mfma_f32_16x16x32_bf16 v[56:59], a[104:107], a[72:75], v[56:59]\n\t"
    "ds_read_b128 a[120:123], v14 offset:36864\n\t"
    "v_cvt_pk_bf16_f32 v111, v50, v51\n\t"
    "v_mfma_f32_16x16x32_bf16 v[56:59], a[108:111], a[76:79], v[56:59]\n\t"
    "buffer_load_dwordx4 v20, s[40:43], s78 offen offset:0 nt lds\n\t"
    "v_mfma_f32_16x16x32_bf16 v[248:251], a[144:147], v[116:119], v[248:251]\n\t"
    "ds_read_b128 a[124:127], v15 offset:36864\n\t"
    "ds_read_b64_tr_b16 a[128:129], v16 offset:0\n\t"
    "v_mfma_f32_16x16x32_bf16 v[248:251], a[148:151], v[128:131], v[248:251]\n\t"
    "buffer_store_dwordx4 a[200:203], v33, s[48:51], s64 offen offset:128 nt\n\t"
    "v_mfma_f32_16x16x32_bf16 v[248:251], a[152:155], v[140:143], v[248:251]\n\t"
    "v_exp_f32 v52, v52\n\t"
    "v_mfma_f32_16x16x32_bf16 v[248:251], a[156:159], v[152:155], v[248:251]\n\t"
    "ds_read_b64_tr_b16 a[130:131], v16 offset:2048\n\t"
    "ds_read_b64_tr_b16 a[132:133], v16 offset:4096\n\t"
    "v_mfma_f32_16x16x32_bf16 v[60:63], a[96:99], a[80:83], v[8:11]\n\t"
    "v_exp_f32 v53, v53\n\t"
    "v_mfma_f32_16x16x32_bf16 v[60:63], a[100:103], a[84:87], v[60:63]\n\t"
    "v_exp_f32 v54, v54\n\t"
    "v_mfma_f32_16x16x32_bf16 v[60:63], a[104:107], a[88:91], v[60:63]\n\t"
    "v_exp_f32 v55, v55\n\t"
    "v_mfma_f32_16x16x32_bf16 v[60:63], a[108:111], a[92:95], v[60:63]\n\t"
    "ds_read_b64_tr_b16 a[134:135], v16 offset:6144\n\t"
    "v_cvt_pk_bf16_f32 v112, v52, v53\n\t"
    "v_mfma_f32_16x16x32_bf16 v[40:43], a[228:231], a[40:43], 0\n\t"
    "ds_read_b64_tr_b16 a[136:137], v16 offset:8192\n\t"
    "v_cvt_pk_bf16_f32 v113, v54, v55\n\t"
    "v_mfma_f32_16x16x32_bf16 v[40:43], a[232:235], a[40:43], v[40:43]\n\t"
    "buffer_load_dwordx4 v21, s[40:43], s78 offen offset:1024 nt lds\n\t"
    "v_mfma_f32_16x16x32_bf16 v[44:47], a[236:239], a[40:43], 0\n\t"
    "ds_read_b64_tr_b16 a[138:139], v16 offset:10240\n\t"
    "ds_read_b64_tr_b16 a[140:141], v16 offset:12288\n\t"
    "v_mfma_f32_16x16x32_bf16 v[44:47], a[240:243], a[40:43], v[44:47]\n\t"
    "v_exp_f32 v56, v56\n\t"
    "v_mfma_f32_16x16x32_bf16 v[252:255], a[144:147], v[120:123], v[252:255]\n\t"
    "v_exp_f32 v57, v57\n\t"
    "v_mfma_f32_16x16x32_bf16 v[252:255], a[148:151], v[132:135], v[252:255]\n\t"
    "v_exp_f32 v58, v58\n\t"
    "v_mfma_f32_16x16x32_bf16 v[252:255], a[152:155], v[144:147], v[252:255]\n\t"
    "ds_read_b64_tr_b16 a[142:143], v16 offset:14336\n\t"
    "v_cvt_pk_bf16_f32 v116, v56, v57\n\t"
    "v_mfma_f32_16x16x32_bf16 v[252:255], a[156:159], v[156:159], v[252:255]\n\t"
    "s_waitcnt lgkmcnt(4)\n\t"
    "v_exp_f32 v59, v59\n\t"
    "v_mfma_f32_16x16x32_bf16 v[48:51], a[112:115], a[48:51], v[0:3]\n\t"
    "ds_read_b128 a[96:99], v12 offset:40960\n\t"
    "v_cvt_pk_bf16_f32 v117, v58, v59\n\t"
    "v_mfma_f32_16x16x32_bf16 v[48:51], a[116:119], a[52:55], v[48:51]\n\t"
    "v_exp_f32 v60, v60\n\t"
    "v_mfma_f32_16x16x32_bf16 v[48:51], a[120:123], a[56:59], v[48:51]\n\t"
    "ds_read_b128 a[100:103], v13 offset:40960\n\t"
    "v_cvt_pk_bf16_f32 v40, v40, v41\n\t"
    "v_mfma_f32_16x16x32_bf16 v[48:51], a[124:127], a[60:63], v[48:51]\n\t"
    "buffer_load_dwordx4 v22, s[40:43], s78 offen offset:2048 nt lds\n\t"
    "v_mfma_f32_16x16x32_bf16 v[160:163], a[128:131], v[64:67], v[160:163]\n\t"
    "ds_read_b128 a[104:107], v14 offset:40960\n\t"
    "v_cvt_pk_bf16_f32 v42, v42, v43\n\t"
    "v_mfma_f32_16x16x32_bf16 v[160:163], a[132:135], v[76:79], v[160:163]\n\t"
    "s_waitcnt lgkmcnt(4)\n\t"
    "v_exp_f32 v61, v61\n\t"
    "v_mfma_f32_16x16x32_bf16 v[160:163], a[136:139], v[88:91], v[160:163]\n\t"
    "s_waitcnt lgkmcnt(3)\n\t"
    "ds_read_b128 a[108:111], v15 offset:40960\n\t"
    "v_cvt_pk_bf16_f32 v120, v60, v61\n\t"
    "v_mfma_f32_16x16x32_bf16 v[160:163], a[140:143], v[100:103], v[160:163]\n\t"
    "v_exp_f32 v62, v62\n\t"
    "v_mfma_f32_16x16x32_bf16 v[52:55], a[112:115], a[64:67], v[4:7]\n\t"
    "ds_read_b64_tr_b16 a[144:145], v17 offset:0\n\t"
    "v_accvgpr_write_b32 a40, v40\n\t"
    "v_mfma_f32_16x16x32_bf16 v[52:55], a[116:119], a[68:71], v[52:55]\n\t"
    "v_exp_f32 v63, v63\n\t"
    "v_mfma_f32_16x16x32_bf16 v[52:55], a[120:123], a[72:75], v[52:55]\n\t"
    "ds_read_b64_tr_b16 a[146:147], v17 offset:2048\n\t"
    "v_cvt_pk_bf16_f32 v121, v62, v63\n\t"
    "v_mfma_f32_16x16x32_bf16 v[52:55], a[124:127], a[76:79], v[52:55]\n\t"
    "v_accvgpr_write_b32 a41, v42\n\t"
    "v_cvt_pk_bf16_f32 v44, v44, v45\n\t"
    "v_mfma_f32_16x16x32_bf16 v[164:167], a[128:131], v[68:71], v[164:167]\n\t"
    "ds_read_b64_tr_b16 a[148:149], v17 offset:4096\n\t"
    "v_cvt_pk_bf16_f32 v46, v46, v47\n\t"
    "v_mfma_f32_16x16x32_bf16 v[164:167], a[132:135], v[80:83], v[164:167]\n\t"
    "buffer_load_dwordx4 v23, s[40:43], s78 offen offset:3072 nt lds\n\t"
    "v_mfma_f32_16x16x32_bf16 v[164:167], a[136:139], v[92:95], v[164:167]\n\t"
    "ds_read_b64_tr_b16 a[150:151], v17 offset:6144\n\t"
    "v_accvgpr_write_b32 a42, v44\n\t"
    "v_mfma_f32_16x16x32_bf16 v[164:167], a[140:143], v[104:107], v[164:167]\n\t"
    "v_accvgpr_write_b32 a43, v46\n\t"
    "v_mfma_f32_16x16x32_bf16 v[56:59], a[112:115], a[80:83], v[8:11]\n\t"
    "ds_read_b64_tr_b16 a[152:153], v17 offset:8192\n\t"
    "ds_read_b64_tr_b16 a[154:155], v17 offset:10240\n\t"
    "v_mfma_f32_16x16x32_bf16 v[56:59], a[116:119], a[84:87], v[56:59]\n\t"
    "v_exp_f32 v48, v48\n\t"
    "v_mfma_f32_16x16x32_bf16 v[56:59], a[120:123], a[88:91], v[56:59]\n\t"
    "v_exp_f32 v49, v49\n\t"
    "v_mfma_f32_16x16x32_bf16 v[56:59], a[124:127], a[92:95], v[56:59]\n\t"
    "v_exp_f32 v50, v50\n\t"
    "v_mfma_f32_16x16x32_bf16 v[168:171], a[128:131], v[72:75], v[168:171]\n\t"
    "s_add_u32 s79, s62, 98432\n\t"
    "ds_read_b64_tr_b16 a[156:157], v17 offset:12288\n\t"
    "v_cvt_pk_bf16_f32 v114, v48, v49\n\t"
    "v_mfma_f32_16x16x32_bf16 v[168:171], a[132:135], v[84:87], v[168:171]\n\t"
    "s_add_u32 m0, s77, 49152\n\t"
    "v_exp_f32 v51, v51\n\t"
    "v_mfma_f32_16x16x32_bf16 v[168:171], a[136:139], v[96:99], v[168:171]\n\t"
    "ds_read_b64_tr_b16 a[158:159], v17 offset:14336\n\t"
    "v_cvt_pk_bf16_f32 v115, v50, v51\n\t"
    "v_mfma_f32_16x16x32_bf16 v[168:171], a[140:143], v[108:111], v[168:171]\n\t"
    "s_waitcnt lgkmcnt(4)\n\t"
    "buffer_load_dwordx4 v24, s[44:47], s79 offen offset:0 nt lds\n\t"
    "v_mfma_f32_16x16x32_bf16 v[60:63], a[96:99], a[48:51], v[0:3]\n\t"
    "ds_read_b128 a[112:115], v12 offset:45056\n\t"
    "v_mfma_f32_16x16x32_bf16 v[60:63], a[100:103], a[52:55], v[60:63]\n\t"
    "v_exp_f32 v52, v52\n\t"
    "v_mfma_f32_16x16x32_bf16 v[60:63], a[104:107], a[56:59], v[60:63]\n\t"
    "ds_read_b128 a[116:119], v13 offset:45056\n\t"
    "v_mfma_f32_16x16x32_bf16 v[60:63], a[108:111], a[60:63], v[60:63]\n\t"
    "v_exp_f32 v53, v53\n\t"
    "v_mfma_f32_16x16x32_bf16 v[172:175], a[144:147], v[64:67], v[172:175]\n\t"
    "v_exp_f32 v54, v54\n\t"
    "v_mfma_f32_16x16x32_bf16 v[172:175], a[148:151], v[76:79], v[172:175]\n\t"
    "s_waitcnt lgkmcnt(3)\n\t"
    "ds_read_b128 a[120:123], v14 offset:45056\n\t"
    "v_cvt_pk_bf16_f32 v118, v52, v53\n\t"
    "v_mfma_f32_16x16x32_bf16 v[172:175], a[152:155], v[88:91], v[172:175]\n\t"
    "s_waitcnt lgkmcnt(3)\n\t"
    "buffer_store_dwordx4 a[204:207], v33, s[48:51], s64 offen offset:2176 nt\n\t"
    "v_mfma_f32_16x16x32_bf16 v[172:175], a[156:159], v[100:103], v[172:175]\n\t"
    "v_exp_f32 v55, v55\n\t"
    "v_mfma_f32_16x16x32_bf16 v[40:43], a[96:99], a[64:67], v[4:7]\n\t"
    "ds_read_b128 a[124:127], v15 offset:45056\n\t"
    "v_cvt_pk_bf16_f32 v119, v54, v55\n\t"
    "v_mfma_f32_16x16x32_bf16 v[40:43], a[100:103], a[68:71], v[40:43]\n\t"
    "buffer_load_dwordx4 v25, s[44:47], s79 offen offset:1024 nt lds\n\t"
    "v_mfma_f32_16x16x32_bf16 v[40:43], a[104:107], a[72:75], v[40:43]\n\t"
    "ds_read_b64_tr_b16 a[128:129], v18 offset:0\n\t"
    "ds_read_b64_tr_b16 a[130:131], v18 offset:2048\n\t"
    "v_mfma_f32_16x16x32_bf16 v[40:43], a[108:111], a[76:79], v[40:43]\n\t"
    "v_exp_f32 v56, v56\n\t"
    "v_mfma_f32_16x16x32_bf16 v[176:179], a[144:147], v[68:71], v[176:179]\n\t"
    "v_exp_f32 v57, v57\n\t"
    "v_mfma_f32_16x16x32_bf16 v[176:179], a[148:151], v[80:83], v[176:179]\n\t"
    "v_exp_f32 v58, v58\n\t"
    "v_mfma_f32_16x16x32_bf16 v[176:179], a[152:155], v[92:95], v[176:179]\n\t"
    "v_exp_f32 v59, v59\n\t"
    "v_mfma_f32_16x16x32_bf16 v[176:179], a[156:159], v[104:107], v[176:179]\n\t"
    "v_cvt_pk_bf16_f32 v122, v56, v57\n\t"
    "v_cvt_pk_bf16_f32 v123, v58, v59\n\t"
    "v_mfma_f32_16x16x32_bf16 a[160:163], a[172:175], v[64:67], a[160:163]\n\t"
    "ds_read_b64_tr_b16 a[132:133], v18 offset:4096\n\t"
    "ds_read_b64_tr_b16 a[134:135], v18 offset:6144\n\t"
    "v_mfma_f32_16x16x32_bf16 a[160:163], a[172:175], v[76:79], a[160:163]\n\t"
    "v_exp_f32 v60, v60\n\t"
    "v_mfma_f32_16x16x32_bf16 a[160:163], a[172:175], v[88:91], a[160:163]\n\t"
    "buffer_load_dwordx4 v26, s[44:47], s79 offen offset:2048 nt lds\n\t"
    "v_mfma_f32_16x16x32_bf16 a[160:163], a[172:175], v[100:103], a[160:163]\n\t"
    "v_exp_f32 v61, v61\n\t"
    "v_mfma_f32_16x16x32_bf16 v[44:47], a[96:99], a[80:83], v[8:11]\n\t"
    "v_exp_f32 v62, v62\n\t"
    "v_mfma_f32_16x16x32_bf16 v[44:47], a[100:103], a[84:87], v[44:47]\n\t"
    "ds_read_b64_tr_b16 a[136:137], v18 offset:8192\n\t"
    "v_cvt_pk_bf16_f32 v124, v60, v61\n\t"
    "v_mfma_f32_16x16x32_bf16 v[44:47], a[104:107], a[88:91], v[44:47]\n\t"
    "v_exp_f32 v63, v63\n\t"
    "v_mfma_f32_16x16x32_bf16 v[44:47], a[108:111], a[92:95], v[44:47]\n\t"
    "v_cvt_pk_bf16_f32 v125, v62, v63\n\t"
    "v_mfma_f32_16x16x32_bf16 v[48:51], a[228:231], a[44:47], 0\n\t"
    "ds_read_b64_tr_b16 a[138:139], v18 offset:10240\n\t"
    "ds_read_b64_tr_b16 a[140:141], v18 offset:12288\n\t"
    "v_mfma_f32_16x16x32_bf16 v[48:51], a[232:235], a[44:47], v[48:51]\n\t"
    "v_exp_f32 v40, v40\n\t"
    "v_mfma_f32_16x16x32_bf16 v[52:55], a[236:239], a[44:47], 0\n\t"
    "v_exp_f32 v41, v41\n\t"
    "v_mfma_f32_16x16x32_bf16 v[52:55], a[240:243], a[44:47], v[52:55]\n\t"
    "v_exp_f32 v42, v42\n\t"
    "v_mfma_f32_16x16x32_bf16 v[180:183], a[144:147], v[72:75], v[180:183]\n\t"
    "buffer_load_dwordx4 v27, s[44:47], s79 offen offset:3072 nt lds\n\t"
    "v_mfma_f32_16x16x32_bf16 v[180:183], a[148:151], v[84:87], v[180:183]\n\t"
    "v_exp_f32 v43, v43\n\t"
    "v_mfma_f32_16x16x32_bf16 v[180:183], a[152:155], v[96:99], v[180:183]\n\t"
    "ds_read_b64_tr_b16 a[142:143], v18 offset:14336\n\t"
    "v_cvt_pk_bf16_f32 v128, v40, v41\n\t"
    "v_mfma_f32_16x16x32_bf16 v[180:183], a[156:159], v[108:111], v[180:183]\n\t"
    "s_waitcnt lgkmcnt(3)\n\t"
    "v_cvt_pk_bf16_f32 v129, v42, v43\n\t"
    "v_cvt_pk_bf16_f32 v48, v48, v49\n\t"
    "v_mfma_f32_16x16x32_bf16 v[56:59], a[112:115], a[48:51], v[0:3]\n\t"
    "ds_read_b128 a[96:99], v12 offset:49152\n\t"
    "v_cvt_pk_bf16_f32 v50, v50, v51\n\t"
    "v_mfma_f32_16x16x32_bf16 v[56:59], a[116:119], a[52:55], v[56:59]\n\t"
    "ds_read_b128 a[100:103], v13 offset:49152\n\t"
    "v_accvgpr_write_b32 a44, v48\n\t"
    "v_mfma_f32_16x16x32_bf16 v[56:59], a[120:123], a[56:59], v[56:59]\n\t"
    "v_exp_f32 v44, v44\n\t"
    "v_mfma_f32_16x16x32_bf16 v[56:59], a[124:127], a[60:63], v[56:59]\n\t"
    "ds_read_b128 a[104:107], v14 offset:49152\n\t"
    "v_accvgpr_write_b32 a45, v50\n\t"
    "v_mfma_f32_16x16x32_bf16 v[184:187], a[128:131], v[64:67], v[184:187]\n\t"
    "v_exp_f32 v45, v45\n\t"
    "v_mfma_f32_16x16x32_bf16 v[184:187], a[132:135], v[76:79], v[184:187]\n\t"
    "s_waitcnt lgkmcnt(4)\n\t"
    "ds_read_b128 a[108:111], v15 offset:49152\n\t"
    "v_cvt_pk_bf16_f32 v132, v44, v45\n\t"
    "v_mfma_f32_16x16x32_bf16 v[184:187], a[136:139], v[88:91], v[184:187]\n\t"
    "s_waitcnt lgkmcnt(4)\n\t"
    "v_exp_f32 v46, v46\n\t"
    "v_mfma_f32_16x16x32_bf16 v[184:187], a[140:143], v[100:103], v[184:187]\n\t"
    "ds_read_b64_tr_b16 a[144:145], v19 offset:0\n\t"
    "v_cvt_pk_bf16_f32 v52, v52, v53\n\t"
    "v_mfma_f32_16x16x32_bf16 v[60:63], a[112:115], a[64:67], v[4:7]\n\t"
    "v_exp_f32 v47, v47\n\t"
    "v_mfma_f32_16x16x32_bf16 v[60:63], a[116:119], a[68:71], v[60:63]\n\t"
    "ds_read_b64_tr_b16 a[146:147], v19 offset:2048\n\t"
    "v_cvt_pk_bf16_f32 v133, v46, v47\n\t"
    "v_mfma_f32_16x16x32_bf16 v[60:63], a[120:123], a[72:75], v[60:63]\n\t"
    "v_cvt_pk_bf16_f32 v54, v54, v55\n\t"
    "v_accvgpr_write_b32 a46, v52\n\t"
    "v_mfma_f32_16x16x32_bf16 v[60:63], a[124:127], a[76:79], v[60:63]\n\t"
    "ds_read_b64_tr_b16 a[148:149], v19 offset:4096\n\t"
    "v_accvgpr_write_b32 a47, v54\n\t"
    "v_mfma_f32_16x16x32_bf16 v[188:191], a[128:131], v[68:71], v[188:191]\n\t"
    "v_exp_f32 v56, v56\n\t"
    "v_mfma_f32_16x16x32_bf16 v[188:191], a[132:135], v[80:83], v[188:191]\n\t"
    "ds_read_b64_tr_b16 a[150:151], v19 offset:6144\n\t"
    "ds_read_b64_tr_b16 a[152:153], v19 offset:8192\n\t"
    "v_mfma_f32_16x16x32_bf16 v[188:191], a[136:139], v[92:95], v[188:191]\n\t"
    "v_exp_f32 v57, v57\n\t"
    "v_mfma_f32_16x16x32_bf16 v[188:191], a[140:143], v[104:107], v[188:191]\n\t"
    "v_exp_f32 v58, v58\n\t"
    "v_mfma_f32_16x16x32_bf16 v[40:43], a[112:115], a[80:83], v[8:11]\n\t"
    "v_exp_f32 v59, v59\n\t"
    "v_mfma_f32_16x16x32_bf16 v[40:43], a[116:119], a[84:87], v[40:43]\n\t"
    "ds_read_b64_tr_b16 a[154:155], v19 offset:10240\n\t"
    "v_cvt_pk_bf16_f32 v126, v56, v57\n\t"
    "v_mfma_f32_16x16x32_bf16 v[40:43], a[120:123], a[88:91], v[40:43]\n\t"
    "v_cvt_pk_bf16_f32 v127, v58, v59\n\t"
    "v_mfma_f32_16x16x32_bf16 v[40:43], a[124:127], a[92:95], v[40:43]\n\t"
    "ds_read_b64_tr_b16 a[156:157], v19 offset:12288\n\t"
    "ds_read_b64_tr_b16 a[158:159], v19 offset:14336\n\t"
    "v_mfma_f32_16x16x32_bf16 v[192:195], a[128:131], v[72:75], v[192:195]\n\t"
    "buffer_store_dwordx4 a[208:211], v33, s[48:51], s65 offen offset:128 nt\n\t"
    "v_mfma_f32_16x16x32_bf16 v[192:195], a[132:135], v[84:87], v[192:195]\n\t"
    "v_exp_f32 v60, v60\n\t"
    "v_mfma_f32_16x16x32_bf16 v[192:195], a[136:139], v[96:99], v[192:195]\n\t"
    "v_exp_f32 v61, v61\n\t"
    "v_mfma_f32_16x16x32_bf16 v[192:195], a[140:143], v[108:111], v[192:195]\n\t"
    "s_waitcnt lgkmcnt(3)\n\t"
    "v_exp_f32 v62, v62\n\t"
    "v_mfma_f32_16x16x32_bf16 v[44:47], a[96:99], a[48:51], v[0:3]\n\t"
    "v_exp_f32 v63, v63\n\t"
    "v_mfma_f32_16x16x32_bf16 v[44:47], a[100:103], a[52:55], v[44:47]\n\t"
    "v_cvt_pk_bf16_f32 v130, v60, v61\n\t"
    "v_cvt_pk_bf16_f32 v131, v62, v63\n\t"
    "v_mfma_f32_16x16x32_bf16 v[44:47], a[104:107], a[56:59], v[44:47]\n\t"
    "v_exp_f32 v40, v40\n\t"
    "v_mfma_f32_16x16x32_bf16 v[44:47], a[108:111], a[60:63], v[44:47]\n\t"
    "v_exp_f32 v41, v41\n\t"
    "v_mfma_f32_16x16x32_bf16 v[196:199], a[144:147], v[64:67], v[196:199]\n\t"
    "v_exp_f32 v42, v42\n\t"
    "v_mfma_f32_16x16x32_bf16 v[196:199], a[148:151], v[76:79], v[196:199]\n\t"
    "s_waitcnt lgkmcnt(0)\n\t"
    "v_exp_f32 v43, v43\n\t"
    "v_mfma_f32_16x16x32_bf16 v[196:199], a[152:155], v[88:91], v[196:199]\n\t"
    "s_waitcnt vmcnt(22) lgkmcnt(0)\n\t"
    "v_cvt_pk_bf16_f32 v134, v40, v41\n\t"
    "v_cvt_pk_bf16_f32 v135, v42, v43\n\t"
    "v_mfma_f32_16x16x32_bf16 v[196:199], a[156:159], v[100:103], v[196:199]\n\t"
    "s_barrier\n\t"
    "ds_read_b128 a[112:115], v12 offset:53248\n\t"
    "v_mfma_f32_16x16x32_bf16 v[48:51], a[96:99], a[64:67], v[4:7]\n\t"
    "s_add_u32 s78, s57, 36864\n\t"
    "ds_read_b128 a[116:119], v13 offset:53248\n\t"
    "v_mfma_f32_16x16x32_bf16 v[48:51], a[100:103], a[68:71], v[48:51]\n\t"
    "s_mov_b32 m0, s75\n\t"
    "ds_read_b128 a[120:123], v14 offset:53248\n\t"
    "v_mfma_f32_16x16x32_bf16 v[48:51], a[104:107], a[72:75], v[48:51]\n\t"
    "buffer_load_dwordx4 v20, s[40:43], s78 offen offset:0 nt lds\n\t"
    "v_mfma_f32_16x16x32_bf16 v[48:51], a[108:111], a[76:79], v[48:51]\n\t"
    "ds_read_b128 a[124:127], v15 offset:53248\n\t"
    "ds_read_b64_tr_b16 a[128:129], v16 offset:16384\n\t"
    "v_mfma_f32_16x16x32_bf16 v[200:203], a[144:147], v[68:71], v[200:203]\n\t"
    "v_exp_f32 v44, v44\n\t"
    "v_mfma_f32_16x16x32_bf16 v[200:203], a[148:151], v[80:83], v[200:203]\n\t"
    "ds_read_b64_tr_b16 a[130:131], v16 offset:18432\n\t"
    "ds_read_b64_tr_b16 a[132:133], v16 offset:20480\n\t"
    "v_mfma_f32_16x16x32_bf16 v[200:203], a[152:155], v[92:95], v[200:203]\n\t"
    "v_exp_f32 v45, v45\n\t"
    "v_mfma_f32_16x16x32_bf16 v[200:203], a[156:159], v[104:107], v[200:203]\n\t"
    "v_exp_f32 v46, v46\n\t"
    "v_mfma_f32_16x16x32_bf16 v[52:55], a[96:99], a[80:83], v[8:11]\n\t"
    "ds_read_b64_tr_b16 a[134:135], v16 offset:22528\n\t"
    "v_cvt_pk_bf16_f32 v136, v44, v45\n\t"
    "v_mfma_f32_16x16x32_bf16 v[52:55], a[100:103], a[84:87], v[52:55]\n\t"
    "ds_read_b64_tr_b16 a[136:137], v16 offset:24576\n\t"
    "ds_read_b64_tr_b16 a[138:139], v16 offset:26624\n\t"
    "v_mfma_f32_16x16x32_bf16 v[52:55], a[104:107], a[88:91], v[52:55]\n\t"
    "v_exp_f32 v47, v47\n\t"
    "v_mfma_f32_16x16x32_bf16 v[52:55], a[108:111], a[92:95], v[52:55]\n\t"
    "v_cvt_pk_bf16_f32 v137, v46, v47\n\t"
    "v_mfma_f32_16x16x32_bf16 v[204:207], a[144:147], v[72:75], v[204:207]\n\t"
    "ds_read_b64_tr_b16 a[140:141], v16 offset:28672\n\t"
    "ds_read_b64_tr_b16 a[142:143], v16 offset:30720\n\t"
    "v_mfma_f32_16x16x32_bf16 v[204:207], a[148:151], v[84:87], v[204:207]\n\t"
    "buffer_load_dwordx4 v21, s[40:43], s78 offen offset:1024 nt lds\n\t"
    "v_mfma_f32_16x16x32_bf16 v[204:207], a[152:155], v[96:99], v[204:207]\n\t"
    "v_exp_f32 v48, v48\n\t"
    "v_mfma_f32_16x16x32_bf16 v[204:207], a[156:159], v[108:111], v[204:207]\n\t"
    "s_waitcnt lgkmcnt(5)\n\t"
    "v_exp_f32 v49, v49\n\t"
    "v_mfma_f32_16x16x32_bf16 v[56:59], a[112:115], a[48:51], v[0:3]\n\t"
    "ds_read_b128 a[96:99], v12 offset:57344\n\t"
    "v_cvt_pk_bf16_f32 v140, v48, v49\n\t"
    "v_mfma_f32_16x16x32_bf16 v[56:59], a[116:119], a[52:55], v[56:59]\n\t"
    "v_exp_f32 v50, v50\n\t"
    "v_mfma_f32_16x16x32_bf16 v[56:59], a[120:123], a[56:59], v[56:59]\n\t"
    "ds_read_b128 a[100:103], v13 offset:57344\n\t"
    "v_mfma_f32_16x16x32_bf16 v[56:59], a[124:127], a[60:63], v[56:59]\n\t"
    "v_exp_f32 v51, v51\n\t"
    "v_mfma_f32_16x16x32_bf16 v[208:211], a[128:131], v[64:67], v[208:211]\n\t"
    "s_waitcnt lgkmcnt(2)\n\t"
    "ds_read_b128 a[104:107], v14 offset:57344\n\t"
    "v_cvt_pk_bf16_f32 v141, v50, v51\n\t"
    "v_mfma_f32_16x16x32_bf16 v[208:211], a[132:135], v[76:79], v[208:211]\n\t"
    "v_exp_f32 v52, v52\n\t"
    "v_mfma_f32_16x16x32_bf16 v[208:211], a[136:139], v[88:91], v[208:211]\n\t"
    "buffer_load_dwordx4 v22, s[40:43], s78 offen offset:2048 nt lds\n\t"
    "v_mfma_f32_16x16x32_bf16 v[208:211], a[140:143], v[100:103], v[208:211]\n\t"
    "ds_read_b128 a[108:111], v15 offset:57344\n\t"
    "ds_read_b64_tr_b16 a[144:145], v17 offset:16384\n\t"
    "v_mfma_f32_16x16x32_bf16 v[60:63], a[112:115], a[64:67], v[4:7]\n\t"
    "v_exp_f32 v53, v53\n\t"
    "v_mfma_f32_16x16x32_bf16 v[60:63], a[116:119], a[68:71], v[60:63]\n\t"
    "v_exp_f32 v54, v54\n\t"
    "v_mfma_f32_16x16x32_bf16 v[60:63], a[120:123], a[72:75], v[60:63]\n\t"
    "v_exp_f32 v55, v55\n\t"
    "v_mfma_f32_16x16x32_bf16 v[60:63], a[124:127], a[76:79], v[60:63]\n\t"
    "ds_read_b64_tr_b16 a[146:147], v17 offset:18432\n\t"
    "v_cvt_pk_bf16_f32 v144, v52, v53\n\t"
    "v_mfma_f32_16x16x32_bf16 v[212:215], a[128:131], v[68:71], v[212:215]\n\t"
    "v_cvt_pk_bf16_f32 v145, v54, v55\n\t"
    "v_mfma_f32_16x16x32_bf16 v[212:215], a[132:135], v[80:83], v[212:215]\n\t"
    "v_exp_f32 v56, v56\n\t"
    "v_mfma_f32_16x16x32_bf16 v[212:215], a[136:139], v[92:95], v[212:215]\n\t"
    "ds_read_b64_tr_b16 a[148:149], v17 offset:20480\n\t"
    "ds_read_b64_tr_b16 a[150:151], v17 offset:22528\n\t"
    "v_mfma_f32_16x16x32_bf16 v[212:215], a[140:143], v[104:107], v[212:215]\n\t"
    "v_exp_f32 v57, v57\n\t"
    "v_mfma_f32_16x16x32_bf16 a[164:167], a[172:175], v[68:71], a[164:167]\n\t"
    "buffer_load_dwordx4 v23, s[40:43], s78 offen offset:3072 nt lds\n\t"
    "v_mfma_f32_16x16x32_bf16 a[164:167], a[172:175], v[80:83], a[164:167]\n\t"
    "buffer_store_dwordx4 a[212:215], v33, s[48:51], s65 offen offset:2176 nt\n\t"
    "v_mfma_f32_16x16x32_bf16 a[164:167], a[172:175], v[92:95], a[164:167]\n\t"
    "ds_read_b64_tr_b16 a[152:153], v17 offset:24576\n\t"
    "v_cvt_pk_bf16_f32 v138, v56, v57\n\t"
    "v_mfma_f32_16x16x32_bf16 a[164:167], a[172:175], v[104:107], a[164:167]\n\t"
    "v_exp_f32 v58, v58\n\t"
    "v_mfma_f32_16x16x32_bf16 v[40:43], a[112:115], a[80:83], v[8:11]\n\t"
    "v_exp_f32 v59, v59\n\t"
    "v_mfma_f32_16x16x32_bf16 v[40:43], a[116:119], a[84:87], v[40:43]\n\t"
    "ds_read_b64_tr_b16 a[154:155], v17 offset:26624\n\t"
    "v_cvt_pk_bf16_f32 v139, v58, v59\n\t"
    "v_mfma_f32_16x16x32_bf16 v[40:43], a[120:123], a[88:91], v[40:43]\n\t"
    "v_exp_f32 v60, v60\n\t"
    "v_mfma_f32_16x16x32_bf16 v[40:43], a[124:127], a[92:95], v[40:43]\n\t"
    "ds_read_b64_tr_b16 a[156:157], v17 offset:28672\n\t"
    "ds_read_b64_tr_b16 a[158:159], v17 offset:30720\n\t"
    "v_mfma_f32_16x16x32_bf16 v[216:219], a[128:131], v[72:75], v[216:219]\n\t"
    "s_add_u32 s79, s63, 0\n\t"
    "v_exp_f32 v61, v61\n\t"
    "v_mfma_f32_16x16x32_bf16 v[216:219], a[132:135], v[84:87], v[216:219]\n\t"
    "s_add_u32 m0, s77, 0\n\t"
    "v_exp_f32 v62, v62\n\t"
    "v_mfma_f32_16x16x32_bf16 v[216:219], a[136:139], v[96:99], v[216:219]\n\t"
    "buffer_load_dwordx4 v24, s[44:47], s79 offen offset:0 nt lds\n\t"
    "v_mfma_f32_16x16x32_bf16 v[216:219], a[140:143], v[108:111], v[216:219]\n\t"
    "s_waitcnt lgkmcnt(3)\n\t"
    "v_exp_f32 v63, v63\n\t"
    "v_mfma_f32_16x16x32_bf16 v[44:47], a[96:99], a[48:51], v[0:3]\n\t"
    "ds_read_b128 a[112:115], v12 offset:61440\n\t"
    "v_cvt_pk_bf16_f32 v142, v60, v61\n\t"
    "v_mfma_f32_16x16x32_bf16 v[44:47], a[100:103], a[52:55], v[44:47]\n\t"
    "v_cvt_pk_bf16_f32 v143, v62, v63\n\t"
    "v_mfma_f32_16x16x32_bf16 v[44:47], a[104:107], a[56:59], v[44:47]\n\t"
    "ds_read_b128 a[116:119], v13 offset:61440\n\t"
    "v_mfma_f32_16x16x32_bf16 v[44:47], a[108:111], a[60:63], v[44:47]\n\t"
    "v_exp_f32 v40, v40\n\t"
    "v_mfma_f32_16x16x32_bf16 v[220:223], a[144:147], v[64:67], v[220:223]\n\t"
    "ds_read_b128 a[120:123], v14 offset:61440\n\t"
    "v_mfma_f32_16x16x32_bf16 v[220:223], a[148:151], v[76:79], v[220:223]\n\t"
    "s_waitcnt lgkmcnt(3)\n\t"
    "v_exp_f32 v41, v41\n\t"
    "v_mfma_f32_16x16x32_bf16 v[220:223], a[152:155], v[88:91], v[220:223]\n\t"
    "ds_read_b128 a[124:127], v15 offset:61440\n\t"
    "v_cvt_pk_bf16_f32 v146, v40, v41\n\t"
    "v_mfma_f32_16x16x32_bf16 v[220:223], a[156:159], v[100:103], v[220:223]\n\t"
    "v_exp_f32 v42, v42\n\t"
    "v_mfma_f32_16x16x32_bf16 v[48:51], a[96:99], a[64:67], v[4:7]\n\t"
    "ds_read_b64_tr_b16 a[128:129], v18 offset:16384\n\t"
    "ds_read_b64_tr_b16 a[130:131], v18 offset:18432\n\t"
    "v_mfma_f32_16x16x32_bf16 v[48:51], a[100:103], a[68:71], v[48:51]\n\t"
    "buffer_load_dwordx4 v25, s[44:47], s79 offen offset:1024 nt lds\n\t"
    "v_mfma_f32_16x16x32_bf16 v[48:51], a[104:107], a[72:75], v[48:51]\n\t"
    "v_exp_f32 v43, v43\n\t"
    "v_mfma_f32_16x16x32_bf16 v[48:51], a[108:111], a[76:79], v[48:51]\n\t"
    "v_cvt_pk_bf16_f32 v147, v42, v43\n\t"
    "v_mfma_f32_16x16x32_bf16 v[224:227], a[144:147], v[68:71], v[224:227]\n\t"
    "ds_read_b64_tr_b16 a[132:133], v18 offset:20480\n\t"
    "ds_read_b64_tr_b16 a[134:135], v18 offset:22528\n\t"
    "v_mfma_f32_16x16x32_bf16 v[224:227], a[148:151], v[80:83], v[224:227]\n\t"
    "v_exp_f32 v44, v44\n\t"
    "v_mfma_f32_16x16x32_bf16 v[224:227], a[152:155], v[92:95], v[224:227]\n\t"
    "v_exp_f32 v45, v45\n\t"
    "v_mfma_f32_16x16x32_bf16 v[224:227], a[156:159], v[104:107], v[224:227]\n\t"
    "v_exp_f32 v46, v46\n\t"
    "v_mfma_f32_16x16x32_bf16 v[52:55], a[96:99], a[80:83], v[8:11]\n\t"
    "ds_read_b64_tr_b16 a[136:137], v18 offset:24576\n\t"
    "v_cvt_pk_bf16_f32 v148, v44, v45\n\t"
    "v_mfma_f32_16x16x32_bf16 v[52:55], a[100:103], a[84:87], v[52:55]\n\t"
    "v_exp_f32 v47, v47\n\t"
    "v_mfma_f32_16x16x32_bf16 v[52:55], a[104:107], a[88:91], v[52:55]\n\t"
    "ds_read_b64_tr_b16 a[138:139], v18 offset:26624\n\t"
    "v_cvt_pk_bf16_f32 v149, v46, v47\n\t"
    "v_mfma_f32_16x16x32_bf16 v[52:55], a[108:111], a[92:95], v[52:55]\n\t"
    "buffer_load_dwordx4 v26, s[44:47], s79 offen offset:2048 nt lds\n\t"
    "v_mfma_f32_16x16x32_bf16 v[228:231], a[144:147], v[72:75], v[228:231]\n\t"
    "ds_read_b64_tr_b16 a[140:141], v18 offset:28672\n\t"
    "ds_read_b64_tr_b16 a[142:143], v18 offset:30720\n\t"
    "v_mfma_f32_16x16x32_bf16 v[228:231], a[148:151], v[84:87], v[228:231]\n\t"
    "v_exp_f32 v48, v48\n\t"
    "v_mfma_f32_16x16x32_bf16 v[228:231], a[152:155], v[96:99], v[228:231]\n\t"
    "v_exp_f32 v49, v49\n\t"
    "v_mfma_f32_16x16x32_bf16 v[228:231], a[156:159], v[108:111], v[228:231]\n\t"
    "s_waitcnt lgkmcnt(4)\n\t"
    "v_exp_f32 v50, v50\n\t"
    "v_mfma_f32_16x16x32_bf16 v[56:59], a[112:115], a[48:51], v[0:3]\n\t"
    "ds_read_b128 a[96:99], v12 offset:0\n\t"
    "v_cvt_pk_bf16_f32 v152, v48, v49\n\t"
    "v_mfma_f32_16x16x32_bf16 v[56:59], a[116:119], a[52:55], v[56:59]\n\t"
    "v_exp_f32 v51, v51\n\t"
    "v_mfma_f32_16x16x32_bf16 v[56:59], a[120:123], a[56:59], v[56:59]\n\t"
    "ds_read_b128 a[100:103], v13 offset:0\n\t"
    "v_cvt_pk_bf16_f32 v153, v50, v51\n\t"
    "v_mfma_f32_16x16x32_bf16 v[56:59], a[124:127], a[60:63], v[56:59]\n\t"
    "v_exp_f32 v52, v52\n\t"
    "v_mfma_f32_16x16x32_bf16 v[232:235], a[128:131], v[64:67], v[232:235]\n\t"
    "ds_read_b128 a[104:107], v14 offset:0\n\t"
    "v_mfma_f32_16x16x32_bf16 v[232:235], a[132:135], v[76:79], v[232:235]\n\t"
    "s_waitcnt lgkmcnt(3)\n\t"
    "buffer_load_dwordx4 v27, s[44:47], s79 offen offset:3072 nt lds\n\t"
    "v_mfma_f32_16x16x32_bf16 v[232:235], a[136:139], v[88:91], v[232:235]\n\t"
    "ds_read_b128 a[108:111], v15 offset:0\n\t"
    "ds_read_b64_tr_b16 a[144:145], v19 offset:16384\n\t"
    "v_mfma_f32_16x16x32_bf16 v[232:235], a[140:143], v[100:103], v[232:235]\n\t"
    "v_exp_f32 v53, v53\n\t"
    "v_mfma_f32_16x16x32_bf16 v[60:63], a[112:115], a[64:67], v[4:7]\n\t"
    "v_exp_f32 v54, v54\n\t"
    "v_mfma_f32_16x16x32_bf16 v[60:63], a[116:119], a[68:71], v[60:63]\n\t"
    "buffer_store_dwordx4 a[216:219], v33, s[48:51], s66 offen offset:128 nt\n\t"
    "v_mfma_f32_16x16x32_bf16 v[60:63], a[120:123], a[72:75], v[60:63]\n\t"
    "v_exp_f32 v55, v55\n\t"
    "v_mfma_f32_16x16x32_bf16 v[60:63], a[124:127], a[76:79], v[60:63]\n\t"
    "ds_read_b64_tr_b16 a[146:147], v19 offset:18432\n\t"
    "v_cvt_pk_bf16_f32 v156, v52, v53\n\t"
    "v_mfma_f32_16x16x32_bf16 v[236:239], a[128:131], v[68:71], v[236:239]\n\t"
    "v_cvt_pk_bf16_f32 v157, v54, v55\n\t"
    "v_mfma_f32_16x16x32_bf16 v[236:239], a[132:135], v[80:83], v[236:239]\n\t"
    "ds_read_b64_tr_b16 a[148:149], v19 offset:20480\n\t"
    "ds_read_b64_tr_b16 a[150:151], v19 offset:22528\n\t"
    "v_mfma_f32_16x16x32_bf16 v[236:239], a[136:139], v[92:95], v[236:239]\n\t"
    "v_exp_f32 v56, v56\n\t"
    "v_mfma_f32_16x16x32_bf16 v[236:239], a[140:143], v[104:107], v[236:239]\n\t"
    "v_exp_f32 v57, v57\n\t"
    "v_mfma_f32_16x16x32_bf16 a[168:171], a[172:175], v[72:75], a[168:171]\n\t"
    "v_exp_f32 v58, v58\n\t"
    "v_mfma_f32_16x16x32_bf16 a[168:171], a[172:175], v[84:87], a[168:171]\n\t"
    "v_exp_f32 v59, v59\n\t"
    "v_mfma_f32_16x16x32_bf16 a[168:171], a[172:175], v[96:99], a[168:171]\n\t"
    "ds_read_b64_tr_b16 a[152:153], v19 offset:24576\n\t"
    "v_cvt_pk_bf16_f32 v150, v56, v57\n\t"
    "v_mfma_f32_16x16x32_bf16 a[168:171], a[172:175], v[108:111], a[168:171]\n\t"
    "v_cvt_pk_bf16_f32 v151, v58, v59\n\t"
    "v_mfma_f32_16x16x32_bf16 v[40:43], a[112:115], a[80:83], v[8:11]\n\t"
    "ds_read_b64_tr_b16 a[154:155], v19 offset:26624\n\t"
    "ds_read_b64_tr_b16 a[156:157], v19 offset:28672\n\t"
    "v_mfma_f32_16x16x32_bf16 v[40:43], a[116:119], a[84:87], v[40:43]\n\t"
    "v_exp_f32 v60, v60\n\t"
    "v_mfma_f32_16x16x32_bf16 v[40:43], a[120:123], a[88:91], v[40:43]\n\t"
    "v_exp_f32 v61, v61\n\t"
    "v_mfma_f32_16x16x32_bf16 v[40:43], a[124:127], a[92:95], v[40:43]\n\t"
    "v_exp_f32 v62, v62\n\t"
    "v_mfma_f32_16x16x32_bf16 v[240:243], a[128:131], v[72:75], v[240:243]\n\t"
    "v_exp_f32 v63, v63\n\t"
    "v_mfma_f32_16x16x32_bf16 v[240:243], a[132:135], v[84:87], v[240:243]\n\t"
    "ds_read_b64_tr_b16 a[158:159], v19 offset:30720\n\t"
    "v_cvt_pk_bf16_f32 v154, v60, v61\n\t"
    "v_mfma_f32_16x16x32_bf16 v[240:243], a[136:139], v[96:99], v[240:243]\n\t"
    "v_cvt_pk_bf16_f32 v155, v62, v63\n\t"
    "v_mfma_f32_16x16x32_bf16 v[240:243], a[140:143], v[108:111], v[240:243]\n\t"
    "s_waitcnt lgkmcnt(3)\n\t"
    "v_exp_f32 v40, v40\n\t"
    "v_mfma_f32_16x16x32_bf16 v[44:47], a[96:99], a[0:3], 0\n\t"
    "v_exp_f32 v41, v41\n\t"
    "v_mfma_f32_16x16x32_bf16 v[44:47], a[100:103], a[4:7], v[44:47]\n\t"
    "v_exp_f32 v42, v42\n\t"
    "v_mfma_f32_16x16x32_bf16 v[44:47], a[104:107], a[8:11], v[44:47]\n\t"
    "v_exp_f32 v43, v43\n\t"
    "v_mfma_f32_16x16x32_bf16 v[44:47], a[108:111], a[12:15], v[44:47]\n\t"
    "v_cvt_pk_bf16_f32 v158, v40, v41\n\t"
    "v_cvt_pk_bf16_f32 v159, v42, v43\n\t"
    "v_mfma_f32_16x16x32_bf16 v[244:247], a[144:147], v[64:67], v[244:247]\n\t"
    "v_mfma_f32_16x16x32_bf16 v[244:247], a[148:151], v[76:79], v[244:247]\n\t"
    "s_waitcnt lgkmcnt(0)\n\t"
    "v_mfma_f32_16x16x32_bf16 v[244:247], a[152:155], v[88:91], v[244:247]\n\t"
    "s_waitcnt vmcnt(22) lgkmcnt(0)\n\t"
    "v_mfma_f32_16x16x32_bf16 v[244:247], a[156:159], v[100:103], v[244:247]\n\t"
    "s_barrier\n\t"
    "ds_read_b128 a[112:115], v12 offset:4096\n\t"
    "v_max3_f32 v0, v44, v45, v46\n\t"
    "v_mfma_f32_16x16x32_bf16 v[48:51], a[96:99], a[16:19], 0\n\t"
    "s_add_u32 s78, s57, 53248\n\t"
    "ds_read_b128 a[116:119], v13 offset:4096\n\t"
    "v_max_f32 v0, v0, v47\n\t"
    "v_mfma_f32_16x16x32_bf16 v[48:51], a[100:103], a[20:23], v[48:51]\n\t"
    "s_mov_b32 m0, s76\n\t"
    "ds_read_b128 a[120:123], v14 offset:4096\n\t"
    "v_mov_b32 v1, v0\n\t"
    "v_mfma_f32_16x16x32_bf16 v[48:51], a[104:107], a[24:27], v[48:51]\n\t"
    "buffer_load_dwordx4 v20, s[40:43], s78 offen offset:0 nt lds\n\t"
    "v_mfma_f32_16x16x32_bf16 v[48:51], a[108:111], a[28:31], v[48:51]\n\t"
    "ds_read_b128 a[124:127], v15 offset:4096\n\t"
    "ds_read_b64_tr_b16 a[128:129], v16 offset:32768\n\t"
    "v_mfma_f32_16x16x32_bf16 v[248:251], a[144:147], v[68:71], v[248:251]\n\t"
    "v_permlane32_swap_b32 v0, v1\n\t"
    "v_mfma_f32_16x16x32_bf16 v[248:251], a[148:151], v[80:83], v[248:251]\n\t"
    "ds_read_b64_tr_b16 a[130:131], v16 offset:34816\n\t"
    "v_max_f32 v0, v0, v1\n\t"
    "v_mfma_f32_16x16x32_bf16 v[248:251], a[152:155], v[92:95], v[248:251]\n\t"
    "v_mov_b32 v1, v0\n\t"
    "v_mfma_f32_16x16x32_bf16 v[248:251], a[156:159], v[104:107], v[248:251]\n\t"
    "ds_read_b64_tr_b16 a[132:133], v16 offset:36864\n\t"
    "v_max3_f32 v4, v48, v49, v50\n\t"
    "v_mfma_f32_16x16x32_bf16 v[52:55], a[96:99], a[32:35], 0\n\t"
    "ds_read_b64_tr_b16 a[134:135], v16 offset:38912\n\t"
    "v_max_f32 v4, v4, v51\n\t"
    "v_mfma_f32_16x16x32_bf16 v[52:55], a[100:103], a[36:39], v[52:55]\n\t"
    "v_permlane16_swap_b32 v0, v1\n\t"
    "v_mfma_f32_16x16x32_bf16 v[52:55], a[104:107], a[40:43], v[52:55]\n\t"
    "ds_read_b64_tr_b16 a[136:137], v16 offset:40960\n\t"
    "v_min_f32 v2, -v0, -v1\n\t"
    "v_mfma_f32_16x16x32_bf16 v[52:55], a[108:111], a[44:47], v[52:55]\n\t"
    "v_mov_b32 v0, v2\n\t"
    "v_mov_b32 v1, v2\n\t"
    "v_mfma_f32_16x16x32_bf16 v[252:255], a[144:147], v[72:75], v[252:255]\n\t"
    "ds_read_b64_tr_b16 a[138:139], v16 offset:43008\n\t"
    "v_mov_b32 v3, v2\n\t"
    "v_mfma_f32_16x16x32_bf16 v[252:255], a[148:151], v[84:87], v[252:255]\n\t"
    "ds_read_b64_tr_b16 a[140:141], v16 offset:45056\n\t"
    "v_mov_b32 v5, v4\n\t"
    "v_mfma_f32_16x16x32_bf16 v[252:255], a[152:155], v[96:99], v[252:255]\n\t"
    "ds_read_b64_tr_b16 a[142:143], v16 offset:47104\n\t"
    "v_add_f32 v44, v44, v0\n\t"
    "v_mfma_f32_16x16x32_bf16 v[252:255], a[156:159], v[108:111], v[252:255]\n\t"
    "s_waitcnt lgkmcnt(5)\n\t"
    "v_permlane32_swap_b32 v4, v5\n\t"
    "v_mfma_f32_16x16x32_bf16 v[56:59], a[112:115], a[0:3], v[0:3]\n\t"
    "v_max_f32 v4, v4, v5\n\t"
    "v_mov_b32 v5, v4\n\t"
    "v_mfma_f32_16x16x32_bf16 v[56:59], a[116:119], a[4:7], v[56:59]\n\t"
    "ds_read_b128 a[96:99], v12 offset:8192\n\t"
    "v_max3_f32 v8, v52, v53, v54\n\t"
    "v_mfma_f32_16x16x32_bf16 v[56:59], a[120:123], a[8:11], v[56:59]\n\t"
    "v_permlane16_swap_b32 v4, v5\n\t"
    "v_mfma_f32_16x16x32_bf16 v[56:59], a[124:127], a[12:15], v[56:59]\n\t"
    "buffer_load_dwordx4 v21, s[40:43], s78 offen offset:1024 nt lds\n\t"
    "v_mfma_f32_16x16x32_bf16 v[160:163], a[128:131], v[112:115], v[160:163]\n\t"
    "s_waitcnt lgkmcnt(3)\n\t"
    "ds_read_b128 a[100:103], v13 offset:8192\n\t"
    "v_min_f32 v6, -v4, -v5\n\t"
    "v_mov_b32 v4, v6\n\t"
    "v_mov_b32 v5, v6\n\t"
    "v_mov_b32 v7, v6\n\t"
    "v_mfma_f32_16x16x32_bf16 v[160:163], a[132:135], v[124:127], v[160:163]\n\t"
    "v_max_f32 v8, v8, v55\n\t"
    "v_mov_b32 v9, v8\n\t"
    "v_mfma_f32_16x16x32_bf16 v[160:163], a[136:139], v[136:139], v[160:163]\n\t"
    "s_waitcnt lgkmcnt(2)\n\t"
    "ds_read_b128 a[104:107], v14 offset:8192\n\t"
    "v_add_f32 v45, v45, v0\n\t"
    "v_mfma_f32_16x16x32_bf16 v[160:163], a[140:143], v[148:151], v[160:163]\n\t"
    "v_permlane32_swap_b32 v8, v9\n\t"
    "v_mfma_f32_16x16x32_bf16 v[60:63], a[112:115], a[16:19], v[4:7]\n\t"
    "v_max_f32 v8, v8, v9\n\t"
    "v_mov_b32 v9, v8\n\t"
    "v_mfma_f32_16x16x32_bf16 v[60:63], a[116:119], a[20:23], v[60:63]\n\t"
    "ds_read_b128 a[108:111], v15 offset:8192\n\t"
    "v_add_f32 v46, v46, v0\n\t"
    "v_mfma_f32_16x16x32_bf16 v[60:63], a[120:123], a[24:27], v[60:63]\n\t"
    "v_permlane16_swap_b32 v8, v9\n\t"
    "v_mfma_f32_16x16x32_bf16 v[60:63], a[124:127], a[28:31], v[60:63]\n\t"
    "ds_read_b64_tr_b16 a[144:145], v17 offset:32768\n\t"
    "v_min_f32 v10, -v8, -v9\n\t"
    "v_mfma_f32_16x16x32_bf16 v[164:167], a[128:131], v[116:119], v[164:167]\n\t"
    "v_mov_b32 v8, v10\n\t"
    "v_mov_b32 v9, v10\n\t"
    "v_mov_b32 v11, v10\n\t"
    "v_mfma_f32_16x16x32_bf16 v[164:167], a[132:135], v[128:131], v[164:167]\n\t"
    "ds_read_b64_tr_b16 a[146:147], v17 offset:34816\n\t"
    "v_add_f32 v47, v47, v0\n\t"
    "v_mfma_f32_16x16x32_bf16 v[164:167], a[136:139], v[140:143], v[164:167]\n\t"
    "buffer_load_dwordx4 v22, s[40:43], s78 offen offset:2048 nt lds\n\t"
    "buffer_store_dwordx4 a[220:223], v33, s[48:51], s66 offen offset:2176 nt\n\t"
    "v_mfma_f32_16x16x32_bf16 v[164:167], a[140:143], v[152:155], v[164:167]\n\t"
    "ds_read_b64_tr_b16 a[148:149], v17 offset:36864\n\t"
    "v_add_f32 v48, v48, v4\n\t"
    "v_mfma_f32_16x16x32_bf16 a[160:163], a[172:175], v[112:115], a[160:163]\n\t"
    "ds_read_b64_tr_b16 a[150:151], v17 offset:38912\n\t"
    "v_add_f32 v49, v49, v4\n\t"
    "v_mfma_f32_16x16x32_bf16 a[160:163], a[172:175], v[124:127], a[160:163]\n\t"
    "buffer_load_dwordx4 v23, s[40:43], s78 offen offset:3072 nt lds\n\t"
    "v_mfma_f32_16x16x32_bf16 a[160:163], a[172:175], v[136:139], a[160:163]\n\t"
    "ds_read_b64_tr_b16 a[152:153], v17 offset:40960\n\t"
    "v_add_f32 v50, v50, v4\n\t"
    "v_mfma_f32_16x16x32_bf16 a[160:163], a[172:175], v[148:151], a[160:163]\n\t"
    "v_exp_f32 v44, v44\n\t"
    "v_mfma_f32_16x16x32_bf16 v[40:43], a[112:115], a[32:35], v[8:11]\n\t"
    "v_exp_f32 v45, v45\n\t"
    "v_mfma_f32_16x16x32_bf16 v[40:43], a[116:119], a[36:39], v[40:43]\n\t"
    "ds_read_b64_tr_b16 a[154:155], v17 offset:43008\n\t"
    "v_cvt_pk_bf16_f32 v64, v44, v45\n\t"
    "v_mfma_f32_16x16x32_bf16 v[40:43], a[120:123], a[40:43], v[40:43]\n\t"
    "v_exp_f32 v46, v46\n\t"
    "v_mfma_f32_16x16x32_bf16 v[40:43], a[124:127], a[44:47], v[40:43]\n\t"
    "ds_read_b64_tr_b16 a[156:157], v17 offset:45056\n\t"
    "v_add_f32 v51, v51, v4\n\t"
    "v_mfma_f32_16x16x32_bf16 v[168:171], a[128:131], v[120:123], v[168:171]\n\t"
    "s_add_u32 s79, s63, 128\n\t"
    "v_exp_f32 v47, v47\n\t"
    "v_mfma_f32_16x16x32_bf16 v[168:171], a[132:135], v[132:135], v[168:171]\n\t"
    "s_add_u32 m0, s77, 16384\n\t"
    "v_cvt_pk_bf16_f32 v65, v46, v47\n\t"
    "v_accvgpr_read_b32 v35, a160\n\t"
    "v_mfma_f32_16x16x32_bf16 v[168:171], a[136:139], v[144:147], v[168:171]\n\t"
    "ds_read_b64_tr_b16 a[158:159], v17 offset:47104\n\t"
    "v_cmp_nlt_f32 s[86:87], v35, s54\n\t"
    "v_mfma_f32_16x16x32_bf16 v[168:171], a[140:143], v[156:159], v[168:171]\n\t"
    "s_waitcnt lgkmcnt(3)\n\t"
    "buffer_load_dwordx4 v24, s[44:47], s79 offen offset:0 nt lds\n\t"
    "v_mfma_f32_16x16x32_bf16 v[44:47], a[96:99], a[0:3], v[0:3]\n\t"
    "s_cmp_lg_u64 s[86:87], 0\n\ts_cselect_b32 s81, 2, 0\n\ts_or_b32 s80, s80, s81\n\t"
    "ds_read_b128 a[112:115], v12 offset:12288\n\t"
    "v_cmp_ngt_f32 s[92:93], s99, v35\n\t"
    "v_mfma_f32_16x16x32_bf16 v[44:47], a[100:103], a[4:7], v[44:47]\n\t"
    "v_exp_f32 v48, v48\n\t"
    "v_mfma_f32_16x16x32_bf16 v[44:47], a[104:107], a[8:11], v[44:47]\n\t"
    "s_cmp_lg_u64 s[92:93], 0\n\ts_cselect_b32 s81, 16, 0\n\ts_or_b32 s80, s80, s81\n\t"
    "ds_read_b128 a[116:119], v13 offset:12288\n\t"
    "v_add_f32 v52, v52, v8\n\t"
    "v_mfma_f32_16x16x32_bf16 v[44:47], a[108:111], a[12:15], v[44:47]\n\t"
    "v_exp_f32 v49, v49\n\t"
    "v_mfma_f32_16x16x32_bf16 v[172:175], a[144:147], v[112:115], v[172:175]\n\t"
    "ds_read_b128 a[120:123], v14 offset:12288\n\t"
    "v_cvt_pk_bf16_f32 v68, v48, v49\n\t"
    "v_mfma_f32_16x16x32_bf16 v[172:175], a[148:151], v[124:127], v[172:175]\n\t"
    "s_waitcnt lgkmcnt(4)\n\t"
    "v_exp_f32 v50, v50\n\t"
    "v_mfma_f32_16x16x32_bf16 v[172:175], a[152:155], v[136:139], v[172:175]\n\t"
    "s_waitcnt lgkmcnt(3)\n\t"
    "v_exp_f32 v51, v51\n\t"
    "v_mfma_f32_16x16x32_bf16 v[172:175], a[156:159], v[148:151], v[172:175]\n\t"
    "ds_read_b128 a[124:127], v15 offset:12288\n\t"
    "v_cvt_pk_bf16_f32 v69, v50, v51\n\t"
    "v_mfma_f32_16x16x32_bf16 v[48:51], a[96:99], a[16:19], v[4:7]\n\t"
    "buffer_load_dwordx4 v25, s[44:47], s79 offen offset:1024 nt lds\n\t"
    "v_mfma_f32_16x16x32_bf16 v[48:51], a[100:103], a[20:23], v[48:51]\n\t"
    "ds_read_b64_tr_b16 a[128:129], v18 offset:32768\n\t"
    "v_add_f32 v53, v53, v8\n\t"
    "v_mfma_f32_16x16x32_bf16 v[48:51], a[104:107], a[24:27], v[48:51]\n\t"
    "v_rcp_f32 v35, v35\n\t"
    "v_mfma_f32_16x16x32_bf16 v[48:51], a[108:111], a[28:31], v[48:51]\n\t"
    "ds_read_b64_tr_b16 a[130:131], v18 offset:34816\n\t"
    "v_add_f32 v54, v54, v8\n\t"
    "v_mfma_f32_16x16x32_bf16 v[176:179], a[144:147], v[116:119], v[176:179]\n\t"
    "v_add_f32 v55, v55, v8\n\t"
    "v_cmp_ngt_f32 s[92:93], s99, v35\n\t"
    "v_mfma_f32_16x16x32_bf16 v[176:179], a[148:151], v[128:131], v[176:179]\n\t"
    "v_exp_f32 v52, v52\n\t"
    "v_mfma_f32_16x16x32_bf16 v[176:179], a[152:155], v[140:143], v[176:179]\n\t"
    "s_cmp_lg_u64 s[92:93], 0\n\ts_cselect_b32 s81, 2, 0\n\ts_or_b32 s80, s80, s81\n\t"
    "ds_read_b64_tr_b16 a[132:133], v18 offset:36864\n\t"
    "v_mul_f32 v160, v35, v160\n\t"
    "v_mfma_f32_16x16x32_bf16 v[176:179], a[156:159], v[152:155], v[176:179]\n\t"
    "v_exp_f32 v53, v53\n\t"
    "v_mfma_f32_16x16x32_bf16 a[164:167], a[172:175], v[116:119], a[164:167]\n\t"
    "ds_read_b64_tr_b16 a[134:135], v18 offset:38912\n\t"
    "v_cvt_pk_bf16_f32 v72, v52, v53\n\t"
    "v_mfma_f32_16x16x32_bf16 a[164:167], a[172:175], v[128:131], a[164:167]\n\t"
    "v_exp_f32 v54, v54\n\t"
    "v_mfma_f32_16x16x32_bf16 a[164:167], a[172:175], v[140:143], a[164:167]\n\t"
    "v_exp_f32 v55, v55\n\t"
    "v_mfma_f32_16x16x32_bf16 a[164:167], a[172:175], v[152:155], a[164:167]\n\t"
    "ds_read_b64_tr_b16 a[136:137], v18 offset:40960\n\t"
    "v_cvt_pk_bf16_f32 v73, v54, v55\n\t"
    "v_mfma_f32_16x16x32_bf16 v[52:55], a[96:99], a[32:35], v[8:11]\n\t"
    "buffer_load_dwordx4 v26, s[44:47], s79 offen offset:2048 nt lds\n\t"
    "v_mfma_f32_16x16x32_bf16 v[52:55], a[100:103], a[36:39], v[52:55]\n\t"
    "ds_read_b64_tr_b16 a[138:139], v18 offset:43008\n\t"
    "v_mul_f32 v161, v35, v161\n\t"
    "v_mfma_f32_16x16x32_bf16 v[52:55], a[104:107], a[40:43], v[52:55]\n\t"
    "v_exp_f32 v56, v56\n\t"
    "v_mfma_f32_16x16x32_bf16 v[52:55], a[108:111], a[44:47], v[52:55]\n\t"
    "ds_read_b64_tr_b16 a[140:141], v18 offset:45056\n\t"
    "v_mul_f32 v162, v35, v162\n\t"
    "v_mfma_f32_16x16x32_bf16 v[180:183], a[144:147], v[120:123], v[180:183]\n\t"
    "v_exp_f32 v57, v57\n\t"
    "v_mfma_f32_16x16x32_bf16 v[180:183], a[148:151], v[132:135], v[180:183]\n\t"
    "v_exp_f32 v58, v58\n\t"
    "v_mfma_f32_16x16x32_bf16 v[180:183], a[152:155], v[144:147], v[180:183]\n\t"
    "ds_read_b64_tr_b16 a[142:143], v18 offset:47104\n\t"
    "v_exp_f32 v59, v59\n\t"
    "v_mfma_f32_16x16x32_bf16 v[180:183], a[156:159], v[156:159], v[180:183]\n\t"
    "s_waitcnt lgkmcnt(3)\n\t"
    "v_cvt_pk_bf16_f32 v66, v56, v57\n\t"
    "v_cvt_pk_bf16_f32 v67, v58, v59\n\t"
    "v_mfma_f32_16x16x32_bf16 v[56:59], a[112:115], a[0:3], v[0:3]\n\t"
    "ds_read_b128 a[96:99], v12 offset:16384\n\t"
    "v_accvgpr_read_b32 v36, a164\n\t"
    "v_mfma_f32_16x16x32_bf16 v[56:59], a[116:119], a[4:7], v[56:59]\n\t"
    "v_exp_f32 v60, v60\n\t"
    "v_mfma_f32_16x16x32_bf16 v[56:59], a[120:123], a[8:11], v[56:59]\n\t"
    "ds_read_b128 a[100:103], v13 offset:16384\n\t"
    "v_cmp_nlt_f32 s[88:89], v36, s54\n\t"
    "v_mfma_f32_16x16x32_bf16 v[56:59], a[124:127], a[12:15], v[56:59]\n\t"
    "v_exp_f32 v61, v61\n\t"
    "v_mfma_f32_16x16x32_bf16 v[184:187], a[128:131], v[112:115], v[184:187]\n\t"
    "s_cmp_lg_u64 s[88:89], 0\n\ts_cselect_b32 s81, 2, 0\n\ts_or_b32 s80, s80, s81\n\t"
    "ds_read_b128 a[104:107], v14 offset:16384\n\t"
    "v_cvt_pk_bf16_f32 v70, v60, v61\n\t"
    "v_mfma_f32_16x16x32_bf16 v[184:187], a[132:135], v[124:127], v[184:187]\n\t"
    "s_waitcnt lgkmcnt(4)\n\t"
    "v_exp_f32 v62, v62\n\t"
    "v_mfma_f32_16x16x32_bf16 v[184:187], a[136:139], v[136:139], v[184:187]\n\t"
    "s_waitcnt lgkmcnt(3)\n\t"
    "v_exp_f32 v63, v63\n\t"
    "v_mfma_f32_16x16x32_bf16 v[184:187], a[140:143], v[148:151], v[184:187]\n\t"
    "ds_read_b128 a[108:111], v15 offset:16384\n\t"
    "v_cvt_pk_bf16_f32 v71, v62, v63\n\t"
    "v_mfma_f32_16x16x32_bf16 v[60:63], a[112:115], a[16:19], v[4:7]\n\t"
    "buffer_load_dwordx4 v27, s[44:47], s79 offen offset:3072 nt lds\n\t"
    "v_mfma_f32_16x16x32_bf16 v[60:63], a[116:119], a[20:23], v[60:63]\n\t"
    "ds_read_b64_tr_b16 a[144:145], v19 offset:32768\n\t"
    "v_cmp_ngt_f32 s[92:93], s99, v36\n\t"
    "v_mfma_f32_16x16x32_bf16 v[60:63], a[120:123], a[24:27], v[60:63]\n\t"
    "v_rcp_f32 v36, v36\n\t"
    "v_mfma_f32_16x16x32_bf16 v[60:63], a[124:127], a[28:31], v[60:63]\n\t"
    "s_cmp_lg_u64 s[92:93], 0\n\ts_cselect_b32 s81, 16, 0\n\ts_or_b32 s80, s80, s81\n\t"
    "ds_read_b64_tr_b16 a[146:147], v19 offset:34816\n\t"
    "v_cmp_ngt_f32 s[92:93], s99, v36\n\t"
    "v_mfma_f32_16x16x32_bf16 v[188:191], a[128:131], v[116:119], v[188:191]\n\t"
    "v_exp_f32 v40, v40\n\t"
    "v_mfma_f32_16x16x32_bf16 v[188:191], a[132:135], v[128:131], v[188:191]\n\t"
    "s_cmp_lg_u64 s[92:93], 0\n\ts_cselect_b32 s81, 2, 0\n\ts_or_b32 s80, s80, s81\n\t"
    "ds_read_b64_tr_b16 a[148:149], v19 offset:36864\n\t"
    "v_mul_f32 v163, v35, v163\n\t"
    "v_mfma_f32_16x16x32_bf16 v[188:191], a[136:139], v[140:143], v[188:191]\n\t"
    "v_exp_f32 v41, v41\n\t"
    "v_mfma_f32_16x16x32_bf16 v[188:191], a[140:143], v[152:155], v[188:191]\n\t"
    "ds_read_b64_tr_b16 a[150:151], v19 offset:38912\n\t"
    "v_cvt_pk_bf16_f32 v74, v40, v41\n\t"
    "v_mfma_f32_16x16x32_bf16 a[168:171], a[172:175], v[120:123], a[168:171]\n\t"
    "v_exp_f32 v42, v42\n\t"
    "v_mfma_f32_16x16x32_bf16 a[168:171], a[172:175], v[132:135], a[168:171]\n\t"
    "v_exp_f32 v43, v43\n\t"
    "v_mfma_f32_16x16x32_bf16 a[168:171], a[172:175], v[144:147], a[168:171]\n\t"
    "ds_read_b64_tr_b16 a[152:153], v19 offset:40960\n\t"
    "v_cvt_pk_bf16_f32 v75, v42, v43\n\t"
    "v_mfma_f32_16x16x32_bf16 a[168:171], a[172:175], v[156:159], a[168:171]\n\t"
    "v_exp_f32 v44, v44\n\t"
    "v_mfma_f32_16x16x32_bf16 v[40:43], a[112:115], a[32:35], v[8:11]\n\t"
    "ds_read_b64_tr_b16 a[154:155], v19 offset:43008\n\t"
    "v_cvt_pk_bf16_f32 v160, v160, v161\n\t"
    "v_mfma_f32_16x16x32_bf16 v[40:43], a[116:119], a[36:39], v[40:43]\n\t"
    "v_exp_f32 v45, v45\n\t"
    "v_mfma_f32_16x16x32_bf16 v[40:43], a[120:123], a[40:43], v[40:43]\n\t"
    "ds_read_b64_tr_b16 a[156:157], v19 offset:45056\n\t"
    "v_cvt_pk_bf16_f32 v76, v44, v45\n\t"
    "v_mfma_f32_16x16x32_bf16 v[40:43], a[124:127], a[44:47], v[40:43]\n\t"
    "v_exp_f32 v46, v46\n\t"
    "v_mfma_f32_16x16x32_bf16 v[192:195], a[128:131], v[120:123], v[192:195]\n\t"
    "v_exp_f32 v47, v47\n\t"
    "v_mfma_f32_16x16x32_bf16 v[192:195], a[132:135], v[132:135], v[192:195]\n\t"
    "ds_read_b64_tr_b16 a[158:159], v19 offset:47104\n\t"
    "v_cvt_pk_bf16_f32 v77, v46, v47\n\t"
    "v_mfma_f32_16x16x32_bf16 v[192:195], a[136:139], v[144:147], v[192:195]\n\t"
    "v_exp_f32 v48, v48\n\t"
    "v_mfma_f32_16x16x32_bf16 v[192:195], a[140:143], v[156:159], v[192:195]\n\t"
    "s_waitcnt lgkmcnt(3)\n\t"
    "v_exp_f32 v49, v49\n\t"
    "v_mfma_f32_16x16x32_bf16 v[44:47], a[96:99], a[0:3], v[0:3]\n\t"
    "v_exp_f32 v50, v50\n\t"
    "v_mfma_f32_16x16x32_bf16 v[44:47], a[100:103], a[4:7], v[44:47]\n\t"
    "v_exp_f32 v51, v51\n\t"
    "v_mfma_f32_16x16x32_bf16 v[44:47], a[104:107], a[8:11], v[44:47]\n\t"
    "v_cvt_pk_bf16_f32 v80, v48, v49\n\t"
    "v_cvt_pk_bf16_f32 v81, v50, v51\n\t"
    "v_mfma_f32_16x16x32_bf16 v[44:47], a[108:111], a[12:15], v[44:47]\n\t"
    "v_exp_f32 v52, v52\n\t"
    "v_mfma_f32_16x16x32_bf16 v[196:199], a[144:147], v[112:115], v[196:199]\n\t"
    "v_exp_f32 v53, v53\n\t"
    "v_mfma_f32_16x16x32_bf16 v[196:199], a[148:151], v[124:127], v[196:199]\n\t"
    "s_waitcnt lgkmcnt(0)\n\t"
    "v_exp_f32 v54, v54\n\t"
    "v_mfma_f32_16x16x32_bf16 v[196:199], a[152:155], v[136:139], v[196:199]\n\t"
    "s_waitcnt vmcnt(20) lgkmcnt(0)\n\t"
    "v_exp_f32 v55, v55\n\t"
    "v_mfma_f32_16x16x32_bf16 v[196:199], a[156:159], v[148:151], v[196:199]\n\t"
    "s_barrier\n\t"
    "ds_read_b128 a[112:115], v12 offset:20480\n\t"
    "v_accvgpr_read_b32 v37, a168\n\t"
    "v_mfma_f32_16x16x32_bf16 v[48:51], a[96:99], a[16:19], v[4:7]\n\t"
    "s_add_u32 s78, s57, 69632\n\t"
    "ds_read_b128 a[116:119], v13 offset:20480\n\t"
    "v_cvt_pk_bf16_f32 v84, v52, v53\n\t"
    "v_mfma_f32_16x16x32_bf16 v[48:51], a[100:103], a[20:23], v[48:51]\n\t"
    "s_mov_b32 m0, s73\n\t"
    "ds_read_b128 a[120:123], v14 offset:20480\n\t"
    "v_cvt_pk_bf16_f32 v85, v54, v55\n\t"
    "v_mfma_f32_16x16x32_bf16 v[48:51], a[104:107], a[24:27], v[48:51]\n\t"
    "v_cmp_nlt_f32 s[90:91], v37, s54\n\t"
    "v_cmp_ngt_f32 s[92:93], s99, v37\n\t"
    "v_mfma_f32_16x16x32_bf16 v[48:51], a[108:111], a[28:31], v[48:51]\n\t"
    "ds_read_b128 a[124:127], v15 offset:20480\n\t"
    "v_cvt_pk_bf16_f32 v161, v162, v163\n\t"
    "v_mfma_f32_16x16x32_bf16 v[200:203], a[144:147], v[116:119], v[200:203]\n\t"
    "s_cmp_lg_u64 s[90:91], 0\n\ts_cselect_b32 s81, 2, 0\n\ts_or_b32 s80, s80, s81\n\t"
    "ds_read_b64_tr_b16 a[128:129], v16 offset:49152\n\t"
    "v_mul_f32 v172, v35, v172\n\t"
    "v_mfma_f32_16x16x32_bf16 v[200:203], a[148:151], v[128:131], v[200:203]\n\t"
    "s_cmp_lg_u64 s[92:93], 0\n\ts_cselect_b32 s81, 16, 0\n\ts_or_b32 s80, s80, s81\n\t"
    "ds_read_b64_tr_b16 a[130:131], v16 offset:51200\n\t"
    "v_mul_f32 v173, v35, v173\n\t"
    "v_mfma_f32_16x16x32_bf16 v[200:203], a[152:155], v[140:143], v[200:203]\n\t"
    "v_rcp_f32 v37, v37\n\t"
    "v_mfma_f32_16x16x32_bf16 v[200:203], a[156:159], v[152:155], v[200:203]\n\t"
    "ds_read_b64_tr_b16 a[132:133], v16 offset:53248\n\t"
    "v_cmp_ngt_f32 s[92:93], s99, v37\n\t"
    "v_mfma_f32_16x16x32_bf16 v[52:55], a[96:99], a[32:35], v[8:11]\n\t"
    "buffer_load_dwordx4 v20, s[40:43], s78 offen offset:0 nt lds\n\t"
    "v_mul_f32 v174, v35, v174\n\t"
    "v_mfma_f32_16x16x32_bf16 v[52:55], a[100:103], a[36:39], v[52:55]\n\t"
    "s_cmp_lg_u64 s[92:93], 0\n\ts_cselect_b32 s81, 2, 0\n\ts_or_b32 s80, s80, s81\n\t"
    "ds_read_b64_tr_b16 a[134:135], v16 offset:55296\n\t"
    "v_mul_f32 v175, v35, v175\n\t"
    "v_mfma_f32_16x16x32_bf16 v[52:55], a[104:107], a[40:43], v[52:55]\n\t"
    "ds_read_b64_tr_b16 a[136:137], v16 offset:57344\n\t"
    "v_cvt_pk_bf16_f32 v172, v172, v173\n\t"
    "v_mfma_f32_16x16x32_bf16 v[52:55], a[108:111], a[44:47], v[52:55]\n\t"
    "ds_read_b64_tr_b16 a[138:139], v16 offset:59392\n\t"
    "v_exp_f32 v56, v56\n\t"
    "v_mfma_f32_16x16x32_bf16 v[204:207], a[144:147], v[120:123], v[204:207]\n\t"
    "ds_read_b64_tr_b16 a[140:141], v16 offset:61440\n\t"
    "v_exp_f32 v57, v57\n\t"
    "v_mfma_f32_16x16x32_bf16 v[204:207], a[148:151], v[132:135], v[204:207]\n\t"
    "v_exp_f32 v58, v58\n\t"
    "v_mfma_f32_16x16x32_bf16 v[204:207], a[152:155], v[144:147], v[204:207]\n\t"
    "ds_read_b64_tr_b16 a[142:143], v16 offset:63488\n\t"
    "v_exp_f32 v59, v59\n\t"
    "v_mfma_f32_16x16x32_bf16 v[204:207], a[156:159], v[156:159], v[204:207]\n\t"
    "s_waitcnt lgkmcnt(5)\n\t"
    "v_cvt_pk_bf16_f32 v78, v56, v57\n\t"
    "v_cvt_pk_bf16_f32 v79, v58, v59\n\t"
    "v_cvt_pk_bf16_f32 v173, v174, v175\n\t"
    "v_mfma_f32_16x16x32_bf16 v[56:59], a[112:115], a[0:3], v[0:3]\n\t"
    "ds_read_b128 a[96:99], v12 offset:24576\n\t"
    "v_mul_f32 v184, v35, v184\n\t"
    "v_mfma_f32_16x16x32_bf16 v[56:59], a[116:119], a[4:7], v[56:59]\n\t"
    "buffer_load_dwordx4 v21, s[40:43], s78 offen offset:1024 nt lds\n\t"
    "v_mul_f32 v185, v35, v185\n\t"
    "v_mfma_f32_16x16x32_bf16 v[56:59], a[120:123], a[8:11], v[56:59]\n\t"
    "ds_read_b128 a[100:103], v13 offset:24576\n\t"
    "v_mul_f32 v186, v35, v186\n\t"
    "v_mfma_f32_16x16x32_bf16 v[56:59], a[124:127], a[12:15], v[56:59]\n\t"
    "v_exp_f32 v60, v60\n\t"
    "v_mfma_f32_16x16x32_bf16 v[208:211], a[128:131], v[112:115], v[208:211]\n\t"
    "s_waitcnt lgkmcnt(3)\n\t"
    "ds_read_b128 a[104:107], v14 offset:24576\n\t"
    "v_exp_f32 v61, v61\n\t"
    "v_mfma_f32_16x16x32_bf16 v[208:211], a[132:135], v[124:127], v[208:211]\n\t"
    "v_exp_f32 v62, v62\n\t"
    "v_mfma_f32_16x16x32_bf16 v[208:211], a[136:139], v[136:139], v[208:211]\n\t"
    "s_waitcnt lgkmcnt(3)\n\t"
    "v_exp_f32 v63, v63\n\t"
    "v_cvt_pk_bf16_f32 v82, v60, v61\n\t"
    "v_mfma_f32_16x16x32_bf16 v[208:211], a[140:143], v[148:151], v[208:211]\n\t"
    "ds_read_b128 a[108:111], v15 offset:24576\n\t"
    "v_cvt_pk_bf16_f32 v83, v62, v63\n\t"
    "v_mfma_f32_16x16x32_bf16 v[60:63], a[112:115], a[16:19], v[4:7]\n\t"
    "ds_read_b64_tr_b16 a[144:145], v17 offset:49152\n\t"
    "v_exp_f32 v40, v40\n\t"
    "v_mfma_f32_16x16x32_bf16 v[60:63], a[116:119], a[20:23], v[60:63]\n\t"
    "buffer_load_dwordx4 v22, s[40:43], s78 offen offset:2048 nt lds\n\t"
    "v_mfma_f32_16x16x32_bf16 v[60:63], a[120:123], a[24:27], v[60:63]\n\t"
    "ds_read_b64_tr_b16 a[146:147], v17 offset:51200\n\t"
    "v_exp_f32 v41, v41\n\t"
    "v_mfma_f32_16x16x32_bf16 v[60:63], a[124:127], a[28:31], v[60:63]\n\t"
    "v_exp_f32 v42, v42\n\t"
    "v_mfma_f32_16x16x32_bf16 v[212:215], a[128:131], v[116:119], v[212:215]\n\t"
    "ds_read_b64_tr_b16 a[148:149], v17 offset:53248\n\t"
    "v_exp_f32 v43, v43\n\t"
    "v_mfma_f32_16x16x32_bf16 v[212:215], a[132:135], v[128:131], v[212:215]\n\t"
    "v_cvt_pk_bf16_f32 v86, v40, v41\n\t"
    "v_cvt_pk_bf16_f32 v87, v42, v43\n\t"
    "v_mfma_f32_16x16x32_bf16 v[212:215], a[136:139], v[140:143], v[212:215]\n\t"
    "ds_read_b64_tr_b16 a[150:151], v17 offset:55296\n\t"
    "v_exp_f32 v44, v44\n\t"
    "v_mfma_f32_16x16x32_bf16 v[212:215], a[140:143], v[152:155], v[212:215]\n\t"
    "v_exp_f32 v45, v45\n\t"
    "v_mfma_f32_16x16x32_bf16 v[40:43], a[112:115], a[32:35], v[8:11]\n\t"
    "ds_read_b64_tr_b16 a[152:153], v17 offset:57344\n\t"
    "v_exp_f32 v46, v46\n\t"
    "v_mfma_f32_16x16x32_bf16 v[40:43], a[116:119], a[36:39], v[40:43]\n\t"
    "v_exp_f32 v47, v47\n\t"
    "v_mfma_f32_16x16x32_bf16 v[40:43], a[120:123], a[40:43], v[40:43]\n\t"
    "ds_read_b64_tr_b16 a[154:155], v17 offset:59392\n\t"
    "v_cvt_pk_bf16_f32 v88, v44, v45\n\t"
    "v_cvt_pk_bf16_f32 v89, v46, v47\n\t"
    "v_mfma_f32_16x16x32_bf16 v[40:43], a[124:127], a[44:47], v[40:43]\n\t"
    "buffer_load_dwordx4 v23, s[40:43], s78 offen offset:3072 nt lds\n\t"
    "v_mfma_f32_16x16x32_bf16 v[216:219], a[128:131], v[120:123], v[216:219]\n\t"
    "ds_read_b64_tr_b16 a[156:157], v17 offset:61440\n\t"
    "v_exp_f32 v48, v48\n\t"
    "v_mfma_f32_16x16x32_bf16 v[216:219], a[132:135], v[132:135], v[216:219]\n\t"
    "v_exp_f32 v49, v49\n\t"
    "v_mfma_f32_16x16x32_bf16 v[216:219], a[136:139], v[144:147], v[216:219]\n\t"
    "ds_read_b64_tr_b16 a[158:159], v17 offset:63488\n\t"
    "v_exp_f32 v50, v50\n\t"
    "v_mfma_f32_16x16x32_bf16 v[216:219], a[140:143], v[156:159], v[216:219]\n\t"
    "s_waitcnt lgkmcnt(4)\n\t"
    "v_exp_f32 v51, v51\n\t"
    "v_mfma_f32_16x16x32_bf16 v[44:47], a[96:99], a[0:3], v[0:3]\n\t"
    "s_add_u32 s79, s63, 32768\n\t"
    "ds_read_b128 a[112:115], v12 offset:28672\n\t"
    "v_cvt_pk_bf16_f32 v92, v48, v49\n\t"
    "v_cvt_pk_bf16_f32 v93, v50, v51\n\t"
    "v_mfma_f32_16x16x32_bf16 v[44:47], a[100:103], a[4:7], v[44:47]\n\t"
    "s_add_u32 m0, s77, 32768\n\t"
    "v_exp_f32 v52, v52\n\t"
    "v_mul_f32 v187, v35, v187\n\t"
    "v_mfma_f32_16x16x32_bf16 v[44:47], a[104:107], a[8:11], v[44:47]\n\t"
    "ds_read_b128 a[116:119], v13 offset:28672\n\t"
    "v_cvt_pk_bf16_f32 v184, v184, v185\n\t"
    "v_mfma_f32_16x16x32_bf16 v[44:47], a[108:111], a[12:15], v[44:47]\n\t"
    "buffer_load_dwordx4 v24, s[44:47], s79 offen offset:0 nt lds\n\t"
    "v_cvt_pk_bf16_f32 v185, v186, v187\n\t"
    "v_mfma_f32_16x16x32_bf16 v[220:223], a[144:147], v[112:115], v[220:223]\n\t"
    "ds_read_b128 a[120:123], v14 offset:28672\n\t"
    "v_mul_f32 v196, v35, v196\n\t"
    "v_mfma_f32_16x16x32_bf16 v[220:223], a[148:151], v[124:127], v[220:223]\n\t"
    "s_waitcnt lgkmcnt(4)\n\t"
    "v_exp_f32 v53, v53\n\t"
    "v_mul_f32 v197, v35, v197\n\t"
    "v_mfma_f32_16x16x32_bf16 v[220:223], a[152:155], v[136:139], v[220:223]\n\t"
    "s_waitcnt lgkmcnt(3)\n\t"
    "ds_read_b128 a[124:127], v15 offset:28672\n\t"
    "v_cvt_pk_bf16_f32 v96, v52, v53\n\t"
    "v_mfma_f32_16x16x32_bf16 v[220:223], a[156:159], v[148:151], v[220:223]\n\t"
    "v_exp_f32 v54, v54\n\t"
    "v_mul_f32 v198, v35, v198\n\t"
    "v_mfma_f32_16x16x32_bf16 v[48:51], a[96:99], a[16:19], v[4:7]\n\t"
    "ds_read_b64_tr_b16 a[128:129], v18 offset:49152\n\t"
    "v_mul_f32 v199, v35, v199\n\t"
    "v_mfma_f32_16x16x32_bf16 v[48:51], a[100:103], a[20:23], v[48:51]\n\t"
    "v_exp_f32 v55, v55\n\t"
    "v_cvt_pk_bf16_f32 v196, v196, v197\n\t"
    "v_mfma_f32_16x16x32_bf16 v[48:51], a[104:107], a[24:27], v[48:51]\n\t"
    "ds_read_b64_tr_b16 a[130:131], v18 offset:51200\n\t"
    "v_cvt_pk_bf16_f32 v97, v54, v55\n\t"
    "v_mfma_f32_16x16x32_bf16 v[48:51], a[108:111], a[28:31], v[48:51]\n\t"
    "v_exp_f32 v56, v56\n\t"
    "v_cvt_pk_bf16_f32 v197, v198, v199\n\t"
    "v_mfma_f32_16x16x32_bf16 v[224:227], a[144:147], v[116:119], v[224:227]\n\t"
    "ds_read_b64_tr_b16 a[132:133], v18 offset:53248\n\t"
    "v_mul_f32 v164, v36, v164\n\t"
    "v_mfma_f32_16x16x32_bf16 v[224:227], a[148:151], v[128:131], v[224:227]\n\t"
    "buffer_load_dwordx4 v25, s[44:47], s79 offen offset:1024 nt lds\n\t"
    "v_mul_f32 v165, v36, v165\n\t"
    "v_mfma_f32_16x16x32_bf16 v[224:227], a[152:155], v[140:143], v[224:227]\n\t"
    "ds_read_b64_tr_b16 a[134:135], v18 offset:55296\n\t"
    "v_exp_f32 v57, v57\n\t"
    "v_mfma_f32_16x16x32_bf16 v[224:227], a[156:159], v[152:155], v[224:227]\n\t"
    "v_exp_f32 v58, v58\n\t"
    "v_mfma_f32_16x16x32_bf16 v[52:55], a[96:99], a[32:35], v[8:11]\n\t"
    "ds_read_b64_tr_b16 a[136:137], v18 offset:57344\n\t"
    "v_exp_f32 v59, v59\n\t"
    "v_mfma_f32_16x16x32_bf16 v[52:55], a[100:103], a[36:39], v[52:55]\n\t"
    "v_cvt_pk_bf16_f32 v90, v56, v57\n\t"
    "v_cvt_pk_bf16_f32 v91, v58, v59\n\t"
    "v_mfma_f32_16x16x32_bf16 v[52:55], a[104:107], a[40:43], v[52:55]\n\t"
    "ds_read_b64_tr_b16 a[138:139], v18 offset:59392\n\t"
    "v_exp_f32 v60, v60\n\t"
    "v_mfma_f32_16x16x32_bf16 v[52:55], a[108:111], a[44:47], v[52:55]\n\t"
    "ds_write_b64 v32, v[160:161] offset:4096\n\t"
    "v_mfma_f32_16x16x32_bf16 v[228:231], a[144:147], v[120:123], v[228:231]\n\t"
    "v_exp_f32 v61, v61\n\t"
    "v_mfma_f32_16x16x32_bf16 v[228:231], a[148:151], v[132:135], v[228:231]\n\t"
    "ds_read_b64_tr_b16 a[140:141], v18 offset:61440\n\t"
    "v_cvt_pk_bf16_f32 v94, v60, v61\n\t"
    "v_mfma_f32_16x16x32_bf16 v[228:231], a[152:155], v[144:147], v[228:231]\n\t"
    "ds_read_b64_tr_b16 a[142:143], v18 offset:63488\n\t"
    "v_exp_f32 v62, v62\n\t"
    "v_mfma_f32_16x16x32_bf16 v[228:231], a[156:159], v[156:159], v[228:231]\n\t"
    "s_waitcnt lgkmcnt(5)\n\t"
    "buffer_load_dwordx4 v26, s[44:47], s79 offen offset:2048 nt lds\n\t"
    "v_mfma_f32_16x16x32_bf16 v[56:59], a[112:115], a[0:3], v[0:3]\n\t"
    "ds_read_b128 a[96:99], v12 offset:32768\n\t"
    "v_exp_f32 v63, v63\n\t"
    "v_mfma_f32_16x16x32_bf16 v[56:59], a[116:119], a[4:7], v[56:59]\n\t"
    "ds_read_b128 a[100:103], v13 offset:32768\n\t"
    "v_cvt_pk_bf16_f32 v95, v62, v63\n\t"
    "v_mul_f32 v166, v36, v166\n\t"
    "v_mfma_f32_16x16x32_bf16 v[56:59], a[120:123], a[8:11], v[56:59]\n\t"
    "v_exp_f32 v40, v40\n\t"
    "v_mfma_f32_16x16x32_bf16 v[56:59], a[124:127], a[12:15], v[56:59]\n\t"
    "ds_read_b128 a[104:107], v14 offset:32768\n\t"
    "v_exp_f32 v41, v41\n\t"
    "v_mfma_f32_16x16x32_bf16 v[232:235], a[128:131], v[112:115], v[232:235]\n\t"
    "ds_write_b64 v32, v[172:173] offset:4128\n\t"
    "v_mfma_f32_16x16x32_bf16 v[232:235], a[132:135], v[124:127], v[232:235]\n\t"
    "s_waitcnt lgkmcnt(5)\n\t"
    "v_exp_f32 v42, v42\n\t"
    "v_mfma_f32_16x16x32_bf16 v[232:235], a[136:139], v[136:139], v[232:235]\n\t"
    "s_waitcnt lgkmcnt(4)\n\t"
    "ds_read_b128 a[108:111], v15 offset:32768\n\t"
    "v_exp_f32 v43, v43\n\t"
    "v_mfma_f32_16x16x32_bf16 v[232:235], a[140:143], v[148:151], v[232:235]\n\t"
    "ds_read_b64_tr_b16 a[144:145], v19 offset:49152\n\t"
    "v_cvt_pk_bf16_f32 v98, v40, v41\n\t"
    "v_mfma_f32_16x16x32_bf16 v[60:63], a[112:115], a[16:19], v[4:7]\n\t"
    "buffer_load_dwordx4 v27, s[44:47], s79 offen offset:3072 nt lds\n\t"
    "v_cvt_pk_bf16_f32 v99, v42, v43\n\t"
    "v_mfma_f32_16x16x32_bf16 v[60:63], a[116:119], a[20:23], v[60:63]\n\t"
    "ds_read_b64_tr_b16 a[146:147], v19 offset:51200\n\t"
    "v_mul_f32 v167, v36, v167\n\t"
    "v_mfma_f32_16x16x32_bf16 v[60:63], a[120:123], a[24:27], v[60:63]\n\t"
    "v_exp_f32 v44, v44\n\t"
    "v_cvt_pk_bf16_f32 v164, v164, v165\n\t"
    "v_mfma_f32_16x16x32_bf16 v[60:63], a[124:127], a[28:31], v[60:63]\n\t"
    "ds_read_b64_tr_b16 a[148:149], v19 offset:53248\n\t"
    "v_exp_f32 v45, v45\n\t"
    "v_mfma_f32_16x16x32_bf16 v[236:239], a[128:131], v[116:119], v[236:239]\n\t"
    "ds_write_b64 v32, v[184:185] offset:4160\n\t"
    "v_mfma_f32_16x16x32_bf16 v[236:239], a[132:135], v[128:131], v[236:239]\n\t"
    "v_exp_f32 v46, v46\n\t"
    "v_mfma_f32_16x16x32_bf16 v[236:239], a[136:139], v[140:143], v[236:239]\n\t"
    "ds_read_b64_tr_b16 a[150:151], v19 offset:55296\n\t"
    "v_exp_f32 v47, v47\n\t"
    "v_mfma_f32_16x16x32_bf16 v[236:239], a[140:143], v[152:155], v[236:239]\n\t"
    "ds_read_b64_tr_b16 a[152:153], v19 offset:57344\n\t"
    "v_cvt_pk_bf16_f32 v100, v44, v45\n\t"
    "v_mfma_f32_16x16x32_bf16 v[40:43], a[112:115], a[32:35], v[8:11]\n\t"
    "v_cvt_pk_bf16_f32 v101, v46, v47\n\t"
    "v_exp_f32 v48, v48\n\t"
    "v_mfma_f32_16x16x32_bf16 v[40:43], a[116:119], a[36:39], v[40:43]\n\t"
    "ds_read_b64_tr_b16 a[154:155], v19 offset:59392\n\t"
    "v_exp_f32 v49, v49\n\t"
    "v_mfma_f32_16x16x32_bf16 v[40:43], a[120:123], a[40:43], v[40:43]\n\t"
    "v_exp_f32 v50, v50\n\t"
    "v_mfma_f32_16x16x32_bf16 v[40:43], a[124:127], a[44:47], v[40:43]\n\t"
    "ds_read_b64_tr_b16 a[156:157], v19 offset:61440\n\t"
    "v_exp_f32 v51, v51\n\t"
    "v_mfma_f32_16x16x32_bf16 v[240:243], a[128:131], v[120:123], v[240:243]\n\t"
    "ds_write_b64 v32, v[196:197] offset:4192\n\t"
    "v_mfma_f32_16x16x32_bf16 v[240:243], a[132:135], v[132:135], v[240:243]\n\t"
    "ds_read_b64_tr_b16 a[158:159], v19 offset:63488\n\t"
    "v_cvt_pk_bf16_f32 v104, v48, v49\n\t"
    "v_mfma_f32_16x16x32_bf16 v[240:243], a[136:139], v[144:147], v[240:243]\n\t"
    "ds_read_b128 a[176:179], v34 offset:4096\n\t"
    "v_cvt_pk_bf16_f32 v105, v50, v51\n\t"
    "v_cvt_pk_bf16_f32 v165, v166, v167\n\t"
    "v_mfma_f32_16x16x32_bf16 v[240:243], a[140:143], v[156:159], v[240:243]\n\t"
    "s_waitcnt lgkmcnt(5)\n\t"
    "ds_read_b128 a[180:183], v34 offset:5248\n\t"
    "v_exp_f32 v52, v52\n\t"
    "v_mfma_f32_16x16x32_bf16 v[44:47], a[96:99], a[0:3], v[0:3]\n\t"
    "v_exp_f32 v53, v53\n\t"
    "v_mfma_f32_16x16x32_bf16 v[44:47], a[100:103], a[4:7], v[44:47]\n\t"
    "v_exp_f32 v54, v54\n\t"
    "v_cvt_pk_bf16_f32 v108, v52, v53\n\t"
    "v_mfma_f32_16x16x32_bf16 v[44:47], a[104:107], a[8:11], v[44:47]\n\t"
    "v_exp_f32 v55, v55\n\t"
    "v_mul_f32 v176, v36, v176\n\t"
    "v_mfma_f32_16x16x32_bf16 v[44:47], a[108:111], a[12:15], v[44:47]\n\t"
    "v_cvt_pk_bf16_f32 v109, v54, v55\n\t"
    "v_mul_f32 v177, v36, v177\n\t"
    "v_mul_f32 v178, v36, v178\n\t"
    "v_mfma_f32_16x16x32_bf16 v[244:247], a[144:147], v[112:115], v[244:247]\n\t"
    "ds_write_b64 v32, v[164:165] offset:4096\n\t"
    "v_mfma_f32_16x16x32_bf16 v[244:247], a[148:151], v[124:127], v[244:247]\n\t"
    "s_waitcnt lgkmcnt(3)\n\t"
    "v_mul_f32 v179, v36, v179\n\t"
    "v_cvt_pk_bf16_f32 v176, v176, v177\n\t"
    "v_mfma_f32_16x16x32_bf16 v[244:247], a[152:155], v[136:139], v[244:247]\n\t"
    "s_waitcnt vmcnt(18) lgkmcnt(3)\n\t"
    "v_cvt_pk_bf16_f32 v177, v178, v179\n\t"
    "v_exp_f32 v56, v56\n\t"
    "v_mfma_f32_16x16x32_bf16 v[244:247], a[156:159], v[148:151], v[244:247]\n\t"
    "s_barrier\n\t"
    "ds_read_b128 a[112:115], v12 offset:36864\n\t"
    "v_mul_f32 v188, v36, v188\n\t"
    "v_mfma_f32_16x16x32_bf16 v[48:51], a[96:99], a[16:19], v[4:7]\n\t"
    "s_add_u32 s78, s57, 86016\n\t"
    "ds_read_b128 a[116:119], v13 offset:36864\n\t"
    "v_exp_f32 v57, v57\n\t"
    "v_mfma_f32_16x16x32_bf16 v[48:51], a[100:103], a[20:23], v[48:51]\n\t"
    "s_mov_b32 m0, s74\n\t"
    "ds_read_b128 a[120:123], v14 offset:36864\n\t"
    "v_exp_f32 v58, v58\n\t"
    "v_mfma_f32_16x16x32_bf16 v[48:51], a[104:107], a[24:27], v[48:51]\n\t"
    "buffer_load_dwordx4 v20, s[40:43], s78 offen offset:0 nt lds\n\t"
    "v_mfma_f32_16x16x32_bf16 v[48:51], a[108:111], a[28:31], v[48:51]\n\t"
    "ds_read_b128 a[124:127], v15 offset:36864\n\t"
    "v_exp_f32 v59, v59\n\t"
    "v_mfma_f32_16x16x32_bf16 v[248:251], a[144:147], v[116:119], v[248:251]\n\t"
    "ds_read_b64_tr_b16 a[128:129], v16 offset:0\n\t"
    "v_cvt_pk_bf16_f32 v102, v56, v57\n\t"
    "v_cvt_pk_bf16_f32 v103, v58, v59\n\t"
    "v_mfma_f32_16x16x32_bf16 v[248:251], a[148:151], v[128:131], v[248:251]\n\t"
    "ds_read_b64_tr_b16 a[130:131], v16 offset:2048\n\t"
    "v_mul_f32 v189, v36, v189\n\t"
    "v_mul_f32 v190, v36, v190\n\t"
    "v_mfma_f32_16x16x32_bf16 v[248:251], a[152:155], v[140:143], v[248:251]\n\t"
    "ds_write_b64 v32, v[176:177] offset:4128\n\t"
    "v_mfma_f32_16x16x32_bf16 v[248:251], a[156:159], v[152:155], v[248:251]\n\t"
    "v_mul_f32 v191, v36, v191\n\t"
    "v_cvt_pk_bf16_f32 v188, v188, v189\n\t"
    "v_mfma_f32_16x16x32_bf16 v[52:55], a[96:99], a[32:35], v[8:11]\n\t"
    "ds_read_b64_tr_b16 a[132:133], v16 offset:4096\n\t"
    "v_cvt_pk_bf16_f32 v189, v190, v191\n\t"
    "v_mul_f32 v200, v36, v200\n\t"
    "v_mfma_f32_16x16x32_bf16 v[52:55], a[100:103], a[36:39], v[52:55]\n\t"
    "ds_read_b64_tr_b16 a[134:135], v16 offset:6144\n\t"
    "v_mul_f32 v201, v36, v201\n\t"
    "v_mfma_f32_16x16x32_bf16 v[52:55], a[104:107], a[40:43], v[52:55]\n\t"
    "ds_read_b64_tr_b16 a[136:137], v16 offset:8192\n\t"
    "v_exp_f32 v60, v60\n\t"
    "v_mfma_f32_16x16x32_bf16 v[52:55], a[108:111], a[44:47], v[52:55]\n\t"
    "ds_read_b64_tr_b16 a[138:139], v16 offset:10240\n\t"
    "v_exp_f32 v61, v61\n\t"
    "v_mfma_f32_16x16x32_bf16 v[252:255], a[144:147], v[120:123], v[252:255]\n\t"
    "ds_read_b64_tr_b16 a[140:141], v16 offset:12288\n\t"
    "v_exp_f32 v62, v62\n\t"
    "v_mfma_f32_16x16x32_bf16 v[252:255], a[148:151], v[132:135], v[252:255]\n\t"
    "buffer_load_dwordx4 v21, s[40:43], s78 offen offset:1024 nt lds\n\t"
    "v_mfma_f32_16x16x32_bf16 v[252:255], a[152:155], v[144:147], v[252:255]\n\t"
    "ds_read_b64_tr_b16 a[142:143], v16 offset:14336\n\t"
    "v_exp_f32 v63, v63\n\t"
    "v_mfma_f32_16x16x32_bf16 v[252:255], a[156:159], v[156:159], v[252:255]\n\t"
    "s_waitcnt lgkmcnt(6)\n\t"
    "v_cvt_pk_bf16_f32 v106, v60, v61\n\t"
    "v_cvt_pk_bf16_f32 v107, v62, v63\n\t"
    "v_mul_f32 v202, v36, v202\n\t"
    "v_mfma_f32_16x16x32_bf16 v[56:59], a[112:115], a[0:3], v[0:3]\n\t"
    "ds_read_b128 a[96:99], v12 offset:40960\n\t"
    "v_mul_f32 v203, v36, v203\n\t"
    "v_cvt_pk_bf16_f32 v200, v200, v201\n\t"
    "v_mfma_f32_16x16x32_bf16 v[56:59], a[116:119], a[4:7], v[56:59]\n\t"
    "ds_write_b64 v32, v[188:189] offset:4160\n\t"
    "v_mfma_f32_16x16x32_bf16 v[56:59], a[120:123], a[8:11], v[56:59]\n\t"
    "v_cvt_pk_bf16_f32 v201, v202, v203\n\t"
    "v_mul_f32 v168, v37, v168\n\t"
    "v_mfma_f32_16x16x32_bf16 v[56:59], a[124:127], a[12:15], v[56:59]\n\t"
    "ds_read_b128 a[100:103], v13 offset:40960\n\t"
    "v_exp_f32 v40, v40\n\t"
    "v_mfma_f32_16x16x32_bf16 v[160:163], a[128:131], v[64:67], 0\n\t"
    "s_waitcnt lgkmcnt(4)\n\t"
    "ds_read_b128 a[104:107], v14 offset:40960\n\t"
    "v_exp_f32 v41, v41\n\t"
    "v_mfma_f32_16x16x32_bf16 v[160:163], a[132:135], v[76:79], v[160:163]\n\t"
    "v_exp_f32 v42, v42\n\t"
    "v_mfma_f32_16x16x32_bf16 v[160:163], a[136:139], v[88:91], v[160:163]\n\t"
    "s_waitcnt lgkmcnt(4)\n\t"
    "ds_read_b128 a[108:111], v15 offset:40960\n\t"
    "v_exp_f32 v43, v43\n\t"
    "v_mfma_f32_16x16x32_bf16 v[160:163], a[140:143], v[100:103], v[160:163]\n\t"
    "buffer_load_dwordx4 v22, s[40:43], s78 offen offset:2048 nt lds\n\t"
    "v_cvt_pk_bf16_f32 v110, v40, v41\n\t"
    "v_mfma_f32_16x16x32_bf16 v[60:63], a[112:115], a[16:19], v[4:7]\n\t"
    "ds_read_b64_tr_b16 a[144:145], v17 offset:0\n\t"
    "v_cvt_pk_bf16_f32 v111, v42, v43\n\t"
    "v_mul_f32 v169, v37, v169\n\t"
    "v_mfma_f32_16x16x32_bf16 v[60:63], a[116:119], a[20:23], v[60:63]\n\t"
    "v_mul_f32 v170, v37, v170\n\t"
    "v_mul_f32 v171, v37, v171\n\t"
    "v_cvt_pk_bf16_f32 v168, v168, v169\n\t"
    "v_mfma_f32_16x16x32_bf16 v[60:63], a[120:123], a[24:27], v[60:63]\n\t"
    "ds_write_b64 v32, v[200:201] offset:4192\n\t"
    "v_mfma_f32_16x16x32_bf16 v[60:63], a[124:127], a[28:31], v[60:63]\n\t"
    "v_cvt_pk_bf16_f32 v169, v170, v171\n\t"
    "v_mul_f32 v180, v37, v180\n\t"
    "v_mfma_f32_16x16x32_bf16 v[164:167], a[128:131], v[68:71], 0\n\t"
    "ds_read_b64_tr_b16 a[146:147], v17 offset:2048\n\t"
    "v_exp_f32 v44, v44\n\t"
    "v_mfma_f32_16x16x32_bf16 v[164:167], a[132:135], v[80:83], v[164:167]\n\t"
    "ds_read_b64_tr_b16 a[148:149], v17 offset:4096\n\t"
    "v_exp_f32 v45, v45\n\t"
    "v_mfma_f32_16x16x32_bf16 v[164:167], a[136:139], v[92:95], v[164:167]\n\t"
    "ds_read_b64_tr_b16 a[150:151], v17 offset:6144\n\t"
    "v_exp_f32 v46, v46\n\t"
    "v_mfma_f32_16x16x32_bf16 v[164:167], a[140:143], v[104:107], v[164:167]\n\t"
    "ds_read_b128 a[184:187], v34 offset:4096\n\t"
    "v_exp_f32 v47, v47\n\t"
    "v_mfma_f32_16x16x32_bf16 v[40:43], a[112:115], a[32:35], v[8:11]\n\t"
    "ds_read_b128 a[188:191], v34 offset:5248\n\t"
    "v_cvt_pk_bf16_f32 v112, v44, v45\n\t"
    "v_cvt_pk_bf16_f32 v113, v46, v47\n\t"
    "v_mfma_f32_16x16x32_bf16 v[40:43], a[116:119], a[36:39], v[40:43]\n\t"
    "ds_read_b64_tr_b16 a[152:153], v17 offset:8192\n\t"
    "v_exp_f32 v48, v48\n\t"
    "v_mfma_f32_16x16x32_bf16 v[40:43], a[120:123], a[40:43], v[40:43]\n\t"
    "ds_write_b64 v32, v[168:169] offset:4096\n\t"
    "v_mfma_f32_16x16x32_bf16 v[40:43], a[124:127], a[44:47], v[40:43]\n\t"
    "v_exp_f32 v49, v49\n\t"
    "v_mfma_f32_16x16x32_bf16 v[168:171], a[128:131], v[72:75], 0\n\t"
    "ds_read_b64_tr_b16 a[154:155], v17 offset:10240\n\t"
    "v_exp_f32 v50, v50\n\t"
    "v_mfma_f32_16x16x32_bf16 v[168:171], a[132:135], v[84:87], v[168:171]\n\t"
    "ds_read_b64_tr_b16 a[156:157], v17 offset:12288\n\t"
    "v_exp_f32 v51, v51\n\t"
    "v_mfma_f32_16x16x32_bf16 v[168:171], a[136:139], v[96:99], v[168:171]\n\t"
    "ds_read_b64_tr_b16 a[158:159], v17 offset:14336\n\t"
    "v_cvt_pk_bf16_f32 v116, v48, v49\n\t"
    "v_cvt_pk_bf16_f32 v117, v50, v51\n\t"
    "v_mfma_f32_16x16x32_bf16 v[168:171], a[140:143], v[108:111], v[168:171]\n\t"
    "s_waitcnt lgkmcnt(6)\n\t"
    "buffer_load_dwordx4 v23, s[40:43], s78 offen offset:3072 nt lds\n\t"
    "v_mul_f32 v181, v37, v181\n\t"
    "v_mfma_f32_16x16x32_bf16 v[44:47], a[96:99], a[0:3], v[0:3]\n\t"
    "s_add_u32 s79, s63, 32896\n\t"
    "ds_read_b128 a[112:115], v12 offset:45056\n\t"
    "v_mul_f32 v182, v37, v182\n\t"
    "v_mul_f32 v183, v37, v183\n\t"
    "v_mfma_f32_16x16x32_bf16 v[44:47], a[100:103], a[4:7], v[44:47]\n\t"
    "s_add_u32 m0, s77, 49152\n\t"
    "v_cvt_pk_bf16_f32 v180, v180, v181\n\t"
    "v_cvt_pk_bf16_f32 v181, v182, v183\n\t"
    "v_mul_f32 v192, v37, v192\n\t"
    "v_mfma_f32_16x16x32_bf16 v[44:47], a[104:107], a[8:11], v[44:47]\n\t"
    "ds_read_b128 a[116:119], v13 offset:45056\n\t"
    "v_mul_f32 v193, v37, v193\n\t"
    "v_mul_f32 v194, v37, v194\n\t"
    "v_mfma_f32_16x16x32_bf16 v[44:47], a[108:111], a[12:15], v[44:47]\n\t"
    "buffer_load_dwordx4 v24, s[44:47], s79 offen offset:0 nt lds\n\t"
    "v_mul_f32 v195, v37, v195\n\t"
    "v_mfma_f32_16x16x32_bf16 v[172:175], a[144:147], v[64:67], 0\n\t"
    "ds_read_b128 a[120:123], v14 offset:45056\n\t"
    "v_cvt_pk_bf16_f32 v192, v192, v193\n\t"
    "v_mfma_f32_16x16x32_bf16 v[172:175], a[148:151], v[76:79], v[172:175]\n\t"
    "s_waitcnt lgkmcnt(4)\n\t"
    "v_cvt_pk_bf16_f32 v193, v194, v195\n\t"
    "v_exp_f32 v52, v52\n\t"
    "v_mfma_f32_16x16x32_bf16 v[172:175], a[152:155], v[88:91], v[172:175]\n\t"
    "s_waitcnt lgkmcnt(3)\n\t"
    "v_exp_f32 v53, v53\n\t"
    "v_mul_f32 v204, v37, v204\n\t"
    "v_mfma_f32_16x16x32_bf16 v[172:175], a[156:159], v[100:103], v[172:175]\n\t"
    "ds_read_b128 a[124:127], v15 offset:45056\n\t"
    "v_exp_f32 v54, v54\n\t"
    "v_mfma_f32_16x16x32_bf16 v[48:51], a[96:99], a[16:19], v[4:7]\n\t"
    "ds_write_b64 v32, v[180:181] offset:4128\n\t"
    "v_mfma_f32_16x16x32_bf16 v[48:51], a[100:103], a[20:23], v[48:51]\n\t"
    "v_exp_f32 v55, v55\n\t"
    "v_cvt_pk_bf16_f32 v120, v52, v53\n\t"
    "v_mfma_f32_16x16x32_bf16 v[48:51], a[104:107], a[24:27], v[48:51]\n\t"
    "ds_read_b64_tr_b16 a[128:129], v18 offset:0\n\t"
    "v_cvt_pk_bf16_f32 v121, v54, v55\n\t"
    "v_mul_f32 v205, v37, v205\n\t"
    "v_mfma_f32_16x16x32_bf16 v[48:51], a[108:111], a[28:31], v[48:51]\n\t"
    "ds_read_b64_tr_b16 a[130:131], v18 offset:2048\n\t"
    "v_mul_f32 v206, v37, v206\n\t"
    "v_mul_f32 v207, v37, v207\n\t"
    "v_mfma_f32_16x16x32_bf16 v[176:179], a[144:147], v[68:71], 0\n\t"
    "buffer_load_dwordx4 v25, s[44:47], s79 offen offset:1024 nt lds\n\t"
    "v_cvt_pk_bf16_f32 v204, v204, v205\n\t"
    "v_mfma_f32_16x16x32_bf16 v[176:179], a[148:151], v[80:83], v[176:179]\n\t"
    "v_cvt_pk_bf16_f32 v205, v206, v207\n\t"
    "v_exp_f32 v56, v56\n\t"
    "v_mfma_f32_16x16x32_bf16 v[176:179], a[152:155], v[92:95], v[176:179]\n\t"
    "ds_read_b64_tr_b16 a[132:133], v18 offset:4096\n\t"
    "v_exp_f32 v57, v57\n\t"
    "v_mfma_f32_16x16x32_bf16 v[176:179], a[156:159], v[104:107], v[176:179]\n\t"
    "v_exp_f32 v58, v58\n\t"
    "v_cvt_pk_bf16_f32 v114, v56, v57\n\t"
    "v_mfma_f32_16x16x32_bf16 a[160:163], a[172:175], v[64:67], 0\n\t"
    "ds_read_b64_tr_b16 a[134:135], v18 offset:6144\n\t"
    "v_mul_f32 v208, v35, v208\n\t"
    "v_mfma_f32_16x16x32_bf16 a[160:163], a[172:175], v[76:79], a[160:163]\n\t"
    "ds_write_b64 v32, v[192:193] offset:4160\n\t"
    "v_mfma_f32_16x16x32_bf16 a[160:163], a[172:175], v[88:91], a[160:163]\n\t"
    "v_exp_f32 v59, v59\n\t"
    "v_mul_f32 v209, v35, v209\n\t"
    "v_mfma_f32_16x16x32_bf16 a[160:163], a[172:175], v[100:103], a[160:163]\n\t"
    "ds_read_b64_tr_b16 a[136:137], v18 offset:8192\n\t"
    "v_cvt_pk_bf16_f32 v115, v58, v59\n\t"
    "v_mul_f32 v210, v35, v210\n\t"
    "v_mfma_f32_16x16x32_bf16 v[52:55], a[96:99], a[32:35], v[8:11]\n\t"
    "v_mul_f32 v211, v35, v211\n\t"
    "v_cvt_pk_bf16_f32 v208, v208, v209\n\t"
    "v_mul_f32 v220, v35, v220\n\t"
    "v_mfma_f32_16x16x32_bf16 v[52:55], a[100:103], a[36:39], v[52:55]\n\t"
    "ds_read_b64_tr_b16 a[138:139], v18 offset:10240\n\t"
    "v_cvt_pk_bf16_f32 v209, v210, v211\n\t"
    "v_mul_f32 v221, v35, v221\n\t"
    "v_mfma_f32_16x16x32_bf16 v[52:55], a[104:107], a[40:43], v[52:55]\n\t"
    "buffer_load_dwordx4 v26, s[44:47], s79 offen offset:2048 nt lds\n\t"
    "v_mul_f32 v222, v35, v222\n\t"
    "v_mfma_f32_16x16x32_bf16 v[52:55], a[108:111], a[44:47], v[52:55]\n\t"
    "ds_read_b64_tr_b16 a[140:141], v18 offset:12288\n\t"
    "v_exp_f32 v60, v60\n\t"
    "v_mfma_f32_16x16x32_bf16 v[180:183], a[144:147], v[72:75], 0\n\t"
    "v_exp_f32 v61, v61\n\t"
    "v_mul_f32 v223, v35, v223\n\t"
    "v_mfma_f32_16x16x32_bf16 v[180:183], a[148:151], v[84:87], v[180:183]\n\t"
    "v_exp_f32 v62, v62\n\t"
    "v_cvt_pk_bf16_f32 v118, v60, v61\n\t"
    "v_mfma_f32_16x16x32_bf16 v[180:183], a[152:155], v[96:99], v[180:183]\n\t"
    "ds_read_b64_tr_b16 a[142:143], v18 offset:14336\n\t"
    "ds_write_b64 v32, v[204:205] offset:4192\n\t"
    "v_mfma_f32_16x16x32_bf16 v[180:183], a[156:159], v[108:111], v[180:183]\n\t"
    "s_waitcnt lgkmcnt(4)\n\t"
    "v_exp_f32 v63, v63\n\t"
    "v_cvt_pk_bf16_f32 v220, v220, v221\n\t"
    "v_mfma_f32_16x16x32_bf16 v[56:59], a[112:115], a[0:3], v[0:3]\n\t"
    "ds_read_b128 a[96:99], v12 offset:49152\n\t"
    "v_cvt_pk_bf16_f32 v119, v62, v63\n\t"
    "v_mfma_f32_16x16x32_bf16 v[56:59], a[116:119], a[4:7], v[56:59]\n\t"
    "ds_read_b128 a[100:103], v13 offset:49152\n\t"
    "v_cvt_pk_bf16_f32 v221, v222, v223\n\t"
    "v_mul_f32 v232, v35, v232\n\t"
    "v_mfma_f32_16x16x32_bf16 v[56:59], a[120:123], a[8:11], v[56:59]\n\t"
    "ds_read_b128 a[192:195], v34 offset:4096\n\t"
    "v_exp_f32 v40, v40\n\t"
    "v_mfma_f32_16x16x32_bf16 v[56:59], a[124:127], a[12:15], v[56:59]\n\t"
    "ds_read_b128 a[104:107], v14 offset:49152\n\t"
    "v_exp_f32 v41, v41\n\t"
    "v_mfma_f32_16x16x32_bf16 v[184:187], a[128:131], v[64:67], 0\n\t"
    "ds_read_b128 a[196:199], v34 offset:5248\n\t"
    "v_exp_f32 v42, v42\n\t"
    "v_mfma_f32_16x16x32_bf16 v[184:187], a[132:135], v[76:79], v[184:187]\n\t"
    "s_waitcnt lgkmcnt(7)\n\t"
    "ds_read_b128 a[108:111], v15 offset:49152\n\t"
    "v_exp_f32 v43, v43\n\t"
    "v_mfma_f32_16x16x32_bf16 v[184:187], a[136:139], v[88:91], v[184:187]\n\t"
    "s_waitcnt lgkmcnt(6)\n\t"
    "ds_write_b64 v32, v[208:209] offset:4096\n\t"
    "v_mfma_f32_16x16x32_bf16 v[184:187], a[140:143], v[100:103], v[184:187]\n\t"
    "v_cvt_pk_bf16_f32 v122, v40, v41\n\t"
    "v_cvt_pk_bf16_f32 v123, v42, v43\n\t"
    "v_mul_f32 v233, v35, v233\n\t"
    "v_mfma_f32_16x16x32_bf16 v[60:63], a[112:115], a[16:19], v[4:7]\n\t"
    "ds_read_b64_tr_b16 a[144:145], v19 offset:0\n\t"
    "v_mul_f32 v234, v35, v234\n\t"
    "v_mul_f32 v235, v35, v235\n\t"
    "v_mfma_f32_16x16x32_bf16 v[60:63], a[116:119], a[20:23], v[60:63]\n\t"
    "ds_read_b64_tr_b16 a[146:147], v19 offset:2048\n\t"
    "v_cvt_pk_bf16_f32 v232, v232, v233\n\t"
    "v_cvt_pk_bf16_f32 v233, v234, v235\n\t"
    "v_mfma_f32_16x16x32_bf16 v[60:63], a[120:123], a[24:27], v[60:63]\n\t"
    "buffer_load_dwordx4 v27, s[44:47], s79 offen offset:3072 nt lds\n\t"
    "v_mul_f32 v244, v35, v244\n\t"
    "v_mfma_f32_16x16x32_bf16 v[60:63], a[124:127], a[28:31], v[60:63]\n\t"
    "ds_read_b64_tr_b16 a[148:149], v19 offset:4096\n\t"
    "v_mul_f32 v245, v35, v245\n\t"
    "v_mfma_f32_16x16x32_bf16 v[188:191], a[128:131], v[68:71], 0\n\t"
    "v_exp_f32 v44, v44\n\t"
    "v_mul_f32 v246, v35, v246\n\t"
    "v_mfma_f32_16x16x32_bf16 v[188:191], a[132:135], v[80:83], v[188:191]\n\t"
    "ds_read_b64_tr_b16 a[150:151], v19 offset:6144\n\t"
    "v_exp_f32 v45, v45\n\t"
    "v_mfma_f32_16x16x32_bf16 v[188:191], a[136:139], v[92:95], v[188:191]\n\t"
    "ds_write_b64 v32, v[220:221] offset:4128\n\t"
    "v_mfma_f32_16x16x32_bf16 v[188:191], a[140:143], v[104:107], v[188:191]\n\t"
    "v_exp_f32 v46, v46\n\t"
    "v_cvt_pk_bf16_f32 v124, v44, v45\n\t"
    "v_mfma_f32_16x16x32_bf16 v[40:43], a[112:115], a[32:35], v[8:11]\n\t"
    "ds_read_b64_tr_b16 a[152:153], v19 offset:8192\n\t"
    "v_exp_f32 v47, v47\n\t"
    "v_mfma_f32_16x16x32_bf16 v[40:43], a[116:119], a[36:39], v[40:43]\n\t"
    "ds_read_b64_tr_b16 a[154:155], v19 offset:10240\n\t"
    "v_cvt_pk_bf16_f32 v125, v46, v47\n\t"
    "v_mul_f32 v247, v35, v247\n\t"
    "v_mfma_f32_16x16x32_bf16 v[40:43], a[120:123], a[40:43], v[40:43]\n\t"
    "v_cvt_pk_bf16_f32 v244, v244, v245\n\t"
    "v_cvt_pk_bf16_f32 v245, v246, v247\n\t"
    "v_mul_f32 v212, v36, v212\n\t"
    "v_mfma_f32_16x16x32_bf16 v[40:43], a[124:127], a[44:47], v[40:43]\n\t"
    "ds_read_b64_tr_b16 a[156:157], v19 offset:12288\n\t"
    "v_exp_f32 v48, v48\n\t"
    "v_mfma_f32_16x16x32_bf16 v[192:195], a[128:131], v[72:75], 0\n\t"
    "v_exp_f32 v49, v49\n\t"
    "v_mul_f32 v213, v36, v213\n\t"
    "v_mfma_f32_16x16x32_bf16 v[192:195], a[132:135], v[84:87], v[192:195]\n\t"
    "ds_read_b64_tr_b16 a[158:159], v19 offset:14336\n\t"
    "v_exp_f32 v50, v50\n\t"
    "v_mfma_f32_16x16x32_bf16 v[192:195], a[136:139], v[96:99], v[192:195]\n\t"
    "ds_write_b64 v32, v[232:233] offset:4160\n\t"
    "v_mfma_f32_16x16x32_bf16 v[192:195], a[140:143], v[108:111], v[192:195]\n\t"
    "s_waitcnt lgkmcnt(5)\n\t"
    "v_exp_f32 v51, v51\n\t"
    "v_mfma_f32_16x16x32_bf16 v[44:47], a[96:99], a[0:3], v[0:3]\n\t"
    "v_cvt_pk_bf16_f32 v128, v48, v49\n\t"
    "v_cvt_pk_bf16_f32 v129, v50, v51\n\t"
    "v_mul_f32 v214, v36, v214\n\t"
    "v_mfma_f32_16x16x32_bf16 v[44:47], a[100:103], a[4:7], v[44:47]\n\t"
    "v_mul_f32 v215, v36, v215\n\t"
    "v_cvt_pk_bf16_f32 v212, v212, v213\n\t"
    "v_mul_f32 v224, v36, v224\n\t"
    "v_mfma_f32_16x16x32_bf16 v[44:47], a[104:107], a[8:11], v[44:47]\n\t"
    "v_cvt_pk_bf16_f32 v213, v214, v215\n\t"
    "v_exp_f32 v52, v52\n\t"
    "v_mfma_f32_16x16x32_bf16 v[44:47], a[108:111], a[12:15], v[44:47]\n\t"
    "v_exp_f32 v53, v53\n\t"
    "v_mul_f32 v225, v36, v225\n\t"
    "v_mfma_f32_16x16x32_bf16 v[196:199], a[144:147], v[64:67], 0\n\t"
    "v_exp_f32 v54, v54\n\t"
    "v_cvt_pk_bf16_f32 v132, v52, v53\n\t"
    "v_mfma_f32_16x16x32_bf16 v[196:199], a[148:151], v[76:79], v[196:199]\n\t"
    "s_waitcnt lgkmcnt(1)\n\t"
    "v_exp_f32 v55, v55\n\t"
    "v_mul_f32 v226, v36, v226\n\t"
    "v_mfma_f32_16x16x32_bf16 v[196:199], a[152:155], v[88:91], v[196:199]\n\t"
    "s_waitcnt vmcnt(16) lgkmcnt(1)\n\t"
    "ds_write_b64 v32, v[244:245] offset:4192\n\t"
    "v_mfma_f32_16x16x32_bf16 v[196:199], a[156:159], v[100:103], v[196:199]\n\t"
    "s_barrier\n\t"
    "v_cvt_pk_bf16_f32 v133, v54, v55\n\t"
    "v_mul_f32 v227, v36, v227\n\t"
    "v_cvt_pk_bf16_f32 v224, v224, v225\n\t"
    "v_mfma_f32_16x16x32_bf16 v[48:51], a[96:99], a[16:19], v[4:7]\n\t"
    "s_add_u32 s78, s57, 102400\n\t"
    "ds_read_b128 a[112:115], v12 offset:53248\n\t"
    "v_cvt_pk_bf16_f32 v225, v226, v227\n\t"
    "v_mul_f32 v236, v36, v236\n\t"
    "v_mfma_f32_16x16x32_bf16 v[48:51], a[100:103], a[20:23], v[48:51]\n\t"
    "s_mov_b32 m0, s75\n\t"
    "ds_read_b128 a[116:119], v13 offset:53248\n\t"
    "v_mul_f32 v237, v36, v237\n\t"
    "v_mfma_f32_16x16x32_bf16 v[48:51], a[104:107], a[24:27], v[48:51]\n\t"
    "ds_read_b128 a[120:123], v14 offset:53248\n\t"
    "v_exp_f32 v56, v56\n\t"
    "v_mfma_f32_16x16x32_bf16 v[48:51], a[108:111], a[28:31], v[48:51]\n\t"
    "ds_read_b128 a[124:127], v15 offset:53248\n\t"
    "v_exp_f32 v57, v57\n\t"
    "v_mfma_f32_16x16x32_bf16 v[200:203], a[144:147], v[68:71], 0\n\t"
    "ds_read_b128 a[200:203], v34 offset:4096\n\t"
    "v_exp_f32 v58, v58\n\t"
    "v_mfma_f32_16x16x32_bf16 v[200:203], a[148:151], v[80:83], v[200:203]\n\t"
    "ds_read_b128 a[204:207], v34 offset:5248\n\t"
    "v_exp_f32 v59, v59\n\t"
    "v_mfma_f32_16x16x32_bf16 v[200:203], a[152:155], v[92:95], v[200:203]\n\t"
    "ds_read_b64_tr_b16 a[128:129], v16 offset:16384\n\t"
    "ds_write_b64 v32, v[212:213] offset:4096\n\t"
    "v_mfma_f32_16x16x32_bf16 v[200:203], a[156:159], v[104:107], v[200:203]\n\t"
    "ds_read_b64_tr_b16 a[130:131], v16 offset:18432\n\t"
    "v_cvt_pk_bf16_f32 v126, v56, v57\n\t"
    "v_cvt_pk_bf16_f32 v127, v58, v59\n\t"
    "v_mfma_f32_16x16x32_bf16 v[52:55], a[96:99], a[32:35], v[8:11]\n\t"
    "ds_read_b64_tr_b16 a[132:133], v16 offset:20480\n\t"
    "v_mul_f32 v238, v36, v238\n\t"
    "v_mul_f32 v239, v36, v239\n\t"
    "v_mfma_f32_16x16x32_bf16 v[52:55], a[100:103], a[36:39], v[52:55]\n\t"
    "ds_read_b64_tr_b16 a[134:135], v16 offset:22528\n\t"
    "v_cvt_pk_bf16_f32 v236, v236, v237\n\t"
    "v_cvt_pk_bf16_f32 v237, v238, v239\n\t"
    "v_mfma_f32_16x16x32_bf16 v[52:55], a[104:107], a[40:43], v[52:55]\n\t"
    "buffer_load_dwordx4 v20, s[40:43], s78 offen offset:0 nt lds\n\t"
    "v_mfma_f32_16x16x32_bf16 v[52:55], a[108:111], a[44:47], v[52:55]\n\t"
    "ds_read_b64_tr_b16 a[136:137], v16 offset:24576\n\t"
    "v_exp_f32 v60, v60\n\t"
    "v_mfma_f32_16x16x32_bf16 v[204:207], a[144:147], v[72:75], 0\n\t"
    "ds_read_b64_tr_b16 a[138:139], v16 offset:26624\n\t"
    "v_exp_f32 v61, v61\n\t"
    "v_mfma_f32_16x16x32_bf16 v[204:207], a[148:151], v[84:87], v[204:207]\n\t"
    "ds_read_b64_tr_b16 a[140:141], v16 offset:28672\n\t"
    "v_exp_f32 v62, v62\n\t"
    "v_mfma_f32_16x16x32_bf16 v[204:207], a[152:155], v[96:99], v[204:207]\n\t"
    "ds_read_b64_tr_b16 a[142:143], v16 offset:30720\n\t"
    "ds_write_b64 v32, v[224:225] offset:4128\n\t"
    "v_mfma_f32_16x16x32_bf16 v[204:207], a[156:159], v[108:111], v[204:207]\n\t"
    "s_waitcnt lgkmcnt(7)\n\t"
    "v_exp_f32 v63, v63\n\t"
    "v_cvt_pk_bf16_f32 v130, v60, v61\n\t"
    "v_mfma_f32_16x16x32_bf16 v[56:59], a[112:115], a[0:3], v[0:3]\n\t"
    "ds_read_b128 a[96:99], v12 offset:57344\n\t"
    "v_cvt_pk_bf16_f32 v131, v62, v63\n\t"
    "v_mul_f32 v248, v36, v248\n\t"
    "v_mfma_f32_16x16x32_bf16 v[56:59], a[116:119], a[4:7], v[56:59]\n\t"
    "v_mul_f32 v249, v36, v249\n\t"
    "v_mul_f32 v250, v36, v250\n\t"
    "v_mul_f32 v251, v36, v251\n\t"
    "v_mfma_f32_16x16x32_bf16 v[56:59], a[120:123], a[8:11], v[56:59]\n\t"
    "buffer_load_dwordx4 v21, s[40:43], s78 offen offset:1024 nt lds\n\t"
    "v_mfma_f32_16x16x32_bf16 v[56:59], a[124:127], a[12:15], v[56:59]\n\t"
    "ds_read_b128 a[100:103], v13 offset:57344\n\t"
    "v_cvt_pk_bf16_f32 v248, v248, v249\n\t"
    "v_cvt_pk_bf16_f32 v249, v250, v251\n\t"
    "v_mfma_f32_16x16x32_bf16 v[208:211], a[128:131], v[64:67], 0\n\t"
    "s_waitcnt lgkmcnt(5)\n\t"
    "ds_read_b128 a[104:107], v14 offset:57344\n\t"
    "v_exp_f32 v40, v40\n\t"
    "v_mfma_f32_16x16x32_bf16 v[208:211], a[132:135], v[76:79], v[208:211]\n\t"
    "buffer_store_dwordx4 a[176:179], v33, s[48:51], s67 offen offset:0 nt\n\t"
    "v_mfma_f32_16x16x32_bf16 v[208:211], a[136:139], v[88:91], v[208:211]\n\t"
    "s_waitcnt lgkmcnt(3)\n\t"
    "ds_write_b64 v32, v[236:237] offset:4160\n\t"
    "v_mfma_f32_16x16x32_bf16 v[208:211], a[140:143], v[100:103], v[208:211]\n\t"
    "v_exp_f32 v41, v41\n\t"
    "v_mfma_f32_16x16x32_bf16 v[60:63], a[112:115], a[16:19], v[4:7]\n\t"
    "buffer_store_dwordx4 a[180:183], v33, s[48:51], s67 offen offset:2048 nt\n\t"
    "v_mfma_f32_16x16x32_bf16 v[60:63], a[116:119], a[20:23], v[60:63]\n\t"
    "ds_read_b128 a[108:111], v15 offset:57344\n\t"
    "v_cvt_pk_bf16_f32 v134, v40, v41\n\t"
    "v_mfma_f32_16x16x32_bf16 v[60:63], a[120:123], a[24:27], v[60:63]\n\t"
    "ds_read_b64_tr_b16 a[144:145], v17 offset:16384\n\t"
    "v_exp_f32 v42, v42\n\t"
    "v_mfma_f32_16x16x32_bf16 v[60:63], a[124:127], a[28:31], v[60:63]\n\t"
    "ds_read_b64_tr_b16 a[146:147], v17 offset:18432\n\t"
    "v_exp_f32 v43, v43\n\t"
    "v_mfma_f32_16x16x32_bf16 v[212:215], a[128:131], v[68:71], 0\n\t"
    "buffer_load_dwordx4 v22, s[40:43], s78 offen offset:2048 nt lds\n\t"
    "v_cvt_pk_bf16_f32 v135, v42, v43\n\t"
    "v_mfma_f32_16x16x32_bf16 v[212:215], a[132:135], v[80:83], v[212:215]\n\t"
    "buffer_store_dwordx4 a[184:187], v33, s[48:51], s68 offen offset:0 nt\n\t"
    "v_mfma_f32_16x16x32_bf16 v[212:215], a[136:139], v[92:95], v[212:215]\n\t"
    "ds_write_b64 v32, v[248:249] offset:4192\n\t"
    "v_mfma_f32_16x16x32_bf16 v[212:215], a[140:143], v[104:107], v[212:215]\n\t"
    "v_mul_f32 v216, v37, v216\n\t"
    "v_mul_f32 v217, v37, v217\n\t"
    "v_mfma_f32_16x16x32_bf16 a[164:167], a[172:175], v[68:71], 0\n\t"
    "ds_read_b64_tr_b16 a[148:149], v17 offset:20480\n\t"
    "buffer_store_dwordx4 a[188:191], v33, s[48:51], s68 offen offset:2048 nt\n\t"
    "v_mfma_f32_16x16x32_bf16 a[164:167], a[172:175], v[80:83], a[164:167]\n\t"
    "ds_read_b64_tr_b16 a[150:151], v17 offset:22528\n\t"
    "v_mul_f32 v218, v37, v218\n\t"
    "v_mfma_f32_16x16x32_bf16 a[164:167], a[172:175], v[92:95], a[164:167]\n\t"
    "ds_read_b64_tr_b16 a[152:153], v17 offset:24576\n\t"
    "v_mul_f32 v219, v37, v219\n\t"
    "v_mfma_f32_16x16x32_bf16 a[164:167], a[172:175], v[104:107], a[164:167]\n\t"
    "ds_read_b128 a[208:211], v34 offset:4096\n\t"
    "v_cvt_pk_bf16_f32 v216, v216, v217\n\t"
    "v_cvt_pk_bf16_f32 v217, v218, v219\n\t"
    "v_mfma_f32_16x16x32_bf16 v[40:43], a[112:115], a[32:35], v[8:11]\n\t"
    "ds_read_b128 a[212:215], v34 offset:5248\n\t"
    "v_mul_f32 v228, v37, v228\n\t"
    "v_mfma_f32_16x16x32_bf16 v[40:43], a[116:119], a[36:39], v[40:43]\n\t"
    "ds_read_b64_tr_b16 a[154:155], v17 offset:26624\n\t"
    "v_exp_f32 v44, v44\n\t"
    "v_mfma_f32_16x16x32_bf16 v[40:43], a[120:123], a[40:43], v[40:43]\n\t"
    "ds_write_b64 v32, v[216:217] offset:4096\n\t"
    "v_mfma_f32_16x16x32_bf16 v[40:43], a[124:127], a[44:47], v[40:43]\n\t"
    "v_exp_f32 v45, v45\n\t"
    "v_mfma_f32_16x16x32_bf16 v[216:219], a[128:131], v[72:75], 0\n\t"
    "buffer_load_dwordx4 v23, s[40:43], s78 offen offset:3072 nt lds\n\t"
    "v_cvt_pk_bf16_f32 v136, v44, v45\n\t"
    "v_mfma_f32_16x16x32_bf16 v[216:219], a[132:135], v[84:87], v[216:219]\n\t"
    "s_add_u32 s79, s63, 65536\n\t"
    "ds_read_b64_tr_b16 a[156:157], v17 offset:28672\n\t"
    "v_exp_f32 v46, v46\n\t"
    "v_mfma_f32_16x16x32_bf16 v[216:219], a[136:139], v[96:99], v[216:219]\n\t"
    "s_add_u32 m0, s77, 0\n\t"
    "ds_read_b64_tr_b16 a[158:159], v17 offset:30720\n\t"
    "v_exp_f32 v47, v47\n\t"
    "v_mfma_f32_16x16x32_bf16 v[216:219], a[140:143], v[108:111], v[216:219]\n\t"
    "s_waitcnt lgkmcnt(5)\n\t"
    "v_cvt_pk_bf16_f32 v137, v46, v47\n\t"
    "v_mul_f32 v229, v37, v229\n\t"
    "v_mfma_f32_16x16x32_bf16 v[44:47], a[96:99], a[0:3], v[0:3]\n\t"
    "ds_read_b128 a[112:115], v12 offset:61440\n\t"
    "v_mul_f32 v230, v37, v230\n\t"
    "v_mfma_f32_16x16x32_bf16 v[44:47], a[100:103], a[4:7], v[44:47]\n\t"
    "buffer_load_dwordx4 v24, s[44:47], s79 offen offset:0 nt lds\n\t"
    "v_mfma_f32_16x16x32_bf16 v[44:47], a[104:107], a[8:11], v[44:47]\n\t"
    "ds_read_b128 a[116:119], v13 offset:61440\n\t"
    "v_exp_f32 v48, v48\n\t"
    "v_mfma_f32_16x16x32_bf16 v[44:47], a[108:111], a[12:15], v[44:47]\n\t"
    "v_exp_f32 v49, v49\n\t"
    "v_mfma_f32_16x16x32_bf16 v[220:223], a[144:147], v[64:67], 0\n\t"
    "ds_read_b128 a[120:123], v14 offset:61440\n\t"
    "v_cvt_pk_bf16_f32 v140, v48, v49\n\t"
    "v_mfma_f32_16x16x32_bf16 v[220:223], a[148:151], v[76:79], v[220:223]\n\t"
    "s_waitcnt lgkmcnt(4)\n\t"
    "v_exp_f32 v50, v50\n\t"
    "v_mfma_f32_16x16x32_bf16 v[220:223], a[152:155], v[88:91], v[220:223]\n\t"
    "s_waitcnt lgkmcnt(3)\n\t"
    "ds_read_b128 a[124:127], v15 offset:61440\n\t"
    "v_exp_f32 v51, v51\n\t"
    "v_mfma_f32_16x16x32_bf16 v[220:223], a[156:159], v[100:103], v[220:223]\n\t"
    "v_cvt_pk_bf16_f32 v141, v50, v51\n\t"
    "v_mul_f32 v231, v37, v231\n\t"
    "v_mfma_f32_16x16x32_bf16 v[48:51], a[96:99], a[16:19], v[4:7]\n\t"
    "ds_read_b64_tr_b16 a[128:129], v18 offset:16384\n\t"
    "v_cvt_pk_bf16_f32 v228, v228, v229\n\t"
    "v_mfma_f32_16x16x32_bf16 v[48:51], a[100:103], a[20:23], v[48:51]\n\t"
    "v_exp_f32 v52, v52\n\t"
    "v_mfma_f32_16x16x32_bf16 v[48:51], a[104:107], a[24:27], v[48:51]\n\t"
    "ds_read_b64_tr_b16 a[130:131], v18 offset:18432\n\t"
    "v_cvt_pk_bf16_f32 v229, v230, v231\n\t"
    "v_mfma_f32_16x16x32_bf16 v[48:51], a[108:111], a[28:31], v[48:51]\n\t"
    "v_exp_f32 v53, v53\n\t"
    "v_mul_f32 v240, v37, v240\n\t"
    "v_mfma_f32_16x16x32_bf16 v[224:227], a[144:147], v[68:71], 0\n\t"
    "ds_read_b64_tr_b16 a[132:133], v18 offset:20480\n\t"
    "v_cvt_pk_bf16_f32 v144, v52, v53\n\t"
    "v_mfma_f32_16x16x32_bf16 v[224:227], a[148:151], v[80:83], v[224:227]\n\t"
    "v_exp_f32 v54, v54\n\t"
    "v_mfma_f32_16x16x32_bf16 v[224:227], a[152:155], v[92:95], v[224:227]\n\t"
    "ds_read_b64_tr_b16 a[134:135], v18 offset:22528\n\t"
    "v_exp_f32 v55, v55\n\t"
    "v_mfma_f32_16x16x32_bf16 v[224:227], a[156:159], v[104:107], v[224:227]\n\t"
    "v_cvt_pk_bf16_f32 v145, v54, v55\n\t"
    "v_mul_f32 v241, v37, v241\n\t"
    "v_mfma_f32_16x16x32_bf16 v[52:55], a[96:99], a[32:35], v[8:11]\n\t"
    "buffer_load_dwordx4 v25, s[44:47], s79 offen offset:1024 nt lds\n\t"
    "v_mfma_f32_16x16x32_bf16 v[52:55], a[100:103], a[36:39], v[52:55]\n\t"
    "ds_read_b64_tr_b16 a[136:137], v18 offset:24576\n\t"
    "v_exp_f32 v56, v56\n\t"
    "v_mfma_f32_16x16x32_bf16 v[52:55], a[104:107], a[40:43], v[52:55]\n\t"
    "ds_write_b64 v32, v[228:229] offset:4128\n\t"
    "v_mfma_f32_16x16x32_bf16 v[52:55], a[108:111], a[44:47], v[52:55]\n\t"
    "v_exp_f32 v57, v57\n\t"
    "v_mfma_f32_16x16x32_bf16 v[228:231], a[144:147], v[72:75], 0\n\t"
    "ds_read_b64_tr_b16 a[138:139], v18 offset:26624\n\t"
    "v_cvt_pk_bf16_f32 v138, v56, v57\n\t"
    "v_mfma_f32_16x16x32_bf16 v[228:231], a[148:151], v[84:87], v[228:231]\n\t"
    "ds_read_b64_tr_b16 a[140:141], v18 offset:28672\n\t"
    "v_exp_f32 v58, v58\n\t"
    "v_mfma_f32_16x16x32_bf16 v[228:231], a[152:155], v[96:99], v[228:231]\n\t"
    "ds_read_b64_tr_b16 a[142:143], v18 offset:30720\n\t"
    "v_exp_f32 v59, v59\n\t"
    "v_mfma_f32_16x16x32_bf16 v[228:231], a[156:159], v[108:111], v[228:231]\n\t"
    "s_waitcnt lgkmcnt(5)\n\t"
    "v_cvt_pk_bf16_f32 v139, v58, v59\n\t"
    "v_mul_f32 v242, v37, v242\n\t"
    "v_mfma_f32_16x16x32_bf16 v[56:59], a[112:115], a[0:3], v[0:3]\n\t"
    "buffer_load_dwordx4 v26, s[44:47], s79 offen offset:2048 nt lds\n\t"
    "v_mul_f32 v243, v37, v243\n\t"
    "v_mfma_f32_16x16x32_bf16 v[56:59], a[116:119], a[4:7], v[56:59]\n\t"
    "ds_read_b128 a[96:99], v12 offset:0\n\t"
    "v_cvt_pk_bf16_f32 v240, v240, v241\n\t"
    "v_mfma_f32_16x16x32_bf16 v[56:59], a[120:123], a[8:11], v[56:59]\n\t"
    "ds_read_b128 a[100:103], v13 offset:0\n\t"
    "v_cvt_pk_bf16_f32 v241, v242, v243\n\t"
    "v_mfma_f32_16x16x32_bf16 v[56:59], a[124:127], a[12:15], v[56:59]\n\t"
    "v_exp_f32 v60, v60\n\t"
    "v_mfma_f32_16x16x32_bf16 v[232:235], a[128:131], v[64:67], 0\n\t"
    "ds_read_b128 a[104:107], v14 offset:0\n\t"
    "v_mul_f32 v252, v37, v252\n\t"
    "v_mfma_f32_16x16x32_bf16 v[232:235], a[132:135], v[76:79], v[232:235]\n\t"
    "s_waitcnt lgkmcnt(4)\n\t"
    "v_exp_f32 v61, v61\n\t"
    "v_mul_f32 v253, v37, v253\n\t"
    "v_mfma_f32_16x16x32_bf16 v[232:235], a[136:139], v[88:91], v[232:235]\n\t"
    "s_waitcnt lgkmcnt(3)\n\t"
    "v_exp_f32 v62, v62\n\t"
    "v_exp_f32 v63, v63\n\t"
    "v_mfma_f32_16x16x32_bf16 v[232:235], a[140:143], v[100:103], v[232:235]\n\t"
    "v_cvt_pk_bf16_f32 v142, v60, v61\n\t"
    "v_cvt_pk_bf16_f32 v143, v62, v63\n\t"
    "v_mfma_f32_16x16x32_bf16 v[60:63], a[112:115], a[16:19], v[4:7]\n\t"
    "ds_read_b128 a[108:111], v15 offset:0\n\t"
    "v_exp_f32 v40, v40\n\t"
    "v_mfma_f32_16x16x32_bf16 v[60:63], a[116:119], a[20:23], v[60:63]\n\t"
    "ds_read_b64_tr_b16 a[144:145], v19 offset:16384\n\t"
    "v_mul_f32 v254, v37, v254\n\t"
    "v_mfma_f32_16x16x32_bf16 v[60:63], a[120:123], a[24:27], v[60:63]\n\t"
    "buffer_load_dwordx4 v27, s[44:47], s79 offen offset:3072 nt lds\n\t"
    "v_mfma_f32_16x16x32_bf16 v[60:63], a[124:127], a[28:31], v[60:63]\n\t"
    "ds_read_b64_tr_b16 a[146:147], v19 offset:18432\n\t"
    "v_mul_f32 v255, v37, v255\n\t"
    "v_mfma_f32_16x16x32_bf16 v[236:239], a[128:131], v[68:71], 0\n\t"
    "buffer_store_dwordx4 a[192:195], v33, s[48:51], s69 offen offset:0 nt\n\t"
    "v_mfma_f32_16x16x32_bf16 v[236:239], a[132:135], v[80:83], v[236:239]\n\t"
    "ds_read_b64_tr_b16 a[148:149], v19 offset:20480\n\t"
    "v_cvt_pk_bf16_f32 v252, v252, v253\n\t"
    "v_mfma_f32_16x16x32_bf16 v[236:239], a[136:139], v[92:95], v[236:239]\n\t"
    "v_exp_f32 v41, v41\n\t"
    "v_mfma_f32_16x16x32_bf16 v[236:239], a[140:143], v[104:107], v[236:239]\n\t"
    "ds_read_b64_tr_b16 a[150:151], v19 offset:22528\n\t"
    "v_cvt_pk_bf16_f32 v146, v40, v41\n\t"
    "v_mfma_f32_16x16x32_bf16 a[168:171], a[172:175], v[72:75], 0\n\t"
    "v_exp_f32 v42, v42\n\t"
    "v_mfma_f32_16x16x32_bf16 a[168:171], a[172:175], v[84:87], a[168:171]\n\t"
    "v_exp_f32 v43, v43\n\t"
    "v_mfma_f32_16x16x32_bf16 a[168:171], a[172:175], v[96:99], a[168:171]\n\t"
    "ds_read_b64_tr_b16 a[152:153], v19 offset:24576\n\t"
    "v_cvt_pk_bf16_f32 v147, v42, v43\n\t"
    "v_mfma_f32_16x16x32_bf16 a[168:171], a[172:175], v[108:111], a[168:171]\n\t"
    "ds_write_b64 v32, v[240:241] offset:4160\n\t"
    "v_mfma_f32_16x16x32_bf16 v[40:43], a[112:115], a[32:35], v[8:11]\n\t"
    "v_exp_f32 v44, v44\n\t"
    "v_mfma_f32_16x16x32_bf16 v[40:43], a[116:119], a[36:39], v[40:43]\n\t"
    "ds_read_b64_tr_b16 a[154:155], v19 offset:26624\n\t"
    "v_cvt_pk_bf16_f32 v253, v254, v255\n\t"
    "v_mfma_f32_16x16x32_bf16 v[40:43], a[120:123], a[40:43], v[40:43]\n\t"
    "ds_read_b64_tr_b16 a[156:157], v19 offset:28672\n\t"
    "ds_read_b64_tr_b16 a[158:159], v19 offset:30720\n\t"
    "v_mfma_f32_16x16x32_bf16 v[40:43], a[124:127], a[44:47], v[40:43]\n\t"
    "v_exp_f32 v45, v45\n\t"
    "v_mfma_f32_16x16x32_bf16 v[240:243], a[128:131], v[72:75], 0\n\t"
    "v_exp_f32 v46, v46\n\t"
    "v_mfma_f32_16x16x32_bf16 v[240:243], a[132:135], v[84:87], v[240:243]\n\t"
    "v_exp_f32 v47, v47\n\t"
    "v_mfma_f32_16x16x32_bf16 v[240:243], a[136:139], v[96:99], v[240:243]\n\t"
    "v_cvt_pk_bf16_f32 v148, v44, v45\n\t"
    "v_cvt_pk_bf16_f32 v149, v46, v47\n\t"
    "v_mfma_f32_16x16x32_bf16 v[240:243], a[140:143], v[108:111], v[240:243]\n\t"
    "s_waitcnt lgkmcnt(3)\n\t"
    "v_exp_f32 v48, v48\n\t"
    "v_mfma_f32_16x16x32_bf16 v[44:47], a[96:99], a[0:3], v[0:3]\n\t"
    "v_exp_f32 v49, v49\n\t"
    "v_mfma_f32_16x16x32_bf16 v[44:47], a[100:103], a[4:7], v[44:47]\n\t"
    "v_exp_f32 v50, v50\n\t"
    "v_mfma_f32_16x16x32_bf16 v[44:47], a[104:107], a[8:11], v[44:47]\n\t"
    "v_exp_f32 v51, v51\n\t"
    "v_mfma_f32_16x16x32_bf16 v[44:47], a[108:111], a[12:15], v[44:47]\n\t"
    "v_cvt_pk_bf16_f32 v152, v48, v49\n\t"
    "v_cvt_pk_bf16_f32 v153, v50, v51\n\t"
    "v_mfma_f32_16x16x32_bf16 v[244:247], a[144:147], v[64:67], 0\n\t"
    "v_exp_f32 v52, v52\n\t"
    "v_mfma_f32_16x16x32_bf16 v[244:247], a[148:151], v[76:79], v[244:247]\n\t"
    "s_waitcnt lgkmcnt(0)\n\t"
    "v_exp_f32 v53, v53\n\t"
    "v_mfma_f32_16x16x32_bf16 v[244:247], a[152:155], v[88:91], v[244:247]\n\t"
    "s_waitcnt vmcnt(21) lgkmcnt(0)\n\t"
    "v_exp_f32 v54, v54\n\t"
    "v_mfma_f32_16x16x32_bf16 v[244:247], a[156:159], v[100:103], v[244:247]\n\t"
    "s_barrier\n\t"
    "ds_read_b128 a[112:115], v12 offset:4096\n\t"
    "v_cvt_pk_bf16_f32 v156, v52, v53\n\t"
    "v_mfma_f32_16x16x32_bf16 v[48:51], a[96:99], a[16:19], v[4:7]\n\t"
    "s_mov_b32 s78, s60\n\t"
    "ds_read_b128 a[116:119], v13 offset:4096\n\t"
    "v_mfma_f32_16x16x32_bf16 v[48:51], a[100:103], a[20:23], v[48:51]\n\t"
    "s_mov_b32 m0, s76\n\t"
    "ds_read_b128 a[120:123], v14 offset:4096\n\t"
    "v_mfma_f32_16x16x32_bf16 v[48:51], a[104:107], a[24:27], v[48:51]\n\t"
    "v_exp_f32 v55, v55\n\t"
    "v_mfma_f32_16x16x32_bf16 v[48:51], a[108:111], a[28:31], v[48:51]\n\t"
    "ds_read_b128 a[124:127], v15 offset:4096\n\t"
    "v_cvt_pk_bf16_f32 v157, v54, v55\n\t"
    "v_mfma_f32_16x16x32_bf16 v[248:251], a[144:147], v[68:71], 0\n\t"
    "ds_read_b64_tr_b16 a[128:129], v16 offset:32768\n\t"
    "ds_read_b64_tr_b16 a[130:131], v16 offset:34816\n\t"
    "v_mfma_f32_16x16x32_bf16 v[248:251], a[148:151], v[80:83], v[248:251]\n\t"
    "buffer_load_dwordx4 v20, s[40:43], s78 offen offset:0 nt lds\n\t"
    "v_mfma_f32_16x16x32_bf16 v[248:251], a[152:155], v[92:95], v[248:251]\n\t"
    "v_exp_f32 v56, v56\n\t"
    "v_mfma_f32_16x16x32_bf16 v[248:251], a[156:159], v[104:107], v[248:251]\n\t"
    "ds_read_b64_tr_b16 a[132:133], v16 offset:36864\n\t"
    "ds_read_b64_tr_b16 a[134:135], v16 offset:38912\n\t"
    "v_mfma_f32_16x16x32_bf16 v[52:55], a[96:99], a[32:35], v[8:11]\n\t"
    "v_exp_f32 v57, v57\n\t"
    "v_mfma_f32_16x16x32_bf16 v[52:55], a[100:103], a[36:39], v[52:55]\n\t"
    "ds_write_b64 v32, v[252:253] offset:4192\n\t"
    "v_mfma_f32_16x16x32_bf16 v[52:55], a[104:107], a[40:43], v[52:55]\n\t"
    "v_exp_f32 v58, v58\n\t"
    "v_mfma_f32_16x16x32_bf16 v[52:55], a[108:111], a[44:47], v[52:55]\n\t"
    "ds_read_b64_tr_b16 a[136:137], v16 offset:40960\n\t"
    "v_cvt_pk_bf16_f32 v150, v56, v57\n\t"
    "v_mfma_f32_16x16x32_bf16 v[252:255], a[144:147], v[72:75], 0\n\t"
    "ds_read_b128 a[216:219], v34 offset:4096\n\t"
    "ds_read_b128 a[220:223], v34 offset:5248\n\t"
    "v_mfma_f32_16x16x32_bf16 v[252:255], a[148:151], v[84:87], v[252:255]\n\t"
    "ds_read_b64_tr_b16 a[138:139], v16 offset:43008\n\t"
    "ds_read_b64_tr_b16 a[140:141], v16 offset:45056\n\t"
    "v_mfma_f32_16x16x32_bf16 v[252:255], a[152:155], v[96:99], v[252:255]\n\t"
    "ds_read_b64_tr_b16 a[142:143], v16 offset:47104\n\t"
    "v_exp_f32 v59, v59\n\t"
    "v_mfma_f32_16x16x32_bf16 v[252:255], a[156:159], v[108:111], v[252:255]\n\t"
    "s_waitcnt lgkmcnt(7)\n\t"
    "v_cvt_pk_bf16_f32 v151, v58, v59\n\t"
    "v_mfma_f32_16x16x32_bf16 v[56:59], a[112:115], a[0:3], v[0:3]\n\t"
    "ds_read_b128 a[96:99], v12 offset:8192\n\t"
    "v_mfma_f32_16x16x32_bf16 v[56:59], a[116:119], a[4:7], v[56:59]\n\t"
    "buffer_load_dwordx4 v21, s[40:43], s78 offen offset:1024 nt lds\n\t"
    "v_mfma_f32_16x16x32_bf16 v[56:59], a[120:123], a[8:11], v[56:59]\n\t"
    "ds_read_b128 a[100:103], v13 offset:8192\n\t"
    "v_mfma_f32_16x16x32_bf16 v[56:59], a[124:127], a[12:15], v[56:59]\n\t"
    "v_exp_f32 v60, v60\n\t"
    "v_mfma_f32_16x16x32_bf16 v[160:163], a[128:131], v[112:115], v[160:163]\n\t"
    "ds_read_b128 a[104:107], v14 offset:8192\n\t"
    "v_mfma_f32_16x16x32_bf16 v[160:163], a[132:135], v[124:127], v[160:163]\n\t"
    "s_waitcnt lgkmcnt(4)\n\t"
    "v_exp_f32 v61, v61\n\t"
    "v_mfma_f32_16x16x32_bf16 v[160:163], a[136:139], v[136:139], v[160:163]\n\t"
    "s_waitcnt lgkmcnt(3)\n\t"
    "v_exp_f32 v62, v62\n\t"
    "v_exp_f32 v63, v63\n\t"
    "v_mfma_f32_16x16x32_bf16 v[160:163], a[140:143], v[148:151], v[160:163]\n\t"
    "v_cvt_pk_bf16_f32 v154, v60, v61\n\t"
    "v_cvt_pk_bf16_f32 v155, v62, v63\n\t"
    "v_mfma_f32_16x16x32_bf16 v[60:63], a[112:115], a[16:19], v[4:7]\n\t"
    "ds_read_b128 a[108:111], v15 offset:8192\n\t"
    "ds_read_b64_tr_b16 a[144:145], v17 offset:32768\n\t"
    "v_mfma_f32_16x16x32_bf16 v[60:63], a[116:119], a[20:23], v[60:63]\n\t"
    "buffer_load_dwordx4 v22, s[40:43], s78 offen offset:2048 nt lds\n\t"
    "v_mfma_f32_16x16x32_bf16 v[60:63], a[120:123], a[24:27], v[60:63]\n\t"
    "ds_read_b64_tr_b16 a[146:147], v17 offset:34816\n\t"
    "ds_read_b64_tr_b16 a[148:149], v17 offset:36864\n\t"
    "v_mfma_f32_16x16x32_bf16 v[60:63], a[124:127], a[28:31], v[60:63]\n\t"
    "v_exp_f32 v40, v40\n\t"
    "v_mfma_f32_16x16x32_bf16 v[164:167], a[128:131], v[116:119], v[164:167]\n\t"
    "v_exp_f32 v41, v41\n\t"
    "v_mfma_f32_16x16x32_bf16 v[164:167], a[132:135], v[128:131], v[164:167]\n\t"
    "v_exp_f32 v42, v42\n\t"
    "v_mfma_f32_16x16x32_bf16 v[164:167], a[136:139], v[140:143], v[164:167]\n\t"
    "v_exp_f32 v43, v43\n\t"
    "v_mfma_f32_16x16x32_bf16 v[164:167], a[140:143], v[152:155], v[164:167]\n\t"
    "v_cvt_pk_bf16_f32 v158, v40, v41\n\t"
    "v_cvt_pk_bf16_f32 v159, v42, v43\n\t"
    "v_mfma_f32_16x16x32_bf16 v[40:43], a[112:115], a[32:35], v[8:11]\n\t"
    "ds_read_b64_tr_b16 a[150:151], v17 offset:38912\n\t"
    "ds_read_b64_tr_b16 a[152:153], v17 offset:40960\n\t"
    "v_mfma_f32_16x16x32_bf16 v[40:43], a[116:119], a[36:39], v[40:43]\n\t"
    "v_exp_f32 v44, v44\n\t"
    "v_mfma_f32_16x16x32_bf16 v[40:43], a[120:123], a[40:43], v[40:43]\n\t"
    "ds_read_b64_tr_b16 a[154:155], v17 offset:43008\n\t"
    "v_exp_f32 v45, v45\n\t"
    "v_mfma_f32_16x16x32_bf16 v[40:43], a[124:127], a[44:47], v[40:43]\n\t"
    "v_exp_f32 v46, v46\n\t"
    "v_mfma_f32_16x16x32_bf16 v[168:171], a[128:131], v[120:123], v[168:171]\n\t"
    "ds_read_b64_tr_b16 a[156:157], v17 offset:45056\n\t"
    "v_cvt_pk_bf16_f32 v64, v44, v45\n\t"
    "v_mfma_f32_16x16x32_bf16 v[168:171], a[132:135], v[132:135], v[168:171]\n\t"
    "v_exp_f32 v47, v47\n\t"
    "v_mfma_f32_16x16x32_bf16 v[168:171], a[136:139], v[144:147], v[168:171]\n\t"
    "ds_read_b64_tr_b16 a[158:159], v17 offset:47104\n\t"
    "v_cvt_pk_bf16_f32 v65, v46, v47\n\t"
    "v_mfma_f32_16x16x32_bf16 v[168:171], a[140:143], v[156:159], v[168:171]\n\t"
    "s_waitcnt lgkmcnt(5)\n\t"
    "buffer_load_dwordx4 v23, s[40:43], s78 offen offset:3072 nt lds\n\t"
    "v_mfma_f32_16x16x32_bf16 v[44:47], a[96:99], a[0:3], v[0:3]\n\t"
    "s_add_u32 s79, s63, 65664\n\t"
    "buffer_store_dwordx4 a[196:199], v33, s[48:51], s69 offen offset:2048 nt\n\t"
    "v_mfma_f32_16x16x32_bf16 v[44:47], a[100:103], a[4:7], v[44:47]\n\t"
    "s_add_u32 m0, s77, 16384\n\t"
    "ds_read_b128 a[112:115], v12 offset:12288\n\t"
    "v_mfma_f32_16x16x32_bf16 v[44:47], a[104:107], a[8:11], v[44:47]\n\t"
    "ds_read_b128 a[116:119], v13 offset:12288\n\t"
    "v_mfma_f32_16x16x32_bf16 v[44:47], a[108:111], a[12:15], v[44:47]\n\t"
    "v_exp_f32 v48, v48\n\t"
    "v_mfma_f32_16x16x32_bf16 v[172:175], a[144:147], v[112:115], v[172:175]\n\t"
    "s_waitcnt lgkmcnt(3)\n\t"
    "ds_read_b128 a[120:123], v14 offset:12288\n\t"
    "v_mfma_f32_16x16x32_bf16 v[172:175], a[148:151], v[124:127], v[172:175]\n\t"
    "v_exp_f32 v49, v49\n\t"
    "v_mfma_f32_16x16x32_bf16 v[172:175], a[152:155], v[136:139], v[172:175]\n\t"
    "s_waitcnt lgkmcnt(3)\n\t"
    "v_exp_f32 v50, v50\n\t"
    "v_exp_f32 v51, v51\n\t"
    "v_mfma_f32_16x16x32_bf16 v[172:175], a[156:159], v[148:151], v[172:175]\n\t"
    "v_cvt_pk_bf16_f32 v68, v48, v49\n\t"
    "v_cvt_pk_bf16_f32 v69, v50, v51\n\t"
    "v_mfma_f32_16x16x32_bf16 v[48:51], a[96:99], a[16:19], v[4:7]\n\t"
    "ds_read_b128 a[124:127], v15 offset:12288\n\t"
    "ds_read_b64_tr_b16 a[128:129], v18 offset:32768\n\t"
    "v_mfma_f32_16x16x32_bf16 v[48:51], a[100:103], a[20:23], v[48:51]\n\t"
    "buffer_load_dwordx4 v24, s[44:47], s79 offen offset:0 nt lds\n\t"
    "v_mfma_f32_16x16x32_bf16 v[48:51], a[104:107], a[24:27], v[48:51]\n\t"
    "v_exp_f32 v52, v52\n\t"
    "v_mfma_f32_16x16x32_bf16 v[48:51], a[108:111], a[28:31], v[48:51]\n\t"
    "ds_read_b64_tr_b16 a[130:131], v18 offset:34816\n\t"
    "ds_read_b64_tr_b16 a[132:133], v18 offset:36864\n\t"
    "v_mfma_f32_16x16x32_bf16 v[176:179], a[144:147], v[116:119], v[176:179]\n\t"
    "v_exp_f32 v53, v53\n\t"
    "v_mfma_f32_16x16x32_bf16 v[176:179], a[148:151], v[128:131], v[176:179]\n\t"
    "v_exp_f32 v54, v54\n\t"
    "v_mfma_f32_16x16x32_bf16 v[176:179], a[152:155], v[140:143], v[176:179]\n\t"
    "v_exp_f32 v55, v55\n\t"
    "v_mfma_f32_16x16x32_bf16 v[176:179], a[156:159], v[152:155], v[176:179]\n\t"
    "v_cvt_pk_bf16_f32 v72, v52, v53\n\t"
    "v_cvt_pk_bf16_f32 v73, v54, v55\n\t"
    "v_mfma_f32_16x16x32_bf16 a[160:163], a[172:175], v[112:115], a[160:163]\n\t"
    "ds_read_b64_tr_b16 a[134:135], v18 offset:38912\n\t"
    "ds_read_b64_tr_b16 a[136:137], v18 offset:40960\n\t"
    "v_mfma_f32_16x16x32_bf16 a[160:163], a[172:175], v[124:127], a[160:163]\n\t"
    "buffer_load_dwordx4 v25, s[44:47], s79 offen offset:1024 nt lds\n\t"
    "v_mfma_f32_16x16x32_bf16 a[160:163], a[172:175], v[136:139], a[160:163]\n\t"
    "v_exp_f32 v56, v56\n\t"
    "v_mfma_f32_16x16x32_bf16 a[160:163], a[172:175], v[148:151], a[160:163]\n\t"
    "v_exp_f32 v57, v57\n\t"
    "v_mfma_f32_16x16x32_bf16 v[52:55], a[96:99], a[32:35], v[8:11]\n\t"
    "v_exp_f32 v58, v58\n\t"
    "v_mfma_f32_16x16x32_bf16 v[52:55], a[100:103], a[36:39], v[52:55]\n\t"
    "ds_read_b64_tr_b16 a[138:139], v18 offset:43008\n\t"
    "v_cvt_pk_bf16_f32 v66, v56, v57\n\t"
    "v_mfma_f32_16x16x32_bf16 v[52:55], a[104:107], a[40:43], v[52:55]\n\t"
    "v_exp_f32 v59, v59\n\t"
    "v_mfma_f32_16x16x32_bf16 v[52:55], a[108:111], a[44:47], v[52:55]\n\t"
    "ds_read_b64_tr_b16 a[140:141], v18 offset:45056\n\t"
    "v_cvt_pk_bf16_f32 v67, v58, v59\n\t"
    "v_mfma_f32_16x16x32_bf16 v[180:183], a[144:147], v[120:123], v[180:183]\n\t"
    "buffer_load_dwordx4 v26, s[44:47], s79 offen offset:2048 nt lds\n\t"
    "v_mfma_f32_16x16x32_bf16 v[180:183], a[148:151], v[132:135], v[180:183]\n\t"
    "v_exp_f32 v60, v60\n\t"
    "v_mfma_f32_16x16x32_bf16 v[180:183], a[152:155], v[144:147], v[180:183]\n\t"
    "ds_read_b64_tr_b16 a[142:143], v18 offset:47104\n\t"
    "v_mfma_f32_16x16x32_bf16 v[180:183], a[156:159], v[156:159], v[180:183]\n\t"
    "s_waitcnt lgkmcnt(3)\n\t"
    "v_exp_f32 v61, v61\n\t"
    "v_mfma_f32_16x16x32_bf16 v[56:59], a[112:115], a[0:3], v[0:3]\n\t"
    "ds_read_b128 a[96:99], v12 offset:16384\n\t"
    "v_cvt_pk_bf16_f32 v70, v60, v61\n\t"
    "v_mfma_f32_16x16x32_bf16 v[56:59], a[116:119], a[4:7], v[56:59]\n\t"
    "ds_read_b128 a[100:103], v13 offset:16384\n\t"
    "v_mfma_f32_16x16x32_bf16 v[56:59], a[120:123], a[8:11], v[56:59]\n\t"
    "v_exp_f32 v62, v62\n\t"
    "v_mfma_f32_16x16x32_bf16 v[56:59], a[124:127], a[12:15], v[56:59]\n\t"
    "ds_read_b128 a[104:107], v14 offset:16384\n\t"
    "v_mfma_f32_16x16x32_bf16 v[184:187], a[128:131], v[112:115], v[184:187]\n\t"
    "v_exp_f32 v63, v63\n\t"
    "v_mfma_f32_16x16x32_bf16 v[184:187], a[132:135], v[124:127], v[184:187]\n\t"
    "s_waitcnt lgkmcnt(4)\n\t"
    "ds_read_b128 a[108:111], v15 offset:16384\n\t"
    "v_cvt_pk_bf16_f32 v71, v62, v63\n\t"
    "v_mfma_f32_16x16x32_bf16 v[184:187], a[136:139], v[136:139], v[184:187]\n\t"
    "s_waitcnt lgkmcnt(4)\n\t"
    "buffer_load_dwordx4 v27, s[44:47], s79 offen offset:3072 nt lds\n\t"
    "v_mfma_f32_16x16x32_bf16 v[184:187], a[140:143], v[148:151], v[184:187]\n\t"
    "ds_read_b64_tr_b16 a[144:145], v19 offset:32768\n\t"
    "ds_read_b64_tr_b16 a[146:147], v19 offset:34816\n\t"
    "v_mfma_f32_16x16x32_bf16 v[60:63], a[112:115], a[16:19], v[4:7]\n\t"
    "v_exp_f32 v40, v40\n\t"
    "v_mfma_f32_16x16x32_bf16 v[60:63], a[116:119], a[20:23], v[60:63]\n\t"
    "v_exp_f32 v41, v41\n\t"
    "v_mfma_f32_16x16x32_bf16 v[60:63], a[120:123], a[24:27], v[60:63]\n\t"
    "v_exp_f32 v42, v42\n\t"
    "v_mfma_f32_16x16x32_bf16 v[60:63], a[124:127], a[28:31], v[60:63]\n\t"
    "ds_read_b64_tr_b16 a[148:149], v19 offset:36864\n\t"
    "v_cvt_pk_bf16_f32 v74, v40, v41\n\t"
    "v_mfma_f32_16x16x32_bf16 v[188:191], a[128:131], v[116:119], v[188:191]\n\t"
    "v_exp_f32 v43, v43\n\t"
    "v_mfma_f32_16x16x32_bf16 v[188:191], a[132:135], v[128:131], v[188:191]\n\t"
    "ds_read_b64_tr_b16 a[150:151], v19 offset:38912\n\t"
    "v_cvt_pk_bf16_f32 v75, v42, v43\n\t"
    "v_mfma_f32_16x16x32_bf16 v[188:191], a[136:139], v[140:143], v[188:191]\n\t"
    "v_exp_f32 v44, v44\n\t"
    "v_mfma_f32_16x16x32_bf16 v[188:191], a[140:143], v[152:155], v[188:191]\n\t"
    "ds_read_b64_tr_b16 a[152:153], v19 offset:40960\n\t"
    "ds_read_b64_tr_b16 a[154:155], v19 offset:43008\n\t"
    "v_mfma_f32_16x16x32_bf16 v[40:43], a[112:115], a[32:35], v[8:11]\n\t"
    "v_exp_f32 v45, v45\n\t"
    "v_mfma_f32_16x16x32_bf16 v[40:43], a[116:119], a[36:39], v[40:43]\n\t"
    "v_exp_f32 v46, v46\n\t"
    "v_mfma_f32_16x16x32_bf16 v[40:43], a[120:123], a[40:43], v[40:43]\n\t"
    "v_exp_f32 v47, v47\n\t"
    "v_mfma_f32_16x16x32_bf16 v[40:43], a[124:127], a[44:47], v[40:43]\n\t"
    "ds_read_b64_tr_b16 a[156:157], v19 offset:45056\n\t"
    "v_cvt_pk_bf16_f32 v76, v44, v45\n\t"
    "v_mfma_f32_16x16x32_bf16 v[192:195], a[128:131], v[120:123], v[192:195]\n\t"
    "v_cvt_pk_bf16_f32 v77, v46, v47\n\t"
    "v_mfma_f32_16x16x32_bf16 v[192:195], a[132:135], v[132:135], v[192:195]\n\t"
    "ds_read_b64_tr_b16 a[158:159], v19 offset:47104\n\t"
    "v_mfma_f32_16x16x32_bf16 v[192:195], a[136:139], v[144:147], v[192:195]\n\t"
    "v_exp_f32 v48, v48\n\t"
    "v_mfma_f32_16x16x32_bf16 v[192:195], a[140:143], v[156:159], v[192:195]\n\t"
    "s_waitcnt lgkmcnt(2)\n\t"
    "v_exp_f32 v49, v49\n\t"
    "v_mfma_f32_16x16x32_bf16 v[44:47], a[96:99], a[0:3], v[0:3]\n\t"
    "v_exp_f32 v50, v50\n\t"
    "v_mfma_f32_16x16x32_bf16 v[44:47], a[100:103], a[4:7], v[44:47]\n\t"
    "v_exp_f32 v51, v51\n\t"
    "v_mfma_f32_16x16x32_bf16 v[44:47], a[104:107], a[8:11], v[44:47]\n\t"
    "v_cvt_pk_bf16_f32 v80, v48, v49\n\t"
    "v_cvt_pk_bf16_f32 v81, v50, v51\n\t"
    "v_mfma_f32_16x16x32_bf16 v[44:47], a[108:111], a[12:15], v[44:47]\n\t"
    "buffer_store_dwordx4 a[200:203], v33, s[48:51], s67 offen offset:128 nt\n\t"
    "v_mfma_f32_16x16x32_bf16 v[196:199], a[144:147], v[112:115], v[196:199]\n\t"
    "v_exp_f32 v52, v52\n\t"
    "v_mfma_f32_16x16x32_bf16 v[196:199], a[148:151], v[124:127], v[196:199]\n\t"
    "v_exp_f32 v53, v53\n\t"
    "v_mfma_f32_16x16x32_bf16 v[196:199], a[152:155], v[136:139], v[196:199]\n\t"
    "s_waitcnt vmcnt(23) lgkmcnt(0)\n\t"
    "v_exp_f32 v54, v54\n\t"
    "v_mfma_f32_16x16x32_bf16 v[196:199], a[156:159], v[148:151], v[196:199]\n\t"
    "s_barrier\n\t"
    "ds_read_b128 a[112:115], v12 offset:20480\n\t"
    "v_cvt_pk_bf16_f32 v84, v52, v53\n\t"
    "v_mfma_f32_16x16x32_bf16 v[48:51], a[96:99], a[16:19], v[4:7]\n\t"
    "s_add_u32 s79, s63, 98304\n\t"
    "ds_read_b128 a[116:119], v13 offset:20480\n\t"
    "v_mfma_f32_16x16x32_bf16 v[48:51], a[100:103], a[20:23], v[48:51]\n\t"
    "s_add_u32 m0, s77, 32768\n\t"
    "ds_read_b128 a[120:123], v14 offset:20480\n\t"
    "v_mfma_f32_16x16x32_bf16 v[48:51], a[104:107], a[24:27], v[48:51]\n\t"
    "v_exp_f32 v55, v55\n\t"
    "v_mfma_f32_16x16x32_bf16 v[48:51], a[108:111], a[28:31], v[48:51]\n\t"
    "ds_read_b128 a[124:127], v15 offset:20480\n\t"
    "v_cvt_pk_bf16_f32 v85, v54, v55\n\t"
    "v_mfma_f32_16x16x32_bf16 v[200:203], a[144:147], v[116:119], v[200:203]\n\t"
    "ds_read_b64_tr_b16 a[128:129], v16 offset:49152\n\t"
    "ds_read_b64_tr_b16 a[130:131], v16 offset:51200\n\t"
    "v_mfma_f32_16x16x32_bf16 v[200:203], a[148:151], v[128:131], v[200:203]\n\t"
    "buffer_load_dwordx4 v24, s[44:47], s79 offen offset:0 nt lds\n\t"
    "v_mfma_f32_16x16x32_bf16 v[200:203], a[152:155], v[140:143], v[200:203]\n\t"
    "v_exp_f32 v56, v56\n\t"
    "v_mfma_f32_16x16x32_bf16 v[200:203], a[156:159], v[152:155], v[200:203]\n\t"
    "ds_read_b64_tr_b16 a[132:133], v16 offset:53248\n\t"
    "ds_read_b64_tr_b16 a[134:135], v16 offset:55296\n\t"
    "v_mfma_f32_16x16x32_bf16 v[52:55], a[96:99], a[32:35], v[8:11]\n\t"
    "v_exp_f32 v57, v57\n\t"
    "v_mfma_f32_16x16x32_bf16 v[52:55], a[100:103], a[36:39], v[52:55]\n\t"
    "ds_read_b64_tr_b16 a[136:137], v16 offset:57344\n\t"
    "v_cvt_pk_bf16_f32 v78, v56, v57\n\t"
    "v_mfma_f32_16x16x32_bf16 v[52:55], a[104:107], a[40:43], v[52:55]\n\t"
    "v_exp_f32 v58, v58\n\t"
    "v_mfma_f32_16x16x32_bf16 v[52:55], a[108:111], a[44:47], v[52:55]\n\t"
    "ds_read_b64_tr_b16 a[138:139], v16 offset:59392\n\t"
    "ds_read_b64_tr_b16 a[140:141], v16 offset:61440\n\t"
    "v_mfma_f32_16x16x32_bf16 v[204:207], a[144:147], v[120:123], v[204:207]\n\t"
    "v_exp_f32 v59, v59\n\t"
    "v_mfma_f32_16x16x32_bf16 v[204:207], a[148:151], v[132:135], v[204:207]\n\t"
    "v_cvt_pk_bf16_f32 v79, v58, v59\n\t"
    "v_mfma_f32_16x16x32_bf16 v[204:207], a[152:155], v[144:147], v[204:207]\n\t"
    "ds_read_b64_tr_b16 a[142:143], v16 offset:63488\n\t"
    "v_mfma_f32_16x16x32_bf16 v[204:207], a[156:159], v[156:159], v[204:207]\n\t"
    "s_waitcnt lgkmcnt(4)\n\t"
    "buffer_load_dwordx4 v25, s[44:47], s79 offen offset:1024 nt lds\n\t"
    "v_mfma_f32_16x16x32_bf16 v[56:59], a[112:115], a[0:3], v[0:3]\n\t"
    "ds_read_b128 a[96:99], v12 offset:24576\n\t"
    "v_mfma_f32_16x16x32_bf16 v[56:59], a[116:119], a[4:7], v[56:59]\n\t"
    "v_exp_f32 v60, v60\n\t"
    "v_mfma_f32_16x16x32_bf16 v[56:59], a[120:123], a[8:11], v[56:59]\n\t"
    "ds_read_b128 a[100:103], v13 offset:24576\n\t"
    "v_mfma_f32_16x16x32_bf16 v[56:59], a[124:127], a[12:15], v[56:59]\n\t"
    "v_exp_f32 v61, v61\n\t"
    "v_mfma_f32_16x16x32_bf16 v[208:211], a[128:131], v[112:115], v[208:211]\n\t"
    "ds_read_b128 a[104:107], v14 offset:24576\n\t"
    "v_cvt_pk_bf16_f32 v82, v60, v61\n\t"
    "v_mfma_f32_16x16x32_bf16 v[208:211], a[132:135], v[124:127], v[208:211]\n\t"
    "s_waitcnt lgkmcnt(4)\n\t"
    "v_exp_f32 v62, v62\n\t"
    "v_mfma_f32_16x16x32_bf16 v[208:211], a[136:139], v[136:139], v[208:211]\n\t"
    "s_waitcnt lgkmcnt(3)\n\t"
    "v_exp_f32 v63, v63\n\t"
    "v_mfma_f32_16x16x32_bf16 v[208:211], a[140:143], v[148:151], v[208:211]\n\t"
    "ds_read_b128 a[108:111], v15 offset:24576\n\t"
    "v_cvt_pk_bf16_f32 v83, v62, v63\n\t"
    "v_mfma_f32_16x16x32_bf16 v[60:63], a[112:115], a[16:19], v[4:7]\n\t"
    "buffer_load_dwordx4 v26, s[44:47], s79 offen offset:2048 nt lds\n\t"
    "v_mfma_f32_16x16x32_bf16 v[60:63], a[116:119], a[20:23], v[60:63]\n\t"
    "ds_read_b64_tr_b16 a[144:145], v17 offset:49152\n\t"
    "ds_read_b64_tr_b16 a[146:147], v17 offset:51200\n\t"
    "v_mfma_f32_16x16x32_bf16 v[60:63], a[120:123], a[24:27], v[60:63]\n\t"
    "v_exp_f32 v40, v40\n\t"
    "v_mfma_f32_16x16x32_bf16 v[60:63], a[124:127], a[28:31], v[60:63]\n\t"
    "v_exp_f32 v41, v41\n\t"
    "v_mfma_f32_16x16x32_bf16 v[212:215], a[128:131], v[116:119], v[212:215]\n\t"
    "v_exp_f32 v42, v42\n\t"
    "v_mfma_f32_16x16x32_bf16 v[212:215], a[132:135], v[128:131], v[212:215]\n\t"
    "v_exp_f32 v43, v43\n\t"
    "v_mfma_f32_16x16x32_bf16 v[212:215], a[136:139], v[140:143], v[212:215]\n\t"
    "ds_read_b64_tr_b16 a[148:149], v17 offset:53248\n\t"
    "v_cvt_pk_bf16_f32 v86, v40, v41\n\t"
    "v_mfma_f32_16x16x32_bf16 v[212:215], a[140:143], v[152:155], v[212:215]\n\t"
    "v_cvt_pk_bf16_f32 v87, v42, v43\n\t"
    "v_mfma_f32_16x16x32_bf16 a[164:167], a[172:175], v[116:119], a[164:167]\n\t"
    "ds_read_b64_tr_b16 a[150:151], v17 offset:55296\n\t"
    "ds_read_b64_tr_b16 a[152:153], v17 offset:57344\n\t"
    "v_mfma_f32_16x16x32_bf16 a[164:167], a[172:175], v[128:131], a[164:167]\n\t"
    "buffer_load_dwordx4 v27, s[44:47], s79 offen offset:3072 nt lds\n\t"
    "v_mfma_f32_16x16x32_bf16 a[164:167], a[172:175], v[140:143], a[164:167]\n\t"
    "v_exp_f32 v44, v44\n\t"
    "v_mfma_f32_16x16x32_bf16 a[164:167], a[172:175], v[152:155], a[164:167]\n\t"
    "v_exp_f32 v45, v45\n\t"
    "v_mfma_f32_16x16x32_bf16 v[40:43], a[112:115], a[32:35], v[8:11]\n\t"
    "v_exp_f32 v46, v46\n\t"
    "v_mfma_f32_16x16x32_bf16 v[40:43], a[116:119], a[36:39], v[40:43]\n\t"
    "ds_read_b64_tr_b16 a[154:155], v17 offset:59392\n\t"
    "v_cvt_pk_bf16_f32 v88, v44, v45\n\t"
    "v_mfma_f32_16x16x32_bf16 v[40:43], a[120:123], a[40:43], v[40:43]\n\t"
    "v_exp_f32 v47, v47\n\t"
    "v_mfma_f32_16x16x32_bf16 v[40:43], a[124:127], a[44:47], v[40:43]\n\t"
    "ds_read_b64_tr_b16 a[156:157], v17 offset:61440\n\t"
    "v_cvt_pk_bf16_f32 v89, v46, v47\n\t"
    "v_mfma_f32_16x16x32_bf16 v[216:219], a[128:131], v[120:123], v[216:219]\n\t"
    "v_exp_f32 v48, v48\n\t"
    "v_mfma_f32_16x16x32_bf16 v[216:219], a[132:135], v[132:135], v[216:219]\n\t"
    "v_exp_f32 v49, v49\n\t"
    "v_mfma_f32_16x16x32_bf16 v[216:219], a[136:139], v[144:147], v[216:219]\n\t"
    "ds_read_b64_tr_b16 a[158:159], v17 offset:63488\n\t"
    "v_cvt_pk_bf16_f32 v92, v48, v49\n\t"
    "v_mfma_f32_16x16x32_bf16 v[216:219], a[140:143], v[156:159], v[216:219]\n\t"
    "s_waitcnt lgkmcnt(3)\n\t"
    "v_exp_f32 v50, v50\n\t"
    "v_mfma_f32_16x16x32_bf16 v[44:47], a[96:99], a[0:3], v[0:3]\n\t"
    "ds_read_b128 a[112:115], v12 offset:28672\n\t"
    "v_mfma_f32_16x16x32_bf16 v[44:47], a[100:103], a[4:7], v[44:47]\n\t"
    "v_exp_f32 v51, v51\n\t"
    "v_mfma_f32_16x16x32_bf16 v[44:47], a[104:107], a[8:11], v[44:47]\n\t"
    "ds_read_b128 a[116:119], v13 offset:28672\n\t"
    "v_cvt_pk_bf16_f32 v93, v50, v51\n\t"
    "v_mfma_f32_16x16x32_bf16 v[44:47], a[108:111], a[12:15], v[44:47]\n\t"
    "v_exp_f32 v52, v52\n\t"
    "v_mfma_f32_16x16x32_bf16 v[220:223], a[144:147], v[112:115], v[220:223]\n\t"
    "ds_read_b128 a[120:123], v14 offset:28672\n\t"
    "v_mfma_f32_16x16x32_bf16 v[220:223], a[148:151], v[124:127], v[220:223]\n\t"
    "s_waitcnt lgkmcnt(4)\n\t"
    "v_exp_f32 v53, v53\n\t"
    "v_mfma_f32_16x16x32_bf16 v[220:223], a[152:155], v[136:139], v[220:223]\n\t"
    "s_waitcnt lgkmcnt(3)\n\t"
    "ds_read_b128 a[124:127], v15 offset:28672\n\t"
    "v_cvt_pk_bf16_f32 v96, v52, v53\n\t"
    "v_mfma_f32_16x16x32_bf16 v[220:223], a[156:159], v[148:151], v[220:223]\n\t"
    "v_exp_f32 v54, v54\n\t"
    "v_mfma_f32_16x16x32_bf16 v[48:51], a[96:99], a[16:19], v[4:7]\n\t"
    "ds_read_b64_tr_b16 a[128:129], v18 offset:49152\n\t"
    "ds_read_b64_tr_b16 a[130:131], v18 offset:51200\n\t"
    "v_mfma_f32_16x16x32_bf16 v[48:51], a[100:103], a[20:23], v[48:51]\n\t"
    "v_exp_f32 v55, v55\n\t"
    "v_mfma_f32_16x16x32_bf16 v[48:51], a[104:107], a[24:27], v[48:51]\n\t"
    "v_cvt_pk_bf16_f32 v97, v54, v55\n\t"
    "v_mfma_f32_16x16x32_bf16 v[48:51], a[108:111], a[28:31], v[48:51]\n\t"
    "v_exp_f32 v56, v56\n\t"
    "v_mfma_f32_16x16x32_bf16 v[224:227], a[144:147], v[116:119], v[224:227]\n\t"
    "ds_read_b64_tr_b16 a[132:133], v18 offset:53248\n\t"
    "ds_read_b64_tr_b16 a[134:135], v18 offset:55296\n\t"
    "v_mfma_f32_16x16x32_bf16 v[224:227], a[148:151], v[128:131], v[224:227]\n\t"
    "v_exp_f32 v57, v57\n\t"
    "v_mfma_f32_16x16x32_bf16 v[224:227], a[152:155], v[140:143], v[224:227]\n\t"
    "v_exp_f32 v58, v58\n\t"
    "v_mfma_f32_16x16x32_bf16 v[224:227], a[156:159], v[152:155], v[224:227]\n\t"
    "v_exp_f32 v59, v59\n\t"
    "v_mfma_f32_16x16x32_bf16 v[52:55], a[96:99], a[32:35], v[8:11]\n\t"
    "ds_read_b64_tr_b16 a[136:137], v18 offset:57344\n\t"
    "v_cvt_pk_bf16_f32 v90, v56, v57\n\t"
    "v_mfma_f32_16x16x32_bf16 v[52:55], a[100:103], a[36:39], v[52:55]\n\t"
    "v_cvt_pk_bf16_f32 v91, v58, v59\n\t"
    "v_mfma_f32_16x16x32_bf16 v[52:55], a[104:107], a[40:43], v[52:55]\n\t"
    "ds_read_b64_tr_b16 a[138:139], v18 offset:59392\n\t"
    "ds_read_b64_tr_b16 a[140:141], v18 offset:61440\n\t"
    "v_mfma_f32_16x16x32_bf16 v[52:55], a[108:111], a[44:47], v[52:55]\n\t"
    "buffer_store_dwordx4 a[204:207], v33, s[48:51], s67 offen offset:2176 nt\n\t"
    "v_mfma_f32_16x16x32_bf16 v[228:231], a[144:147], v[120:123], v[228:231]\n\t"
    "v_exp_f32 v60, v60\n\t"
    "v_mfma_f32_16x16x32_bf16 v[228:231], a[148:151], v[132:135], v[228:231]\n\t"
    "v_exp_f32 v61, v61\n\t"
    "v_mfma_f32_16x16x32_bf16 v[228:231], a[152:155], v[144:147], v[228:231]\n\t"
    "ds_read_b64_tr_b16 a[142:143], v18 offset:63488\n\t"
    "v_cvt_pk_bf16_f32 v94, v60, v61\n\t"
    "v_mfma_f32_16x16x32_bf16 v[228:231], a[156:159], v[156:159], v[228:231]\n\t"
    "s_waitcnt lgkmcnt(4)\n\t"
    "v_exp_f32 v62, v62\n\t"
    "v_mfma_f32_16x16x32_bf16 v[56:59], a[112:115], a[0:3], v[0:3]\n\t"
    "ds_read_b128 a[96:99], v12 offset:32768\n\t"
    "v_mfma_f32_16x16x32_bf16 v[56:59], a[116:119], a[4:7], v[56:59]\n\t"
    "v_exp_f32 v63, v63\n\t"
    "v_mfma_f32_16x16x32_bf16 v[56:59], a[120:123], a[8:11], v[56:59]\n\t"
    "ds_read_b128 a[100:103], v13 offset:32768\n\t"
    "v_cvt_pk_bf16_f32 v95, v62, v63\n\t"
    "v_mfma_f32_16x16x32_bf16 v[56:59], a[124:127], a[12:15], v[56:59]\n\t"
    "v_exp_f32 v40, v40\n\t"
    "v_mfma_f32_16x16x32_bf16 v[232:235], a[128:131], v[112:115], v[232:235]\n\t"
    "ds_read_b128 a[104:107], v14 offset:32768\n\t"
    "v_mfma_f32_16x16x32_bf16 v[232:235], a[132:135], v[124:127], v[232:235]\n\t"
    "s_waitcnt lgkmcnt(4)\n\t"
    "v_exp_f32 v41, v41\n\t"
    "v_mfma_f32_16x16x32_bf16 v[232:235], a[136:139], v[136:139], v[232:235]\n\t"
    "s_waitcnt lgkmcnt(3)\n\t"
    "ds_read_b128 a[108:111], v15 offset:32768\n\t"
    "v_cvt_pk_bf16_f32 v98, v40, v41\n\t"
    "v_mfma_f32_16x16x32_bf16 v[232:235], a[140:143], v[148:151], v[232:235]\n\t"
    "v_exp_f32 v42, v42\n\t"
    "v_mfma_f32_16x16x32_bf16 v[60:63], a[112:115], a[16:19], v[4:7]\n\t"
    "v_exp_f32 v43, v43\n\t"
    "v_mfma_f32_16x16x32_bf16 v[60:63], a[116:119], a[20:23], v[60:63]\n\t"
    "ds_read_b64_tr_b16 a[144:145], v19 offset:49152\n\t"
    "v_cvt_pk_bf16_f32 v99, v42, v43\n\t"
    "v_mfma_f32_16x16x32_bf16 v[60:63], a[120:123], a[24:27], v[60:63]\n\t"
    "v_exp_f32 v44, v44\n\t"
    "v_mfma_f32_16x16x32_bf16 v[60:63], a[124:127], a[28:31], v[60:63]\n\t"
    "ds_read_b64_tr_b16 a[146:147], v19 offset:51200\n\t"
    "ds_read_b64_tr_b16 a[148:149], v19 offset:53248\n\t"
    "v_mfma_f32_16x16x32_bf16 v[236:239], a[128:131], v[116:119], v[236:239]\n\t"
    "v_exp_f32 v45, v45\n\t"
    "v_mfma_f32_16x16x32_bf16 v[236:239], a[132:135], v[128:131], v[236:239]\n\t"
    "v_exp_f32 v46, v46\n\t"
    "v_mfma_f32_16x16x32_bf16 v[236:239], a[136:139], v[140:143], v[236:239]\n\t"
    "v_exp_f32 v47, v47\n\t"
    "v_mfma_f32_16x16x32_bf16 v[236:239], a[140:143], v[152:155], v[236:239]\n\t"
    "ds_read_b64_tr_b16 a[150:151], v19 offset:55296\n\t"
    "v_cvt_pk_bf16_f32 v100, v44, v45\n\t"
    "v_mfma_f32_16x16x32_bf16 a[168:171], a[172:175], v[120:123], a[168:171]\n\t"
    "v_cvt_pk_bf16_f32 v101, v46, v47\n\t"
    "v_mfma_f32_16x16x32_bf16 a[168:171], a[172:175], v[132:135], a[168:171]\n\t"
    "v_exp_f32 v48, v48\n\t"
    "v_mfma_f32_16x16x32_bf16 a[168:171], a[172:175], v[144:147], a[168:171]\n\t"
    "ds_read_b64_tr_b16 a[152:153], v19 offset:57344\n\t"
    "ds_read_b64_tr_b16 a[154:155], v19 offset:59392\n\t"
    "v_mfma_f32_16x16x32_bf16 a[168:171], a[172:175], v[156:159], a[168:171]\n\t"
    "v_exp_f32 v49, v49\n\t"
    "v_mfma_f32_16x16x32_bf16 v[40:43], a[112:115], a[32:35], v[8:11]\n\t"
    "v_exp_f32 v50, v50\n\t"
    "v_mfma_f32_16x16x32_bf16 v[40:43], a[116:119], a[36:39], v[40:43]\n\t"
    "v_exp_f32 v51, v51\n\t"
    "v_mfma_f32_16x16x32_bf16 v[40:43], a[120:123], a[40:43], v[40:43]\n\t"
    "ds_read_b64_tr_b16 a[156:157], v19 offset:61440\n\t"
    "v_cvt_pk_bf16_f32 v104, v48, v49\n\t"
    "v_mfma_f32_16x16x32_bf16 v[40:43], a[124:127], a[44:47], v[40:43]\n\t"
    "v_cvt_pk_bf16_f32 v105, v50, v51\n\t"
    "v_mfma_f32_16x16x32_bf16 v[240:243], a[128:131], v[120:123], v[240:243]\n\t"
    "v_exp_f32 v52, v52\n\t"
    "v_mfma_f32_16x16x32_bf16 v[240:243], a[132:135], v[132:135], v[240:243]\n\t"
    "ds_read_b64_tr_b16 a[158:159], v19 offset:63488\n\t"
    "v_mfma_f32_16x16x32_bf16 v[240:243], a[136:139], v[144:147], v[240:243]\n\t"
    "v_exp_f32 v53, v53\n\t"
    "v_mfma_f32_16x16x32_bf16 v[240:243], a[140:143], v[156:159], v[240:243]\n\t"
    "s_waitcnt lgkmcnt(2)\n\t"
    "v_exp_f32 v54, v54\n\t"
    "v_mfma_f32_16x16x32_bf16 v[44:47], a[96:99], a[0:3], v[0:3]\n\t"
    "v_exp_f32 v55, v55\n\t"
    "v_mfma_f32_16x16x32_bf16 v[44:47], a[100:103], a[4:7], v[44:47]\n\t"
    "v_cvt_pk_bf16_f32 v108, v52, v53\n\t"
    "v_cvt_pk_bf16_f32 v109, v54, v55\n\t"
    "v_mfma_f32_16x16x32_bf16 v[44:47], a[104:107], a[8:11], v[44:47]\n\t"
    "v_exp_f32 v56, v56\n\t"
    "v_mfma_f32_16x16x32_bf16 v[44:47], a[108:111], a[12:15], v[44:47]\n\t"
    "v_exp_f32 v57, v57\n\t"
    "v_mfma_f32_16x16x32_bf16 v[244:247], a[144:147], v[112:115], v[244:247]\n\t"
    "v_exp_f32 v58, v58\n\t"
    "v_mfma_f32_16x16x32_bf16 v[244:247], a[148:151], v[124:127], v[244:247]\n\t"
    "v_exp_f32 v59, v59\n\t"
    "v_mfma_f32_16x16x32_bf16 v[244:247], a[152:155], v[136:139], v[244:247]\n\t"
    "s_waitcnt vmcnt(16) lgkmcnt(0)\n\t"
    "v_cvt_pk_bf16_f32 v102, v56, v57\n\t"
    "v_cvt_pk_bf16_f32 v103, v58, v59\n\t"
    "v_mfma_f32_16x16x32_bf16 v[244:247], a[156:159], v[148:151], v[244:247]\n\t"
    "s_barrier\n\t"
    "ds_read_b128 a[112:115], v12 offset:36864\n\t"
    "v_mfma_f32_16x16x32_bf16 v[48:51], a[96:99], a[16:19], v[4:7]\n\t"
    "s_add_u32 s79, s63, 98432\n\t"
    "ds_read_b128 a[116:119], v13 offset:36864\n\t"
    "v_mfma_f32_16x16x32_bf16 v[48:51], a[100:103], a[20:23], v[48:51]\n\t"
    "s_add_u32 m0, s77, 49152\n\t"
    "ds_read_b128 a[120:123], v14 offset:36864\n\t"
    "v_mfma_f32_16x16x32_bf16 v[48:51], a[104:107], a[24:27], v[48:51]\n\t"
    "buffer_load_dwordx4 v24, s[44:47], s79 offen offset:0 nt lds\n\t"
    "v_mfma_f32_16x16x32_bf16 v[48:51], a[108:111], a[28:31], v[48:51]\n\t"
    "ds_read_b128 a[124:127], v15 offset:36864\n\t"
    "ds_read_b64_tr_b16 a[128:129], v16 offset:0\n\t"
    "v_mfma_f32_16x16x32_bf16 v[248:251], a[144:147], v[116:119], v[248:251]\n\t"
    "v_exp_f32 v60, v60\n\t"
    "v_mfma_f32_16x16x32_bf16 v[248:251], a[148:151], v[128:131], v[248:251]\n\t"
    "ds_read_b64_tr_b16 a[130:131], v16 offset:2048\n\t"
    "ds_read_b64_tr_b16 a[132:133], v16 offset:4096\n\t"
    "v_mfma_f32_16x16x32_bf16 v[248:251], a[152:155], v[140:143], v[248:251]\n\t"
    "v_exp_f32 v61, v61\n\t"
    "v_mfma_f32_16x16x32_bf16 v[248:251], a[156:159], v[152:155], v[248:251]\n\t"
    "v_exp_f32 v62, v62\n\t"
    "v_mfma_f32_16x16x32_bf16 v[52:55], a[96:99], a[32:35], v[8:11]\n\t"
    "ds_read_b64_tr_b16 a[134:135], v16 offset:6144\n\t"
    "v_cvt_pk_bf16_f32 v106, v60, v61\n\t"
    "v_mfma_f32_16x16x32_bf16 v[52:55], a[100:103], a[36:39], v[52:55]\n\t"
    "ds_read_b64_tr_b16 a[136:137], v16 offset:8192\n\t"
    "ds_read_b64_tr_b16 a[138:139], v16 offset:10240\n\t"
    "v_mfma_f32_16x16x32_bf16 v[52:55], a[104:107], a[40:43], v[52:55]\n\t"
    "v_exp_f32 v63, v63\n\t"
    "v_mfma_f32_16x16x32_bf16 v[52:55], a[108:111], a[44:47], v[52:55]\n\t"
    "v_cvt_pk_bf16_f32 v107, v62, v63\n\t"
    "v_mfma_f32_16x16x32_bf16 v[252:255], a[144:147], v[120:123], v[252:255]\n\t"
    "ds_read_b64_tr_b16 a[140:141], v16 offset:12288\n\t"
    "ds_read_b64_tr_b16 a[142:143], v16 offset:14336\n\t"
    "v_mfma_f32_16x16x32_bf16 v[252:255], a[148:151], v[132:135], v[252:255]\n\t"
    "buffer_load_dwordx4 v25, s[44:47], s79 offen offset:1024 nt lds\n\t"
    "v_mfma_f32_16x16x32_bf16 v[252:255], a[152:155], v[144:147], v[252:255]\n\t"
    "v_exp_f32 v40, v40\n\t"
    "v_mfma_f32_16x16x32_bf16 v[252:255], a[156:159], v[156:159], v[252:255]\n\t"
    "s_waitcnt lgkmcnt(5)\n\t"
    "v_exp_f32 v41, v41\n\t"
    "v_mfma_f32_16x16x32_bf16 v[56:59], a[112:115], a[0:3], v[0:3]\n\t"
    "ds_read_b128 a[96:99], v12 offset:40960\n\t"
    "v_cvt_pk_bf16_f32 v110, v40, v41\n\t"
    "v_mfma_f32_16x16x32_bf16 v[56:59], a[116:119], a[4:7], v[56:59]\n\t"
    "buffer_store_dwordx4 a[208:211], v33, s[48:51], s68 offen offset:128 nt\n\t"
    "v_mfma_f32_16x16x32_bf16 v[56:59], a[120:123], a[8:11], v[56:59]\n\t"
    "ds_read_b128 a[100:103], v13 offset:40960\n\t"
    "v_mfma_f32_16x16x32_bf16 v[56:59], a[124:127], a[12:15], v[56:59]\n\t"
    "v_exp_f32 v42, v42\n\t"
    "v_mfma_f32_16x16x32_bf16 v[160:163], a[128:131], v[64:67], v[160:163]\n\t"
    "s_waitcnt lgkmcnt(2)\n\t"
    "ds_read_b128 a[104:107], v14 offset:40960\n\t"
    "v_mfma_f32_16x16x32_bf16 v[160:163], a[132:135], v[76:79], v[160:163]\n\t"
    "v_exp_f32 v43, v43\n\t"
    "v_mfma_f32_16x16x32_bf16 v[160:163], a[136:139], v[88:91], v[160:163]\n\t"
    "ds_read_b128 a[108:111], v15 offset:40960\n\t"
    "v_cvt_pk_bf16_f32 v111, v42, v43\n\t"
    "v_mfma_f32_16x16x32_bf16 v[160:163], a[140:143], v[100:103], v[160:163]\n\t"
    "buffer_load_dwordx4 v26, s[44:47], s79 offen offset:2048 nt lds\n\t"
    "v_mfma_f32_16x16x32_bf16 v[60:63], a[112:115], a[16:19], v[4:7]\n\t"
    "ds_read_b64_tr_b16 a[144:145], v17 offset:0\n\t"
    "ds_read_b64_tr_b16 a[146:147], v17 offset:2048\n\t"
    "v_mfma_f32_16x16x32_bf16 v[60:63], a[116:119], a[20:23], v[60:63]\n\t"
    "v_exp_f32 v44, v44\n\t"
    "v_mfma_f32_16x16x32_bf16 v[60:63], a[120:123], a[24:27], v[60:63]\n\t"
    "v_exp_f32 v45, v45\n\t"
    "v_mfma_f32_16x16x32_bf16 v[60:63], a[124:127], a[28:31], v[60:63]\n\t"
    "v_exp_f32 v46, v46\n\t"
    "v_mfma_f32_16x16x32_bf16 v[164:167], a[128:131], v[68:71], v[164:167]\n\t"
    "ds_read_b64_tr_b16 a[148:149], v17 offset:4096\n\t"
    "v_cvt_pk_bf16_f32 v112, v44, v45\n\t"
    "v_mfma_f32_16x16x32_bf16 v[164:167], a[132:135], v[80:83], v[164:167]\n\t"
    "v_exp_f32 v47, v47\n\t"
    "v_mfma_f32_16x16x32_bf16 v[164:167], a[136:139], v[92:95], v[164:167]\n\t"
    "ds_read_b64_tr_b16 a[150:151], v17 offset:6144\n\t"
    "v_cvt_pk_bf16_f32 v113, v46, v47\n\t"
    "v_mfma_f32_16x16x32_bf16 v[164:167], a[140:143], v[104:107], v[164:167]\n\t"
    "v_exp_f32 v48, v48\n\t"
    "v_mfma_f32_16x16x32_bf16 v[40:43], a[112:115], a[32:35], v[8:11]\n\t"
    "ds_read_b64_tr_b16 a[152:153], v17 offset:8192\n\t"
    "ds_read_b64_tr_b16 a[154:155], v17 offset:10240\n\t"
    "v_mfma_f32_16x16x32_bf16 v[40:43], a[116:119], a[36:39], v[40:43]\n\t"
    "buffer_load_dwordx4 v27, s[44:47], s79 offen offset:3072 nt lds\n\t"
    "v_mfma_f32_16x16x32_bf16 v[40:43], a[120:123], a[40:43], v[40:43]\n\t"
    "v_exp_f32 v49, v49\n\t"
    "v_mfma_f32_16x16x32_bf16 v[40:43], a[124:127], a[44:47], v[40:43]\n\t"
    "v_exp_f32 v50, v50\n\t"
    "v_mfma_f32_16x16x32_bf16 v[168:171], a[128:131], v[72:75], v[168:171]\n\t"
    "ds_read_b64_tr_b16 a[156:157], v17 offset:12288\n\t"
    "v_cvt_pk_bf16_f32 v116, v48, v49\n\t"
    "v_mfma_f32_16x16x32_bf16 v[168:171], a[132:135], v[84:87], v[168:171]\n\t"
    "v_exp_f32 v51, v51\n\t"
    "v_mfma_f32_16x16x32_bf16 v[168:171], a[136:139], v[96:99], v[168:171]\n\t"
    "ds_read_b64_tr_b16 a[158:159], v17 offset:14336\n\t"
    "v_cvt_pk_bf16_f32 v117, v50, v51\n\t"
    "v_mfma_f32_16x16x32_bf16 v[168:171], a[140:143], v[108:111], v[168:171]\n\t"
    "s_waitcnt lgkmcnt(4)\n\t"
    "v_exp_f32 v52, v52\n\t"
    "v_mfma_f32_16x16x32_bf16 v[44:47], a[96:99], a[0:3], v[0:3]\n\t"
    "ds_read_b128 a[112:115], v12 offset:45056\n\t"
    "v_mfma_f32_16x16x32_bf16 v[44:47], a[100:103], a[4:7], v[44:47]\n\t"
    "v_exp_f32 v53, v53\n\t"
    "v_mfma_f32_16x16x32_bf16 v[44:47], a[104:107], a[8:11], v[44:47]\n\t"
    "ds_read_b128 a[116:119], v13 offset:45056\n\t"
    "v_cvt_pk_bf16_f32 v120, v52, v53\n\t"
    "v_mfma_f32_16x16x32_bf16 v[44:47], a[108:111], a[12:15], v[44:47]\n\t"
    "v_exp_f32 v54, v54\n\t"
    "v_mfma_f32_16x16x32_bf16 v[172:175], a[144:147], v[64:67], v[172:175]\n\t"
    "ds_read_b128 a[120:123], v14 offset:45056\n\t"
    "v_mfma_f32_16x16x32_bf16 v[172:175], a[148:151], v[76:79], v[172:175]\n\t"
    "s_waitcnt lgkmcnt(4)\n\t"
    "v_exp_f32 v55, v55\n\t"
    "v_mfma_f32_16x16x32_bf16 v[172:175], a[152:155], v[88:91], v[172:175]\n\t"
    "s_waitcnt lgkmcnt(3)\n\t"
    "v_cvt_pk_bf16_f32 v121, v54, v55\n\t"
    "v_mfma_f32_16x16x32_bf16 v[172:175], a[156:159], v[100:103], v[172:175]\n\t"
    "ds_read_b128 a[124:127], v15 offset:45056\n\t"
    "ds_read_b64_tr_b16 a[128:129], v18 offset:0\n\t"
    "v_mfma_f32_16x16x32_bf16 v[48:51], a[96:99], a[16:19], v[4:7]\n\t"
    "v_exp_f32 v56, v56\n\t"
    "v_mfma_f32_16x16x32_bf16 v[48:51], a[100:103], a[20:23], v[48:51]\n\t"
    "v_exp_f32 v57, v57\n\t"
    "v_mfma_f32_16x16x32_bf16 v[48:51], a[104:107], a[24:27], v[48:51]\n\t"
    "v_exp_f32 v58, v58\n\t"
    "v_mfma_f32_16x16x32_bf16 v[48:51], a[108:111], a[28:31], v[48:51]\n\t"
    "ds_read_b64_tr_b16 a[130:131], v18 offset:2048\n\t"
    "v_cvt_pk_bf16_f32 v114, v56, v57\n\t"
    "v_mfma_f32_16x16x32_bf16 v[176:179], a[144:147], v[68:71], v[176:179]\n\t"
    "v_exp_f32 v59, v59\n\t"
    "v_mfma_f32_16x16x32_bf16 v[176:179], a[148:151], v[80:83], v[176:179]\n\t"
    "v_cvt_pk_bf16_f32 v115, v58, v59\n\t"
    "v_mfma_f32_16x16x32_bf16 v[176:179], a[152:155], v[92:95], v[176:179]\n\t"
    "ds_read_b64_tr_b16 a[132:133], v18 offset:4096\n\t"
    "ds_read_b64_tr_b16 a[134:135], v18 offset:6144\n\t"
    "v_mfma_f32_16x16x32_bf16 v[176:179], a[156:159], v[104:107], v[176:179]\n\t"
    "v_exp_f32 v60, v60\n\t"
    "v_mfma_f32_16x16x32_bf16 a[160:163], a[172:175], v[64:67], a[160:163]\n\t"
    "v_exp_f32 v61, v61\n\t"
    "v_mfma_f32_16x16x32_bf16 a[160:163], a[172:175], v[76:79], a[160:163]\n\t"
    "v_exp_f32 v62, v62\n\t"
    "v_mfma_f32_16x16x32_bf16 a[160:163], a[172:175], v[88:91], a[160:163]\n\t"
    "ds_read_b64_tr_b16 a[136:137], v18 offset:8192\n\t"
    "v_cvt_pk_bf16_f32 v118, v60, v61\n\t"
    "v_mfma_f32_16x16x32_bf16 a[160:163], a[172:175], v[100:103], a[160:163]\n\t"
    "v_exp_f32 v63, v63\n\t"
    "v_mfma_f32_16x16x32_bf16 v[52:55], a[96:99], a[32:35], v[8:11]\n\t"
    "v_cvt_pk_bf16_f32 v119, v62, v63\n\t"
    "v_mfma_f32_16x16x32_bf16 v[52:55], a[100:103], a[36:39], v[52:55]\n\t"
    "ds_read_b64_tr_b16 a[138:139], v18 offset:10240\n\t"
    "ds_read_b64_tr_b16 a[140:141], v18 offset:12288\n\t"
    "v_mfma_f32_16x16x32_bf16 v[52:55], a[104:107], a[40:43], v[52:55]\n\t"
    "v_exp_f32 v40, v40\n\t"
    "v_mfma_f32_16x16x32_bf16 v[52:55], a[108:111], a[44:47], v[52:55]\n\t"
    "v_exp_f32 v41, v41\n\t"
    "v_mfma_f32_16x16x32_bf16 v[180:183], a[144:147], v[72:75], v[180:183]\n\t"
    "v_exp_f32 v42, v42\n\t"
    "v_mfma_f32_16x16x32_bf16 v[180:183], a[148:151], v[84:87], v[180:183]\n\t"
    "v_exp_f32 v43, v43\n\t"
    "v_mfma_f32_16x16x32_bf16 v[180:183], a[152:155], v[96:99], v[180:183]\n\t"
    "ds_read_b64_tr_b16 a[142:143], v18 offset:14336\n\t"
    "v_cvt_pk_bf16_f32 v122, v40, v41\n\t"
    "v_mfma_f32_16x16x32_bf16 v[180:183], a[156:159], v[108:111], v[180:183]\n\t"
    "s_waitcnt lgkmcnt(3)\n\t"
    "v_cvt_pk_bf16_f32 v123, v42, v43\n\t"
    "v_mfma_f32_16x16x32_bf16 v[56:59], a[112:115], a[0:3], v[0:3]\n\t"
    "ds_read_b128 a[96:99], v12 offset:49152\n\t"
    "v_mfma_f32_16x16x32_bf16 v[56:59], a[116:119], a[4:7], v[56:59]\n\t"
    "ds_read_b128 a[100:103], v13 offset:49152\n\t"
    "v_mfma_f32_16x16x32_bf16 v[56:59], a[120:123], a[8:11], v[56:59]\n\t"
    "v_exp_f32 v44, v44\n\t"
    "v_mfma_f32_16x16x32_bf16 v[56:59], a[124:127], a[12:15], v[56:59]\n\t"
    "ds_read_b128 a[104:107], v14 offset:49152\n\t"
    "v_mfma_f32_16x16x32_bf16 v[184:187], a[128:131], v[64:67], v[184:187]\n\t"
    "v_exp_f32 v45, v45\n\t"
    "v_mfma_f32_16x16x32_bf16 v[184:187], a[132:135], v[76:79], v[184:187]\n\t"
    "s_waitcnt lgkmcnt(4)\n\t"
    "ds_read_b128 a[108:111], v15 offset:49152\n\t"
    "v_cvt_pk_bf16_f32 v124, v44, v45\n\t"
    "v_mfma_f32_16x16x32_bf16 v[184:187], a[136:139], v[88:91], v[184:187]\n\t"
    "s_waitcnt lgkmcnt(4)\n\t"
    "v_exp_f32 v46, v46\n\t"
    "v_mfma_f32_16x16x32_bf16 v[184:187], a[140:143], v[100:103], v[184:187]\n\t"
    "ds_read_b64_tr_b16 a[144:145], v19 offset:0\n\t"
    "ds_read_b64_tr_b16 a[146:147], v19 offset:2048\n\t"
    "v_mfma_f32_16x16x32_bf16 v[60:63], a[112:115], a[16:19], v[4:7]\n\t"
    "v_exp_f32 v47, v47\n\t"
    "v_mfma_f32_16x16x32_bf16 v[60:63], a[116:119], a[20:23], v[60:63]\n\t"
    "v_cvt_pk_bf16_f32 v125, v46, v47\n\t"
    "v_mfma_f32_16x16x32_bf16 v[60:63], a[120:123], a[24:27], v[60:63]\n\t"
    "v_exp_f32 v48, v48\n\t"
    "v_mfma_f32_16x16x32_bf16 v[60:63], a[124:127], a[28:31], v[60:63]\n\t"
    "ds_read_b64_tr_b16 a[148:149], v19 offset:4096\n\t"
    "ds_read_b64_tr_b16 a[150:151], v19 offset:6144\n\t"
    "v_mfma_f32_16x16x32_bf16 v[188:191], a[128:131], v[68:71], v[188:191]\n\t"
    "v_exp_f32 v49, v49\n\t"
    "v_mfma_f32_16x16x32_bf16 v[188:191], a[132:135], v[80:83], v[188:191]\n\t"
    "buffer_store_dwordx4 a[212:215], v33, s[48:51], s68 offen offset:2176 nt\n\t"
    "v_mfma_f32_16x16x32_bf16 v[188:191], a[136:139], v[92:95], v[188:191]\n\t"
    "v_exp_f32 v50, v50\n\t"
    "v_mfma_f32_16x16x32_bf16 v[188:191], a[140:143], v[104:107], v[188:191]\n\t"
    "ds_read_b64_tr_b16 a[152:153], v19 offset:8192\n\t"
    "v_cvt_pk_bf16_f32 v128, v48, v49\n\t"
    "v_mfma_f32_16x16x32_bf16 v[40:43], a[112:115], a[32:35], v[8:11]\n\t"
    "v_exp_f32 v51, v51\n\t"
    "v_mfma_f32_16x16x32_bf16 v[40:43], a[116:119], a[36:39], v[40:43]\n\t"
    "ds_read_b64_tr_b16 a[154:155], v19 offset:10240\n\t"
    "v_cvt_pk_bf16_f32 v129, v50, v51\n\t"
    "v_mfma_f32_16x16x32_bf16 v[40:43], a[120:123], a[40:43], v[40:43]\n\t"
    "v_exp_f32 v52, v52\n\t"
    "v_mfma_f32_16x16x32_bf16 v[40:43], a[124:127], a[44:47], v[40:43]\n\t"
    "ds_read_b64_tr_b16 a[156:157], v19 offset:12288\n\t"
    "ds_read_b64_tr_b16 a[158:159], v19 offset:14336\n\t"
    "v_mfma_f32_16x16x32_bf16 v[192:195], a[128:131], v[72:75], v[192:195]\n\t"
    "v_exp_f32 v53, v53\n\t"
    "v_mfma_f32_16x16x32_bf16 v[192:195], a[132:135], v[84:87], v[192:195]\n\t"
    "v_exp_f32 v54, v54\n\t"
    "v_mfma_f32_16x16x32_bf16 v[192:195], a[136:139], v[96:99], v[192:195]\n\t"
    "v_exp_f32 v55, v55\n\t"
    "v_mfma_f32_16x16x32_bf16 v[192:195], a[140:143], v[108:111], v[192:195]\n\t"
    "s_waitcnt lgkmcnt(3)\n\t"
    "v_cvt_pk_bf16_f32 v132, v52, v53\n\t"
    "v_cvt_pk_bf16_f32 v133, v54, v55\n\t"
    "v_mfma_f32_16x16x32_bf16 v[44:47], a[96:99], a[0:3], v[0:3]\n\t"
    "v_exp_f32 v56, v56\n\t"
    "v_mfma_f32_16x16x32_bf16 v[44:47], a[100:103], a[4:7], v[44:47]\n\t"
    "v_exp_f32 v57, v57\n\t"
    "v_mfma_f32_16x16x32_bf16 v[44:47], a[104:107], a[8:11], v[44:47]\n\t"
    "v_exp_f32 v58, v58\n\t"
    "v_mfma_f32_16x16x32_bf16 v[44:47], a[108:111], a[12:15], v[44:47]\n\t"
    "v_exp_f32 v59, v59\n\t"
    "v_mfma_f32_16x16x32_bf16 v[196:199], a[144:147], v[64:67], v[196:199]\n\t"
    "v_cvt_pk_bf16_f32 v126, v56, v57\n\t"
    "v_cvt_pk_bf16_f32 v127, v58, v59\n\t"
    "v_mfma_f32_16x16x32_bf16 v[196:199], a[148:151], v[76:79], v[196:199]\n\t"
    "s_waitcnt lgkmcnt(0)\n\t"
    "v_exp_f32 v60, v60\n\t"
    "v_mfma_f32_16x16x32_bf16 v[196:199], a[152:155], v[88:91], v[196:199]\n\t"
    "s_waitcnt vmcnt(12) lgkmcnt(0)\n\t"
    "v_exp_f32 v61, v61\n\t"
    "v_mfma_f32_16x16x32_bf16 v[196:199], a[156:159], v[100:103], v[196:199]\n\t"
    "s_barrier\n\t"
    "ds_read_b128 a[112:115], v12 offset:53248\n\t"
    "v_cvt_pk_bf16_f32 v130, v60, v61\n\t"
    "v_mfma_f32_16x16x32_bf16 v[48:51], a[96:99], a[16:19], v[4:7]\n\t"
    "ds_read_b128 a[116:119], v13 offset:53248\n\t"
    "v_mfma_f32_16x16x32_bf16 v[48:51], a[100:103], a[20:23], v[48:51]\n\t"
    "ds_read_b128 a[120:123], v14 offset:53248\n\t"
    "v_mfma_f32_16x16x32_bf16 v[48:51], a[104:107], a[24:27], v[48:51]\n\t"
    "v_exp_f32 v62, v62\n\t"
    "v_mfma_f32_16x16x32_bf16 v[48:51], a[108:111], a[28:31], v[48:51]\n\t"
    "ds_read_b128 a[124:127], v15 offset:53248\n\t"
    "ds_read_b64_tr_b16 a[128:129], v16 offset:16384\n\t"
    "v_mfma_f32_16x16x32_bf16 v[200:203], a[144:147], v[68:71], v[200:203]\n\t"
    "v_exp_f32 v63, v63\n\t"
    "v_mfma_f32_16x16x32_bf16 v[200:203], a[148:151], v[80:83], v[200:203]\n\t"
    "ds_read_b64_tr_b16 a[130:131], v16 offset:18432\n\t"
    "v_cvt_pk_bf16_f32 v131, v62, v63\n\t"
    "v_mfma_f32_16x16x32_bf16 v[200:203], a[152:155], v[92:95], v[200:203]\n\t"
    "v_exp_f32 v40, v40\n\t"
    "v_mfma_f32_16x16x32_bf16 v[200:203], a[156:159], v[104:107], v[200:203]\n\t"
    "ds_read_b64_tr_b16 a[132:133], v16 offset:20480\n\t"
    "ds_read_b64_tr_b16 a[134:135], v16 offset:22528\n\t"
    "v_mfma_f32_16x16x32_bf16 v[52:55], a[96:99], a[32:35], v[8:11]\n\t"
    "v_exp_f32 v41, v41\n\t"
    "v_mfma_f32_16x16x32_bf16 v[52:55], a[100:103], a[36:39], v[52:55]\n\t"
    "ds_read_b64_tr_b16 a[136:137], v16 offset:24576\n\t"
    "v_cvt_pk_bf16_f32 v134, v40, v41\n\t"
    "v_mfma_f32_16x16x32_bf16 v[52:55], a[104:107], a[40:43], v[52:55]\n\t"
    "v_exp_f32 v42, v42\n\t"
    "v_mfma_f32_16x16x32_bf16 v[52:55], a[108:111], a[44:47], v[52:55]\n\t"
    "ds_read_b64_tr_b16 a[138:139], v16 offset:26624\n\t"
    "ds_read_b64_tr_b16 a[140:141], v16 offset:28672\n\t"
    "v_mfma_f32_16x16x32_bf16 v[204:207], a[144:147], v[72:75], v[204:207]\n\t"
    "v_exp_f32 v43, v43\n\t"
    "v_mfma_f32_16x16x32_bf16 v[204:207], a[148:151], v[84:87], v[204:207]\n\t"
    "v_cvt_pk_bf16_f32 v135, v42, v43\n\t"
    "v_mfma_f32_16x16x32_bf16 v[204:207], a[152:155], v[96:99], v[204:207]\n\t"
    "ds_read_b64_tr_b16 a[142:143], v16 offset:30720\n\t"
    "v_mfma_f32_16x16x32_bf16 v[204:207], a[156:159], v[108:111], v[204:207]\n\t"
    "s_waitcnt lgkmcnt(4)\n\t"
    "v_exp_f32 v44, v44\n\t"
    "v_mfma_f32_16x16x32_bf16 v[56:59], a[112:115], a[0:3], v[0:3]\n\t"
    "ds_read_b128 a[96:99], v12 offset:57344\n\t"
    "v_mfma_f32_16x16x32_bf16 v[56:59], a[116:119], a[4:7], v[56:59]\n\t"
    "v_exp_f32 v45, v45\n\t"
    "v_mfma_f32_16x16x32_bf16 v[56:59], a[120:123], a[8:11], v[56:59]\n\t"
    "ds_read_b128 a[100:103], v13 offset:57344\n\t"
    "v_cvt_pk_bf16_f32 v136, v44, v45\n\t"
    "v_mfma_f32_16x16x32_bf16 v[56:59], a[124:127], a[12:15], v[56:59]\n\t"
    "v_exp_f32 v46, v46\n\t"
    "v_mfma_f32_16x16x32_bf16 v[208:211], a[128:131], v[64:67], v[208:211]\n\t"
    "ds_read_b128 a[104:107], v14 offset:57344\n\t"
    "v_mfma_f32_16x16x32_bf16 v[208:211], a[132:135], v[76:79], v[208:211]\n\t"
    "s_waitcnt lgkmcnt(4)\n\t"
    "v_exp_f32 v47, v47\n\t"
    "v_mfma_f32_16x16x32_bf16 v[208:211], a[136:139], v[88:91], v[208:211]\n\t"
    "s_waitcnt lgkmcnt(3)\n\t"
    "v_cvt_pk_bf16_f32 v137, v46, v47\n\t"
    "v_mfma_f32_16x16x32_bf16 v[208:211], a[140:143], v[100:103], v[208:211]\n\t"
    "ds_read_b128 a[108:111], v15 offset:57344\n\t"
    "ds_read_b64_tr_b16 a[144:145], v17 offset:16384\n\t"
    "v_mfma_f32_16x16x32_bf16 v[60:63], a[112:115], a[16:19], v[4:7]\n\t"
    "v_exp_f32 v48, v48\n\t"
    "v_mfma_f32_16x16x32_bf16 v[60:63], a[116:119], a[20:23], v[60:63]\n\t"
    "v_exp_f32 v49, v49\n\t"
    "v_mfma_f32_16x16x32_bf16 v[60:63], a[120:123], a[24:27], v[60:63]\n\t"
    "v_exp_f32 v50, v50\n\t"
    "v_mfma_f32_16x16x32_bf16 v[60:63], a[124:127], a[28:31], v[60:63]\n\t"
    "ds_read_b64_tr_b16 a[146:147], v17 offset:18432\n\t"
    "v_cvt_pk_bf16_f32 v140, v48, v49\n\t"
    "v_mfma_f32_16x16x32_bf16 v[212:215], a[128:131], v[68:71], v[212:215]\n\t"
    "v_exp_f32 v51, v51\n\t"
    "v_mfma_f32_16x16x32_bf16 v[212:215], a[132:135], v[80:83], v[212:215]\n\t"
    "v_cvt_pk_bf16_f32 v141, v50, v51\n\t"
    "v_mfma_f32_16x16x32_bf16 v[212:215], a[136:139], v[92:95], v[212:215]\n\t"
    "ds_read_b64_tr_b16 a[148:149], v17 offset:20480\n\t"
    "ds_read_b64_tr_b16 a[150:151], v17 offset:22528\n\t"
    "v_mfma_f32_16x16x32_bf16 v[212:215], a[140:143], v[104:107], v[212:215]\n\t"
    "v_exp_f32 v52, v52\n\t"
    "v_mfma_f32_16x16x32_bf16 a[164:167], a[172:175], v[68:71], a[164:167]\n\t"
    "v_exp_f32 v53, v53\n\t"
    "v_mfma_f32_16x16x32_bf16 a[164:167], a[172:175], v[80:83], a[164:167]\n\t"
    "v_exp_f32 v54, v54\n\t"
    "v_mfma_f32_16x16x32_bf16 a[164:167], a[172:175], v[92:95], a[164:167]\n\t"
    "ds_read_b64_tr_b16 a[152:153], v17 offset:24576\n\t"
    "v_cvt_pk_bf16_f32 v144, v52, v53\n\t"
    "v_mfma_f32_16x16x32_bf16 a[164:167], a[172:175], v[104:107], a[164:167]\n\t"
    "v_exp_f32 v55, v55\n\t"
    "v_mfma_f32_16x16x32_bf16 v[40:43], a[112:115], a[32:35], v[8:11]\n\t"
    "v_cvt_pk_bf16_f32 v145, v54, v55\n\t"
    "v_mfma_f32_16x16x32_bf16 v[40:43], a[116:119], a[36:39], v[40:43]\n\t"
    "ds_read_b64_tr_b16 a[154:155], v17 offset:26624\n\t"
    "ds_read_b64_tr_b16 a[156:157], v17 offset:28672\n\t"
    "v_mfma_f32_16x16x32_bf16 v[40:43], a[120:123], a[40:43], v[40:43]\n\t"
    "v_exp_f32 v56, v56\n\t"
    "v_mfma_f32_16x16x32_bf16 v[40:43], a[124:127], a[44:47], v[40:43]\n\t"
    "v_exp_f32 v57, v57\n\t"
    "v_mfma_f32_16x16x32_bf16 v[216:219], a[128:131], v[72:75], v[216:219]\n\t"
    "v_exp_f32 v58, v58\n\t"
    "v_mfma_f32_16x16x32_bf16 v[216:219], a[132:135], v[84:87], v[216:219]\n\t"
    "v_exp_f32 v59, v59\n\t"
    "v_mfma_f32_16x16x32_bf16 v[216:219], a[136:139], v[96:99], v[216:219]\n\t"
    "ds_read_b64_tr_b16 a[158:159], v17 offset:30720\n\t"
    "v_cvt_pk_bf16_f32 v138, v56, v57\n\t"
    "v_mfma_f32_16x16x32_bf16 v[216:219], a[140:143], v[108:111], v[216:219]\n\t"
    "s_waitcnt lgkmcnt(3)\n\t"
    "v_cvt_pk_bf16_f32 v139, v58, v59\n\t"
    "v_mfma_f32_16x16x32_bf16 v[44:47], a[96:99], a[0:3], v[0:3]\n\t"
    "ds_read_b128 a[112:115], v12 offset:61440\n\t"
    "v_mfma_f32_16x16x32_bf16 v[44:47], a[100:103], a[4:7], v[44:47]\n\t"
    "buffer_store_dwordx4 a[216:219], v33, s[48:51], s69 offen offset:128 nt\n\t"
    "v_mfma_f32_16x16x32_bf16 v[44:47], a[104:107], a[8:11], v[44:47]\n\t"
    "ds_read_b128 a[116:119], v13 offset:61440\n\t"
    "v_mfma_f32_16x16x32_bf16 v[44:47], a[108:111], a[12:15], v[44:47]\n\t"
    "v_exp_f32 v60, v60\n\t"
    "v_mfma_f32_16x16x32_bf16 v[220:223], a[144:147], v[64:67], v[220:223]\n\t"
    "ds_read_b128 a[120:123], v14 offset:61440\n\t"
    "v_mfma_f32_16x16x32_bf16 v[220:223], a[148:151], v[76:79], v[220:223]\n\t"
    "s_waitcnt lgkmcnt(4)\n\t"
    "v_exp_f32 v61, v61\n\t"
    "v_mfma_f32_16x16x32_bf16 v[220:223], a[152:155], v[88:91], v[220:223]\n\t"
    "s_waitcnt lgkmcnt(3)\n\t"
    "ds_read_b128 a[124:127], v15 offset:61440\n\t"
    "v_cvt_pk_bf16_f32 v142, v60, v61\n\t"
    "v_mfma_f32_16x16x32_bf16 v[220:223], a[156:159], v[100:103], v[220:223]\n\t"
    "v_exp_f32 v62, v62\n\t"
    "v_mfma_f32_16x16x32_bf16 v[48:51], a[96:99], a[16:19], v[4:7]\n\t"
    "ds_read_b64_tr_b16 a[128:129], v18 offset:16384\n\t"
    "ds_read_b64_tr_b16 a[130:131], v18 offset:18432\n\t"
    "v_mfma_f32_16x16x32_bf16 v[48:51], a[100:103], a[20:23], v[48:51]\n\t"
    "v_exp_f32 v63, v63\n\t"
    "v_mfma_f32_16x16x32_bf16 v[48:51], a[104:107], a[24:27], v[48:51]\n\t"
    "v_cvt_pk_bf16_f32 v143, v62, v63\n\t"
    "v_mfma_f32_16x16x32_bf16 v[48:51], a[108:111], a[28:31], v[48:51]\n\t"
    "v_exp_f32 v40, v40\n\t"
    "v_mfma_f32_16x16x32_bf16 v[224:227], a[144:147], v[68:71], v[224:227]\n\t"
    "ds_read_b64_tr_b16 a[132:133], v18 offset:20480\n\t"
    "ds_read_b64_tr_b16 a[134:135], v18 offset:22528\n\t"
    "v_mfma_f32_16x16x32_bf16 v[224:227], a[148:151], v[80:83], v[224:227]\n\t"
    "v_exp_f32 v41, v41\n\t"
    "v_mfma_f32_16x16x32_bf16 v[224:227], a[152:155], v[92:95], v[224:227]\n\t"
    "v_exp_f32 v42, v42\n\t"
    "v_mfma_f32_16x16x32_bf16 v[224:227], a[156:159], v[104:107], v[224:227]\n\t"
    "v_exp_f32 v43, v43\n\t"
    "v_mfma_f32_16x16x32_bf16 v[52:55], a[96:99], a[32:35], v[8:11]\n\t"
    "ds_read_b64_tr_b16 a[136:137], v18 offset:24576\n\t"
    "v_cvt_pk_bf16_f32 v146, v40, v41\n\t"
    "v_mfma_f32_16x16x32_bf16 v[52:55], a[100:103], a[36:39], v[52:55]\n\t"
    "v_cvt_pk_bf16_f32 v147, v42, v43\n\t"
    "v_mfma_f32_16x16x32_bf16 v[52:55], a[104:107], a[40:43], v[52:55]\n\t"
    "ds_read_b64_tr_b16 a[138:139], v18 offset:26624\n\t"
    "ds_read_b64_tr_b16 a[140:141], v18 offset:28672\n\t"
    "v_mfma_f32_16x16x32_bf16 v[52:55], a[108:111], a[44:47], v[52:55]\n\t"
    "v_exp_f32 v44, v44\n\t"
    "v_mfma_f32_16x16x32_bf16 v[228:231], a[144:147], v[72:75], v[228:231]\n\t"
    "v_exp_f32 v45, v45\n\t"
    "v_mfma_f32_16x16x32_bf16 v[228:231], a[148:151], v[84:87], v[228:231]\n\t"
    "v_exp_f32 v46, v46\n\t"
    "v_mfma_f32_16x16x32_bf16 v[228:231], a[152:155], v[96:99], v[228:231]\n\t"
    "ds_read_b64_tr_b16 a[142:143], v18 offset:30720\n\t"
    "v_cvt_pk_bf16_f32 v148, v44, v45\n\t"
    "v_mfma_f32_16x16x32_bf16 v[228:231], a[156:159], v[108:111], v[228:231]\n\t"
    "s_waitcnt lgkmcnt(4)\n\t"
    "v_exp_f32 v47, v47\n\t"
    "v_mfma_f32_16x16x32_bf16 v[56:59], a[112:115], a[0:3], v[0:3]\n\t"
    "ds_read_b64_tr_b16 a[144:145], v19 offset:16384\n\t"
    "v_cvt_pk_bf16_f32 v149, v46, v47\n\t"
    "v_mfma_f32_16x16x32_bf16 v[56:59], a[116:119], a[4:7], v[56:59]\n\t"
    "v_exp_f32 v48, v48\n\t"
    "v_mfma_f32_16x16x32_bf16 v[56:59], a[120:123], a[8:11], v[56:59]\n\t"
    "ds_read_b64_tr_b16 a[146:147], v19 offset:18432\n\t"
    "ds_read_b64_tr_b16 a[148:149], v19 offset:20480\n\t"
    "v_mfma_f32_16x16x32_bf16 v[56:59], a[124:127], a[12:15], v[56:59]\n\t"
    "v_exp_f32 v49, v49\n\t"
    "v_mfma_f32_16x16x32_bf16 v[232:235], a[128:131], v[64:67], v[232:235]\n\t"
    "v_exp_f32 v50, v50\n\t"
    "v_mfma_f32_16x16x32_bf16 v[232:235], a[132:135], v[76:79], v[232:235]\n\t"
    "s_waitcnt lgkmcnt(4)\n\t"
    "v_exp_f32 v51, v51\n\t"
    "v_mfma_f32_16x16x32_bf16 v[232:235], a[136:139], v[88:91], v[232:235]\n\t"
    "s_waitcnt lgkmcnt(3)\n\t"
    "v_cvt_pk_bf16_f32 v152, v48, v49\n\t"
    "v_cvt_pk_bf16_f32 v153, v50, v51\n\t"
    "v_mfma_f32_16x16x32_bf16 v[232:235], a[140:143], v[100:103], v[232:235]\n\t"
    "v_exp_f32 v56, v56\n\t"
    "v_mfma_f32_16x16x32_bf16 v[60:63], a[112:115], a[16:19], v[4:7]\n\t"
    "ds_read_b64_tr_b16 a[150:151], v19 offset:22528\n\t"
    "ds_read_b64_tr_b16 a[152:153], v19 offset:24576\n\t"
    "v_mfma_f32_16x16x32_bf16 v[60:63], a[116:119], a[20:23], v[60:63]\n\t"
    "v_exp_f32 v57, v57\n\t"
    "v_mfma_f32_16x16x32_bf16 v[60:63], a[120:123], a[24:27], v[60:63]\n\t"
    "v_exp_f32 v58, v58\n\t"
    "v_mfma_f32_16x16x32_bf16 v[60:63], a[124:127], a[28:31], v[60:63]\n\t"
    "v_exp_f32 v59, v59\n\t"
    "v_mfma_f32_16x16x32_bf16 v[236:239], a[128:131], v[68:71], v[236:239]\n\t"
    "v_cvt_pk_bf16_f32 v150, v56, v57\n\t"
    "v_cvt_pk_bf16_f32 v151, v58, v59\n\t"
    "v_mfma_f32_16x16x32_bf16 v[236:239], a[132:135], v[80:83], v[236:239]\n\t"
    "v_exp_f32 v52, v52\n\t"
    "v_mfma_f32_16x16x32_bf16 v[236:239], a[136:139], v[92:95], v[236:239]\n\t"
    "ds_read_b64_tr_b16 a[154:155], v19 offset:26624\n\t"
    "ds_read_b64_tr_b16 a[156:157], v19 offset:28672\n\t"
    "v_mfma_f32_16x16x32_bf16 v[236:239], a[140:143], v[104:107], v[236:239]\n\t"
    "v_exp_f32 v60, v60\n\t"
    "v_mfma_f32_16x16x32_bf16 a[168:171], a[172:175], v[72:75], a[168:171]\n\t"
    "v_exp_f32 v61, v61\n\t"
    "v_mfma_f32_16x16x32_bf16 a[168:171], a[172:175], v[84:87], a[168:171]\n\t"
    "v_exp_f32 v62, v62\n\t"
    "v_mfma_f32_16x16x32_bf16 a[168:171], a[172:175], v[96:99], a[168:171]\n\t"
    "v_exp_f32 v63, v63\n\t"
    "v_mfma_f32_16x16x32_bf16 a[168:171], a[172:175], v[108:111], a[168:171]\n\t"
    "v_cvt_pk_bf16_f32 v154, v60, v61\n\t"
    "v_cvt_pk_bf16_f32 v155, v62, v63\n\t"
    "v_mfma_f32_16x16x32_bf16 v[40:43], a[112:115], a[32:35], v[8:11]\n\t"
    "ds_read_b64_tr_b16 a[158:159], v19 offset:30720\n\t"
    "v_mfma_f32_16x16x32_bf16 v[40:43], a[116:119], a[36:39], v[40:43]\n\t"
    "v_exp_f32 v53, v53\n\t"
    "v_mfma_f32_16x16x32_bf16 v[40:43], a[120:123], a[40:43], v[40:43]\n\t"
    "v_exp_f32 v54, v54\n\t"
    "v_cvt_pk_bf16_f32 v156, v52, v53\n\t"
    "v_mfma_f32_16x16x32_bf16 v[40:43], a[124:127], a[44:47], v[40:43]\n\t"
    "v_exp_f32 v55, v55\n\t"
    "v_mfma_f32_16x16x32_bf16 v[240:243], a[128:131], v[72:75], v[240:243]\n\t"
    "v_cvt_pk_bf16_f32 v157, v54, v55\n\t"
    "v_mfma_f32_16x16x32_bf16 v[240:243], a[132:135], v[84:87], v[240:243]\n\t"
    "v_mfma_f32_16x16x32_bf16 v[240:243], a[136:139], v[96:99], v[240:243]\n\t"
    "v_mfma_f32_16x16x32_bf16 v[240:243], a[140:143], v[108:111], v[240:243]\n\t"
    "s_waitcnt lgkmcnt(1)\n\t"
    "s_nop 0\n\t"
    "v_exp_f32 v40, v40\n\t"
    "v_mfma_f32_16x16x32_bf16 v[244:247], a[144:147], v[64:67], v[244:247]\n\t"
    "v_exp_f32 v41, v41\n\t"
    "v_mfma_f32_16x16x32_bf16 v[244:247], a[148:151], v[76:79], v[244:247]\n\t"
    "s_waitcnt vmcnt(8) lgkmcnt(0)\n\t"
    "v_exp_f32 v42, v42\n\t"
    "v_cvt_pk_bf16_f32 v158, v40, v41\n\t"
    "v_mfma_f32_16x16x32_bf16 v[244:247], a[152:155], v[88:91], v[244:247]\n\t"
    "s_barrier\n\t"
    "ds_read_b64_tr_b16 a[128:129], v16 offset:32768\n\t"
    "v_exp_f32 v43, v43\n\t"
    "v_mfma_f32_16x16x32_bf16 v[244:247], a[156:159], v[100:103], v[244:247]\n\t"
    "ds_read_b64_tr_b16 a[130:131], v16 offset:34816\n\t"
    "v_cvt_pk_bf16_f32 v159, v42, v43\n\t"
    "v_mfma_f32_16x16x32_bf16 v[248:251], a[144:147], v[68:71], v[248:251]\n\t"
    "ds_read_b64_tr_b16 a[132:133], v16 offset:36864\n\t"
    "ds_read_b64_tr_b16 a[134:135], v16 offset:38912\n\t"
    "v_mfma_f32_16x16x32_bf16 v[248:251], a[148:151], v[80:83], v[248:251]\n\t"
    "ds_read_b64_tr_b16 a[136:137], v16 offset:40960\n\t"
    "ds_read_b64_tr_b16 a[138:139], v16 offset:43008\n\t"
    "v_mfma_f32_16x16x32_bf16 v[248:251], a[152:155], v[92:95], v[248:251]\n\t"
    "ds_read_b64_tr_b16 a[96:97], v17 offset:32768\n\t"
    "ds_read_b64_tr_b16 a[140:141], v16 offset:45056\n\t"
    "v_mfma_f32_16x16x32_bf16 v[248:251], a[156:159], v[104:107], v[248:251]\n\t"
    "ds_read_b64_tr_b16 a[98:99], v17 offset:34816\n\t"
    "ds_read_b64_tr_b16 a[142:143], v16 offset:47104\n\t"
    "v_mfma_f32_16x16x32_bf16 v[252:255], a[144:147], v[72:75], v[252:255]\n\t"
    "ds_read_b64_tr_b16 a[100:101], v17 offset:36864\n\t"
    "ds_read_b64_tr_b16 a[102:103], v17 offset:38912\n\t"
    "v_mfma_f32_16x16x32_bf16 v[252:255], a[148:151], v[84:87], v[252:255]\n\t"
    "ds_read_b64_tr_b16 a[104:105], v17 offset:40960\n\t"
    "ds_read_b64_tr_b16 a[106:107], v17 offset:43008\n\t"
    "v_mfma_f32_16x16x32_bf16 v[252:255], a[152:155], v[96:99], v[252:255]\n\t"
    "ds_read_b64_tr_b16 a[108:109], v17 offset:45056\n\t"
    "ds_read_b64_tr_b16 a[110:111], v17 offset:47104\n\t"
    "v_mfma_f32_16x16x32_bf16 v[252:255], a[156:159], v[108:111], v[252:255]\n\t"
    "ds_read_b64_tr_b16 a[112:113], v18 offset:32768\n\t"
    "ds_read_b64_tr_b16 a[114:115], v18 offset:34816\n\t"
    "v_mfma_f32_16x16x32_bf16 a[160:163], a[172:175], v[112:115], a[160:163]\n\t"
    "ds_read_b64_tr_b16 a[116:117], v18 offset:36864\n\t"
    "ds_read_b64_tr_b16 a[118:119], v18 offset:38912\n\t"
    "v_mfma_f32_16x16x32_bf16 a[160:163], a[172:175], v[124:127], a[160:163]\n\t"
    "ds_read_b64_tr_b16 a[120:121], v18 offset:40960\n\t"
    "ds_read_b64_tr_b16 a[122:123], v18 offset:43008\n\t"
    "v_mfma_f32_16x16x32_bf16 a[160:163], a[172:175], v[136:139], a[160:163]\n\t"
    "ds_read_b64_tr_b16 a[124:125], v18 offset:45056\n\t"
    "ds_read_b64_tr_b16 a[126:127], v18 offset:47104\n\t"
    "v_mfma_f32_16x16x32_bf16 a[160:163], a[172:175], v[148:151], a[160:163]\n\t"
    "s_waitcnt lgkmcnt(14)\n\t"
    "ds_read_b64_tr_b16 a[144:145], v19 offset:32768\n\t"
    "ds_read_b64_tr_b16 a[146:147], v19 offset:34816\n\t"
    "v_mfma_f32_16x16x32_bf16 v[160:163], a[128:131], v[112:115], v[160:163]\n\t"
    "ds_read_b64_tr_b16 a[148:149], v19 offset:36864\n\t"
    "ds_read_b64_tr_b16 a[150:151], v19 offset:38912\n\t"
    "v_mfma_f32_16x16x32_bf16 v[160:163], a[132:135], v[124:127], v[160:163]\n\t"
    "ds_read_b64_tr_b16 a[152:153], v19 offset:40960\n\t"
    "ds_read_b64_tr_b16 a[154:155], v19 offset:43008\n\t"
    "v_mfma_f32_16x16x32_bf16 v[160:163], a[136:139], v[136:139], v[160:163]\n\t"
    "ds_read_b64_tr_b16 a[156:157], v19 offset:45056\n\t"
    "ds_read_b64_tr_b16 a[158:159], v19 offset:47104\n\t"
    "v_mfma_f32_16x16x32_bf16 v[160:163], a[140:143], v[148:151], v[160:163]\n\t"
    "buffer_store_dwordx4 a[220:223], v33, s[48:51], s69 offen offset:2176 nt\n\t"
    "v_mfma_f32_16x16x32_bf16 v[172:175], a[96:99], v[112:115], v[172:175]\n\t"
    "s_waitcnt lgkmcnt(12)\n\t"
    "v_accvgpr_read_b32 v35, a160\n\t"
    "v_mfma_f32_16x16x32_bf16 v[172:175], a[100:103], v[124:127], v[172:175]\n\t"
    "v_cmp_nlt_f32 s[86:87], v35, s54\n\t"
    "v_cmp_ngt_f32 s[92:93], s99, v35\n\t"
    "v_mfma_f32_16x16x32_bf16 v[172:175], a[104:107], v[136:139], v[172:175]\n\t"
    "v_rcp_f32 v35, v35\n\t"
    "v_mfma_f32_16x16x32_bf16 v[172:175], a[108:111], v[148:151], v[172:175]\n\t"
    "s_cmp_lg_u64 s[86:87], 0\n\ts_cselect_b32 s81, 4, 0\n\ts_or_b32 s80, s80, s81\n\t"
    "v_mfma_f32_16x16x32_bf16 v[184:187], a[112:115], v[112:115], v[184:187]\n\t"
    "s_cmp_lg_u64 s[92:93], 0\n\ts_cselect_b32 s81, 32, 0\n\ts_or_b32 s80, s80, s81\n\t"
    "v_mul_f32 v160, v35, v160\n\t"
    "v_mul_f32 v161, v35, v161\n\t"
    "v_mul_f32 v162, v35, v162\n\t"
    "v_mfma_f32_16x16x32_bf16 v[184:187], a[116:119], v[124:127], v[184:187]\n\t"
    "s_waitcnt lgkmcnt(2)\n\t"
    "v_mul_f32 v163, v35, v163\n\t"
    "v_cvt_pk_bf16_f32 v160, v160, v161\n\t"
    "v_cmp_ngt_f32 s[92:93], s99, v35\n\t"
    "v_mfma_f32_16x16x32_bf16 v[184:187], a[120:123], v[136:139], v[184:187]\n\t"
    "v_cvt_pk_bf16_f32 v161, v162, v163\n\t"
    "v_mfma_f32_16x16x32_bf16 v[184:187], a[124:127], v[148:151], v[184:187]\n\t"
    "s_cmp_lg_u64 s[92:93], 0\n\ts_cselect_b32 s81, 4, 0\n\ts_or_b32 s80, s80, s81\n\t"
    "ds_write_b64 v32, v[160:161] offset:0\n\t"
    "v_mfma_f32_16x16x32_bf16 v[196:199], a[144:147], v[112:115], v[196:199]\n\t"
    "v_mul_f32 v172, v35, v172\n\t"
    "v_mul_f32 v173, v35, v173\n\t"
    "v_mul_f32 v174, v35, v174\n\t"
    "v_mfma_f32_16x16x32_bf16 v[196:199], a[148:151], v[124:127], v[196:199]\n\t"
    "v_mul_f32 v175, v35, v175\n\t"
    "v_cvt_pk_bf16_f32 v172, v172, v173\n\t"
    "v_mfma_f32_16x16x32_bf16 v[196:199], a[152:155], v[136:139], v[196:199]\n\t"
    "s_waitcnt lgkmcnt(1)\n\t"
    "v_cvt_pk_bf16_f32 v173, v174, v175\n\t"
    "v_mfma_f32_16x16x32_bf16 v[196:199], a[156:159], v[148:151], v[196:199]\n\t"
    "ds_write_b64 v32, v[172:173] offset:32\n\t"
    "v_mfma_f32_16x16x32_bf16 a[164:167], a[172:175], v[116:119], a[164:167]\n\t"
    "v_mul_f32 v184, v35, v184\n\t"
    "v_mul_f32 v185, v35, v185\n\t"
    "v_mul_f32 v186, v35, v186\n\t"
    "v_mfma_f32_16x16x32_bf16 a[164:167], a[172:175], v[128:131], a[164:167]\n\t"
    "v_mul_f32 v187, v35, v187\n\t"
    "v_cvt_pk_bf16_f32 v184, v184, v185\n\t"
    "v_mfma_f32_16x16x32_bf16 a[164:167], a[172:175], v[140:143], a[164:167]\n\t"
    "v_cvt_pk_bf16_f32 v185, v186, v187\n\t"
    "v_mfma_f32_16x16x32_bf16 a[164:167], a[172:175], v[152:155], a[164:167]\n\t"
    "ds_write_b64 v32, v[184:185] offset:64\n\t"
    "v_mfma_f32_16x16x32_bf16 v[164:167], a[128:131], v[116:119], v[164:167]\n\t"
    "v_mul_f32 v196, v35, v196\n\t"
    "v_mul_f32 v197, v35, v197\n\t"
    "v_mul_f32 v198, v35, v198\n\t"
    "v_mul_f32 v199, v35, v199\n\t"
    "v_mfma_f32_16x16x32_bf16 v[164:167], a[132:135], v[128:131], v[164:167]\n\t"
    "v_cvt_pk_bf16_f32 v196, v196, v197\n\t"
    "v_cvt_pk_bf16_f32 v197, v198, v199\n\t"
    "v_mfma_f32_16x16x32_bf16 v[164:167], a[136:139], v[140:143], v[164:167]\n\t"
    "ds_write_b64 v32, v[196:197] offset:96\n\t"
    "v_mfma_f32_16x16x32_bf16 v[164:167], a[140:143], v[152:155], v[164:167]\n\t"
    "ds_read_b128 a[176:179], v34 offset:0\n\t"
    "v_mfma_f32_16x16x32_bf16 v[176:179], a[96:99], v[116:119], v[176:179]\n\t"
    "ds_read_b128 a[180:183], v34 offset:1152\n\t"
    "v_accvgpr_read_b32 v36, a164\n\t"
    "v_mfma_f32_16x16x32_bf16 v[176:179], a[100:103], v[128:131], v[176:179]\n\t"
    "v_cmp_nlt_f32 s[88:89], v36, s54\n\t"
    "v_cmp_ngt_f32 s[92:93], s99, v36\n\t"
    "v_rcp_f32 v36, v36\n\t"
    "v_mfma_f32_16x16x32_bf16 v[176:179], a[104:107], v[140:143], v[176:179]\n\t"
    "v_mfma_f32_16x16x32_bf16 v[176:179], a[108:111], v[152:155], v[176:179]\n\t"
    "s_cmp_lg_u64 s[88:89], 0\n\ts_cselect_b32 s81, 4, 0\n\ts_or_b32 s80, s80, s81\n\t"
    "v_mfma_f32_16x16x32_bf16 v[188:191], a[112:115], v[116:119], v[188:191]\n\t"
    "s_cmp_lg_u64 s[92:93], 0\n\ts_cselect_b32 s81, 32, 0\n\ts_or_b32 s80, s80, s81\n\t"
    "v_mul_f32 v164, v36, v164\n\t"
    "v_mul_f32 v165, v36, v165\n\t"
    "v_mul_f32 v166, v36, v166\n\t"
    "v_mul_f32 v167, v36, v167\n\t"
    "v_mfma_f32_16x16x32_bf16 v[188:191], a[116:119], v[128:131], v[188:191]\n\t"
    "v_cvt_pk_bf16_f32 v164, v164, v165\n\t"
    "v_cvt_pk_bf16_f32 v165, v166, v167\n\t"
    "v_cmp_ngt_f32 s[92:93], s99, v36\n\t"
    "v_mfma_f32_16x16x32_bf16 v[188:191], a[120:123], v[140:143], v[188:191]\n\t"
    "ds_write_b64 v32, v[164:165] offset:2304\n\t"
    "v_mfma_f32_16x16x32_bf16 v[188:191], a[124:127], v[152:155], v[188:191]\n\t"
    "s_waitcnt lgkmcnt(2)\n\t"
    "s_cmp_lg_u64 s[92:93], 0\n\ts_cselect_b32 s81, 4, 0\n\ts_or_b32 s80, s80, s81\n\t"
    "buffer_store_dwordx4 a[176:179], v33, s[48:51], s70 offen offset:0 nt\n\t"
    "v_mfma_f32_16x16x32_bf16 v[200:203], a[144:147], v[116:119], v[200:203]\n\t"
    "v_mul_f32 v176, v36, v176\n\t"
    "v_mul_f32 v177, v36, v177\n\t"
    "v_mul_f32 v178, v36, v178\n\t"
    "v_mfma_f32_16x16x32_bf16 v[200:203], a[148:151], v[128:131], v[200:203]\n\t"
    "v_mul_f32 v179, v36, v179\n\t"
    "v_cvt_pk_bf16_f32 v176, v176, v177\n\t"
    "v_mfma_f32_16x16x32_bf16 v[200:203], a[152:155], v[140:143], v[200:203]\n\t"
    "s_waitcnt lgkmcnt(1)\n\t"
    "buffer_store_dwordx4 a[180:183], v33, s[48:51], s70 offen offset:2048 nt\n\t"
    "v_mfma_f32_16x16x32_bf16 v[200:203], a[156:159], v[152:155], v[200:203]\n\t"
    "v_cvt_pk_bf16_f32 v177, v178, v179\n\t"
    "v_mfma_f32_16x16x32_bf16 a[168:171], a[172:175], v[120:123], a[168:171]\n\t"
    "v_mul_f32 v188, v36, v188\n\t"
    "v_mul_f32 v189, v36, v189\n\t"
    "v_mul_f32 v190, v36, v190\n\t"
    "v_mul_f32 v191, v36, v191\n\t"
    "v_mfma_f32_16x16x32_bf16 a[168:171], a[172:175], v[132:135], a[168:171]\n\t"
    "v_cvt_pk_bf16_f32 v188, v188, v189\n\t"
    "v_cvt_pk_bf16_f32 v189, v190, v191\n\t"
    "v_mfma_f32_16x16x32_bf16 a[168:171], a[172:175], v[144:147], a[168:171]\n\t"
    "ds_write_b64 v32, v[176:177] offset:2336\n\t"
    "v_mfma_f32_16x16x32_bf16 a[168:171], a[172:175], v[156:159], a[168:171]\n\t"
    "ds_write_b64 v32, v[188:189] offset:2368\n\t"
    "v_mfma_f32_16x16x32_bf16 v[168:171], a[128:131], v[120:123], v[168:171]\n\t"
    "s_waitcnt vmcnt(5)\n\t"
    "v_mul_f32 v200, v36, v200\n\t"
    "v_mul_f32 v201, v36, v201\n\t"
    "v_mul_f32 v202, v36, v202\n\t"
    "v_mul_f32 v203, v36, v203\n\t"
    "v_mfma_f32_16x16x32_bf16 v[168:171], a[132:135], v[132:135], v[168:171]\n\t"
    "s_barrier\n\t"
    "ds_read_b64_tr_b16 a[128:129], v16 offset:49152\n\t"
    "v_cvt_pk_bf16_f32 v200, v200, v201\n\t"
    "v_cvt_pk_bf16_f32 v201, v202, v203\n\t"
    "v_mfma_f32_16x16x32_bf16 v[168:171], a[136:139], v[144:147], v[168:171]\n\t"
    "ds_read_b64_tr_b16 a[130:131], v16 offset:51200\n\t"
    "ds_read_b64_tr_b16 a[132:133], v16 offset:53248\n\t"
    "v_mfma_f32_16x16x32_bf16 v[168:171], a[140:143], v[156:159], v[168:171]\n\t"
    "ds_read_b64_tr_b16 a[134:135], v16 offset:55296\n\t"
    "ds_read_b64_tr_b16 a[136:137], v16 offset:57344\n\t"
    "v_mfma_f32_16x16x32_bf16 v[180:183], a[96:99], v[120:123], v[180:183]\n\t"
    "ds_write_b64 v32, v[200:201] offset:2400\n\t"
    "v_accvgpr_read_b32 v37, a168\n\t"
    "v_mfma_f32_16x16x32_bf16 v[180:183], a[100:103], v[132:135], v[180:183]\n\t"
    "v_cmp_nlt_f32 s[90:91], v37, s54\n\t"
    "v_cmp_ngt_f32 s[92:93], s99, v37\n\t"
    "v_rcp_f32 v37, v37\n\t"
    "v_mfma_f32_16x16x32_bf16 v[180:183], a[104:107], v[144:147], v[180:183]\n\t"
    "ds_read_b64_tr_b16 a[138:139], v16 offset:59392\n\t"
    "ds_read_b64_tr_b16 a[140:141], v16 offset:61440\n\t"
    "v_mfma_f32_16x16x32_bf16 v[180:183], a[108:111], v[156:159], v[180:183]\n\t"
    "s_cmp_lg_u64 s[90:91], 0\n\ts_cselect_b32 s81, 4, 0\n\ts_or_b32 s80, s80, s81\n\t"
    "ds_read_b64_tr_b16 a[142:143], v16 offset:63488\n\t"
    "ds_read_b128 a[184:187], v34 offset:2304\n\t"
    "v_mfma_f32_16x16x32_bf16 v[192:195], a[112:115], v[120:123], v[192:195]\n\t"
    "s_cmp_lg_u64 s[92:93], 0\n\ts_cselect_b32 s81, 32, 0\n\ts_or_b32 s80, s80, s81\n\t"
    "ds_read_b128 a[188:191], v34 offset:3456\n\t"
    "v_mul_f32 v168, v37, v168\n\t"
    "v_mul_f32 v169, v37, v169\n\t"
    "v_mul_f32 v170, v37, v170\n\t"
    "v_mfma_f32_16x16x32_bf16 v[192:195], a[116:119], v[132:135], v[192:195]\n\t"
    "ds_read_b64_tr_b16 a[96:97], v17 offset:49152\n\t"
    "v_mul_f32 v171, v37, v171\n\t"
    "v_cvt_pk_bf16_f32 v168, v168, v169\n\t"
    "v_cmp_ngt_f32 s[92:93], s99, v37\n\t"
    "v_mfma_f32_16x16x32_bf16 v[192:195], a[120:123], v[144:147], v[192:195]\n\t"
    "ds_read_b64_tr_b16 a[98:99], v17 offset:51200\n\t"
    "v_cvt_pk_bf16_f32 v169, v170, v171\n\t"
    "v_mfma_f32_16x16x32_bf16 v[192:195], a[124:127], v[156:159], v[192:195]\n\t"
    "s_cmp_lg_u64 s[92:93], 0\n\ts_cselect_b32 s81, 4, 0\n\ts_or_b32 s80, s80, s81\n\t"
    "ds_read_b64_tr_b16 a[100:101], v17 offset:53248\n\t"
    "ds_read_b64_tr_b16 a[102:103], v17 offset:55296\n\t"
    "v_mfma_f32_16x16x32_bf16 v[204:207], a[144:147], v[120:123], v[204:207]\n\t"
    "ds_read_b64_tr_b16 a[104:105], v17 offset:57344\n\t"
    "v_mul_f32 v180, v37, v180\n\t"
    "v_mul_f32 v181, v37, v181\n\t"
    "v_mul_f32 v182, v37, v182\n\t"
    "v_mul_f32 v183, v37, v183\n\t"
    "v_mfma_f32_16x16x32_bf16 v[204:207], a[148:151], v[132:135], v[204:207]\n\t"
    "ds_read_b64_tr_b16 a[106:107], v17 offset:59392\n\t"
    "v_cvt_pk_bf16_f32 v180, v180, v181\n\t"
    "v_cvt_pk_bf16_f32 v181, v182, v183\n\t"
    "v_mfma_f32_16x16x32_bf16 v[204:207], a[152:155], v[144:147], v[204:207]\n\t"
    "ds_read_b64_tr_b16 a[108:109], v17 offset:61440\n\t"
    "ds_read_b64_tr_b16 a[110:111], v17 offset:63488\n\t"
    "v_mfma_f32_16x16x32_bf16 v[204:207], a[156:159], v[156:159], v[204:207]\n\t"
    "s_waitcnt lgkmcnt(9)\n\t"
    "ds_read_b64_tr_b16 a[112:113], v18 offset:49152\n\t"
    "ds_read_b64_tr_b16 a[114:115], v18 offset:51200\n\t"
    "v_mfma_f32_16x16x32_bf16 v[208:211], a[128:131], v[112:115], v[208:211]\n\t"
    "ds_read_b64_tr_b16 a[116:117], v18 offset:53248\n\t"
    "ds_read_b64_tr_b16 a[118:119], v18 offset:55296\n\t"
    "ds_write_b64 v32, v[168:169] offset:4608\n\t"
    "v_mul_f32 v192, v37, v192\n\t"
    "v_mul_f32 v193, v37, v193\n\t"
    "v_mul_f32 v194, v37, v194\n\t"
    "v_mfma_f32_16x16x32_bf16 v[208:211], a[132:135], v[124:127], v[208:211]\n\t"
    "s_waitcnt lgkmcnt(12)\n\t"
    "ds_read_b64_tr_b16 a[120:121], v18 offset:57344\n\t"
    "ds_read_b64_tr_b16 a[122:123], v18 offset:59392\n\t"
    "buffer_store_dwordx4 a[184:187], v33, s[48:51], s71 offen offset:0 nt\n\t"
    "buffer_store_dwordx4 a[188:191], v33, s[48:51], s71 offen offset:2048 nt\n\t"
    "v_mfma_f32_16x16x32_bf16 v[208:211], a[136:139], v[136:139], v[208:211]\n\t"
    "ds_read_b64_tr_b16 a[124:125], v18 offset:61440\n\t"
    "ds_read_b64_tr_b16 a[126:127], v18 offset:63488\n\t"
    "v_mul_f32 v195, v37, v195\n\t"
    "v_mfma_f32_16x16x32_bf16 v[208:211], a[140:143], v[148:151], v[208:211]\n\t"
    "s_waitcnt lgkmcnt(13)\n\t"
    "ds_read_b64_tr_b16 a[144:145], v19 offset:49152\n\t"
    "ds_read_b64_tr_b16 a[146:147], v19 offset:51200\n\t"
    "v_cvt_pk_bf16_f32 v192, v192, v193\n\t"
    "v_cvt_pk_bf16_f32 v193, v194, v195\n\t"
    "v_mfma_f32_16x16x32_bf16 v[220:223], a[96:99], v[112:115], v[220:223]\n\t"
    "ds_read_b64_tr_b16 a[148:149], v19 offset:53248\n\t"
    "ds_read_b64_tr_b16 a[150:151], v19 offset:55296\n\t"
    "ds_write_b64 v32, v[180:181] offset:4640\n\t"
    "v_mfma_f32_16x16x32_bf16 v[220:223], a[100:103], v[124:127], v[220:223]\n\t"
    "s_waitcnt lgkmcnt(15)\n\t"
    "ds_read_b64_tr_b16 a[152:153], v19 offset:57344\n\t"
    "ds_read_b64_tr_b16 a[154:155], v19 offset:59392\n\t"
    "v_mfma_f32_16x16x32_bf16 v[220:223], a[104:107], v[136:139], v[220:223]\n\t"
    "s_waitcnt lgkmcnt(15)\n\t"
    "ds_read_b64_tr_b16 a[156:157], v19 offset:61440\n\t"
    "ds_read_b64_tr_b16 a[158:159], v19 offset:63488\n\t"
    "v_mul_f32 v204, v37, v204\n\t"
    "v_mfma_f32_16x16x32_bf16 v[220:223], a[108:111], v[148:151], v[220:223]\n\t"
    "s_waitcnt lgkmcnt(15)\n\t"
    "ds_write_b64 v32, v[192:193] offset:4672\n\t"
    "v_mfma_f32_16x16x32_bf16 v[232:235], a[112:115], v[112:115], v[232:235]\n\t"
    "s_waitcnt lgkmcnt(14)\n\t"
    "v_mul_f32 v205, v37, v205\n\t"
    "v_mul_f32 v206, v37, v206\n\t"
    "v_mul_f32 v207, v37, v207\n\t"
    "v_mfma_f32_16x16x32_bf16 v[232:235], a[116:119], v[124:127], v[232:235]\n\t"
    "s_waitcnt lgkmcnt(12)\n\t"
    "v_cvt_pk_bf16_f32 v204, v204, v205\n\t"
    "v_cvt_pk_bf16_f32 v205, v206, v207\n\t"
    "v_mul_f32 v208, v35, v208\n\t"
    "v_mfma_f32_16x16x32_bf16 v[232:235], a[120:123], v[136:139], v[232:235]\n\t"
    "s_waitcnt lgkmcnt(10)\n\t"
    "ds_write_b64 v32, v[204:205] offset:4704\n\t"
    "v_mfma_f32_16x16x32_bf16 v[232:235], a[124:127], v[148:151], v[232:235]\n\t"
    "s_waitcnt lgkmcnt(9)\n\t"
    "v_mul_f32 v209, v35, v209\n\t"
    "v_mul_f32 v210, v35, v210\n\t"
    "v_mfma_f32_16x16x32_bf16 v[244:247], a[144:147], v[112:115], v[244:247]\n\t"
    "s_waitcnt lgkmcnt(6)\n\t"
    "ds_read_b128 a[192:195], v34 offset:4608\n\t"
    "v_mul_f32 v211, v35, v211\n\t"
    "v_cvt_pk_bf16_f32 v208, v208, v209\n\t"
    "v_mfma_f32_16x16x32_bf16 v[244:247], a[148:151], v[124:127], v[244:247]\n\t"
    "s_waitcnt lgkmcnt(5)\n\t"
    "ds_read_b128 a[196:199], v34 offset:5760\n\t"
    "v_cvt_pk_bf16_f32 v209, v210, v211\n\t"
    "v_mul_f32 v220, v35, v220\n\t"
    "v_mfma_f32_16x16x32_bf16 v[244:247], a[152:155], v[136:139], v[244:247]\n\t"
    "s_waitcnt lgkmcnt(4)\n\t"
    "ds_write_b64 v32, v[208:209] offset:0\n\t"
    "v_mfma_f32_16x16x32_bf16 v[244:247], a[156:159], v[148:151], v[244:247]\n\t"
    "v_mul_f32 v221, v35, v221\n\t"
    "v_mul_f32 v222, v35, v222\n\t"
    "v_mfma_f32_16x16x32_bf16 v[212:215], a[128:131], v[116:119], v[212:215]\n\t"
    "v_mul_f32 v223, v35, v223\n\t"
    "v_cvt_pk_bf16_f32 v220, v220, v221\n\t"
    "v_mul_f32 v232, v35, v232\n\t"
    "v_mfma_f32_16x16x32_bf16 v[212:215], a[132:135], v[128:131], v[212:215]\n\t"
    "v_cvt_pk_bf16_f32 v221, v222, v223\n\t"
    "v_mul_f32 v233, v35, v233\n\t"
    "v_mul_f32 v234, v35, v234\n\t"
    "v_mfma_f32_16x16x32_bf16 v[212:215], a[136:139], v[140:143], v[212:215]\n\t"
    "ds_write_b64 v32, v[220:221] offset:32\n\t"
    "v_mfma_f32_16x16x32_bf16 v[212:215], a[140:143], v[152:155], v[212:215]\n\t"
    "v_mul_f32 v235, v35, v235\n\t"
    "v_cvt_pk_bf16_f32 v232, v232, v233\n\t"
    "v_mfma_f32_16x16x32_bf16 v[224:227], a[96:99], v[116:119], v[224:227]\n\t"
    "v_cvt_pk_bf16_f32 v233, v234, v235\n\t"
    "v_mul_f32 v244, v35, v244\n\t"
    "v_mul_f32 v245, v35, v245\n\t"
    "v_mul_f32 v246, v35, v246\n\t"
    "v_mul_f32 v247, v35, v247\n\t"
    "v_mfma_f32_16x16x32_bf16 v[224:227], a[100:103], v[128:131], v[224:227]\n\t"
    "s_waitcnt lgkmcnt(2)\n\t"
    "buffer_store_dwordx4 a[192:195], v33, s[48:51], s72 offen offset:0 nt\n\t"
    "buffer_store_dwordx4 a[196:199], v33, s[48:51], s72 offen offset:2048 nt\n\t"
    "v_mfma_f32_16x16x32_bf16 v[224:227], a[104:107], v[140:143], v[224:227]\n\t"
    "v_cvt_pk_bf16_f32 v244, v244, v245\n\t"
    "v_cvt_pk_bf16_f32 v245, v246, v247\n\t"
    "v_mfma_f32_16x16x32_bf16 v[224:227], a[108:111], v[152:155], v[224:227]\n\t"
    "ds_write_b64 v32, v[232:233] offset:64\n\t"
    "v_mfma_f32_16x16x32_bf16 v[236:239], a[112:115], v[116:119], v[236:239]\n\t"
    "v_mul_f32 v212, v36, v212\n\t"
    "v_mul_f32 v213, v36, v213\n\t"
    "v_mfma_f32_16x16x32_bf16 v[236:239], a[116:119], v[128:131], v[236:239]\n\t"
    "ds_write_b64 v32, v[244:245] offset:96\n\t"
    "v_mfma_f32_16x16x32_bf16 v[236:239], a[120:123], v[140:143], v[236:239]\n\t"
    "v_mul_f32 v214, v36, v214\n\t"
    "v_mul_f32 v215, v36, v215\n\t"
    "v_cvt_pk_bf16_f32 v212, v212, v213\n\t"
    "v_mfma_f32_16x16x32_bf16 v[236:239], a[124:127], v[152:155], v[236:239]\n\t"
    "ds_read_b128 a[200:203], v34 offset:0\n\t"
    "v_cvt_pk_bf16_f32 v213, v214, v215\n\t"
    "v_mfma_f32_16x16x32_bf16 v[248:251], a[144:147], v[116:119], v[248:251]\n\t"
    "ds_read_b128 a[204:207], v34 offset:1152\n\t"
    "v_mul_f32 v224, v36, v224\n\t"
    "v_mul_f32 v225, v36, v225\n\t"
    "v_mfma_f32_16x16x32_bf16 v[248:251], a[148:151], v[128:131], v[248:251]\n\t"
    "ds_write_b64 v32, v[212:213] offset:2304\n\t"
    "v_mfma_f32_16x16x32_bf16 v[248:251], a[152:155], v[140:143], v[248:251]\n\t"
    "v_mul_f32 v226, v36, v226\n\t"
    "v_mul_f32 v227, v36, v227\n\t"
    "v_mfma_f32_16x16x32_bf16 v[248:251], a[156:159], v[152:155], v[248:251]\n\t"
    "v_cvt_pk_bf16_f32 v224, v224, v225\n\t"
    "v_cvt_pk_bf16_f32 v225, v226, v227\n\t"
    "v_mfma_f32_16x16x32_bf16 v[216:219], a[128:131], v[120:123], v[216:219]\n\t"
    "ds_write_b64 v32, v[224:225] offset:2336\n\t"
    "v_mfma_f32_16x16x32_bf16 v[216:219], a[132:135], v[132:135], v[216:219]\n\t"
    "v_mul_f32 v236, v36, v236\n\t"
    "v_mul_f32 v237, v36, v237\n\t"
    "v_mul_f32 v238, v36, v238\n\t"
    "v_mfma_f32_16x16x32_bf16 v[216:219], a[136:139], v[144:147], v[216:219]\n\t"
    "v_mul_f32 v239, v36, v239\n\t"
    "v_cvt_pk_bf16_f32 v236, v236, v237\n\t"
    "v_mfma_f32_16x16x32_bf16 v[216:219], a[140:143], v[156:159], v[216:219]\n\t"
    "v_cvt_pk_bf16_f32 v237, v238, v239\n\t"
    "v_mfma_f32_16x16x32_bf16 v[228:231], a[96:99], v[120:123], v[228:231]\n\t"
    "s_waitcnt lgkmcnt(2)\n\t"
    "buffer_store_dwordx4 a[200:203], v33, s[48:51], s70 offen offset:128 nt\n\t"
    "v_mul_f32 v248, v36, v248\n\t"
    "v_mfma_f32_16x16x32_bf16 v[228:231], a[100:103], v[132:135], v[228:231]\n\t"
    "buffer_store_dwordx4 a[204:207], v33, s[48:51], s70 offen offset:2176 nt\n\t"
    "v_mfma_f32_16x16x32_bf16 v[228:231], a[104:107], v[144:147], v[228:231]\n\t"
    "v_mul_f32 v249, v36, v249\n\t"
    "v_mul_f32 v250, v36, v250\n\t"
    "v_mfma_f32_16x16x32_bf16 v[228:231], a[108:111], v[156:159], v[228:231]\n\t"
    "v_mul_f32 v251, v36, v251\n\t"
    "v_cvt_pk_bf16_f32 v248, v248, v249\n\t"
    "v_mfma_f32_16x16x32_bf16 v[240:243], a[112:115], v[120:123], v[240:243]\n\t"
    "ds_write_b64 v32, v[236:237] offset:2368\n\t"
    "v_cvt_pk_bf16_f32 v249, v250, v251\n\t"
    "v_mfma_f32_16x16x32_bf16 v[240:243], a[116:119], v[132:135], v[240:243]\n\t"
    "ds_write_b64 v32, v[248:249] offset:2400\n\t"
    "v_mfma_f32_16x16x32_bf16 v[240:243], a[120:123], v[144:147], v[240:243]\n\t"
    "v_mul_f32 v216, v37, v216\n\t"
    "v_mul_f32 v217, v37, v217\n\t"
    "v_mfma_f32_16x16x32_bf16 v[240:243], a[124:127], v[156:159], v[240:243]\n\t"
    "ds_read_b128 a[208:211], v34 offset:2304\n\t"
    "v_mul_f32 v218, v37, v218\n\t"
    "v_mfma_f32_16x16x32_bf16 v[252:255], a[144:147], v[120:123], v[252:255]\n\t"
    "ds_read_b128 a[212:215], v34 offset:3456\n\t"
    "v_mul_f32 v219, v37, v219\n\t"
    "v_cvt_pk_bf16_f32 v216, v216, v217\n\t"
    "v_mfma_f32_16x16x32_bf16 v[252:255], a[148:151], v[132:135], v[252:255]\n\t"
    "v_cvt_pk_bf16_f32 v217, v218, v219\n\t"
    "v_mul_f32 v228, v37, v228\n\t"
    "v_mfma_f32_16x16x32_bf16 v[252:255], a[152:155], v[144:147], v[252:255]\n\t"
    "ds_write_b64 v32, v[216:217] offset:4608\n\t"
    "v_mfma_f32_16x16x32_bf16 v[252:255], a[156:159], v[156:159], v[252:255]\n\t"
    "s_waitcnt lgkmcnt(1)\n\t"
    "s_waitcnt lgkmcnt(2)\n\t"
    "v_mul_f32 v229, v37, v229\n\t"
    "v_mul_f32 v230, v37, v230\n\t"
    "v_mul_f32 v231, v37, v231\n\t"
    "v_cvt_pk_bf16_f32 v228, v228, v229\n\t"
    "v_mul_f32 v240, v37, v240\n\t"
    "v_mul_f32 v241, v37, v241\n\t"
    "v_mul_f32 v242, v37, v242\n\t"
    "v_mul_f32 v243, v37, v243\n\t"
    "v_cvt_pk_bf16_f32 v229, v230, v231\n\t"
    "v_cvt_pk_bf16_f32 v240, v240, v241\n\t"
    "v_cvt_pk_bf16_f32 v241, v242, v243\n\t"
    "ds_write_b64 v32, v[228:229] offset:4640\n\t"
    "ds_write_b64 v32, v[240:241] offset:4672\n\t"
    "buffer_store_dwordx4 a[208:211], v33, s[48:51], s71 offen offset:128 nt\n\t"
    "v_mul_f32 v252, v37, v252\n\t"
    "v_mul_f32 v253, v37, v253\n\t"
    "v_mul_f32 v254, v37, v254\n\t"
    "v_mul_f32 v255, v37, v255\n\t"
    "buffer_store_dwordx4 a[212:215], v33, s[48:51], s71 offen offset:2176 nt\n\t"
    "v_cvt_pk_bf16_f32 v252, v252, v253\n\t"
    "v_cvt_pk_bf16_f32 v253, v254, v255\n\t"
    "ds_write_b64 v32, v[252:253] offset:4704\n\t"
    "ds_read_b128 a[216:219], v34 offset:4608\n\t"
    "ds_read_b128 a[220:223], v34 offset:5760\n\t"
    "s_waitcnt lgkmcnt(1)\n\t"
    "buffer_store_dwordx4 a[216:219], v33, s[48:51], s72 offen offset:128 nt\n\t"
    "s_waitcnt lgkmcnt(0)\n\t"
    "buffer_store_dwordx4 a[220:223], v33, s[48:51], s72 offen offset:2176 nt\n\t"
    "s_mov_b32 %0, s80\n\t"
    "L_end_%=:\n\t"

    : "=&s"(flag)
    : "s"(__builtin_amdgcn_kernarg_segment_ptr()), "s"(bid), "s"(wv), "s"((unsigned)(size_t)lds)
    : "v0", "v1", "v2", "v3", "v4", "v5", "v6", "v7", "v8", "v9", "v10", "v11", "v12", "v13", "v14", "v15", "v16", "v17", "v18", "v19", "v20", "v21", "v22", "v23", "v24", "v25", "v26", "v27", "v28", "v29", "v30", "v31", "v32", "v33", "v34", "v35", "v36", "v37", "v38", "v39", "v40", "v41", "v42", "v43", "v44", "v45", "v46", "v47", "v48", "v49", "v50", "v51", "v52", "v53", "v54", "v55", "v56", "v57", "v58", "v59", "v60", "v61", "v62", "v63", "v64", "v65", "v66", "v67", "v68", "v69", "v70", "v71", "v72", "v73", "v74", "v75", "v76", "v77", "v78", "v79", "v80", "v81", "v82", "v83", "v84", "v85", "v86", "v87", "v88", "v89", "v90", "v91", "v92", "v93", "v94", "v95", "v96", "v97", "v98", "v99", "v100", "v101", "v102", "v103", "v104", "v105", "v106", "v107", "v108", "v109", "v110", "v111", "v112", "v113", "v114", "v115", "v116", "v117", "v118", "v119", "v120", "v121", "v122", "v123", "v124", "v125", "v126", "v127", "v128", "v129", "v130", "v131", "v132", "v133", "v134", "v135", "v136", "v137", "v138", "v139", "v140", "v141", "v142", "v143", "v144", "v145", "v146", "v147", "v148", "v149", "v150", "v151", "v152", "v153", "v154", "v155", "v156", "v157", "v158", "v159", "v160", "v161", "v162", "v163", "v164", "v165", "v166", "v167", "v168", "v169", "v170", "v171", "v172", "v173", "v174", "v175", "v176", "v177", "v178", "v179", "v180", "v181", "v182", "v183", "v184", "v185", "v186", "v187", "v188", "v189", "v190", "v191", "v192", "v193", "v194", "v195", "v196", "v197", "v198", "v199", "v200", "v201", "v202", "v203", "v204", "v205", "v206", "v207", "v208", "v209", "v210", "v211", "v212", "v213", "v214", "v215", "v216", "v217", "v218", "v219", "v220", "v221", "v222", "v223", "v224", "v225", "v226", "v227", "v228", "v229", "v230", "v231", "v232", "v233", "v234", "v235", "v236", "v237", "v238", "v239", "v240", "v241", "v242", "v243", "v244", "v245", "v246", "v247", "v248", "v249", "v250", "v251", "v252", "v253", "v254", "v255", "a0", "a1", "a2", "a3", "a4", "a5", "a6", "a7", "a8", "a9", "a10", "a11", "a12", "a13", "a14", "a15", "a16", "a17", "a18", "a19", "a20", "a21", "a22", "a23", "a24", "a25", "a26", "a27", "a28", "a29", "a30", "a31", "a32", "a33", "a34", "a35", "a36", "a37", "a38", "a39", "a40", "a41", "a42", "a43", "a44", "a45", "a46", "a47", "a48", "a49", "a50", "a51", "a52", "a53", "a54", "a55", "a56", "a57", "a58", "a59", "a60", "a61", "a62", "a63", "a64", "a65", "a66", "a67", "a68", "a69", "a70", "a71", "a72", "a73", "a74", "a75", "a76", "a77", "a78", "a79", "a80", "a81", "a82", "a83", "a84", "a85", "a86", "a87", "a88", "a89", "a90", "a91", "a92", "a93", "a94", "a95", "a96", "a97", "a98", "a99", "a100", "a101", "a102", "a103", "a104", "a105", "a106", "a107", "a108", "a109", "a110", "a111", "a112", "a113", "a114", "a115", "a116", "a117", "a118", "a119", "a120", "a121", "a122", "a123", "a124", "a125", "a126", "a127", "a128", "a129", "a130", "a131", "a132", "a133", "a134", "a135", "a136", "a137", "a138", "a139", "a140", "a141", "a142", "a143", "a144", "a145", "a146", "a147", "a148", "a149", "a150", "a151", "a152", "a153", "a154", "a155", "a156", "a157", "a158", "a159", "a160", "a161", "a162", "a163", "a164", "a165", "a166", "a167", "a168", "a169", "a170", "a171", "a172", "a173", "a174", "a175", "a176", "a177", "a178", "a179", "a180", "a181", "a182", "a183", "a184", "a185", "a186", "a187", "a188", "a189", "a190", "a191", "a192", "a193", "a194", "a195", "a196", "a197", "a198", "a199", "a200", "a201", "a202", "a203", "a204", "a205", "a206", "a207", "a208", "a209", "a210", "a211", "a212", "a213", "a214", "a215", "a216", "a217", "a218", "a219", "a220", "a221", "a222", "a223", "a224", "a225", "a226", "a227", "a228", "a229", "a230", "a231", "a232", "a233", "a234", "a235", "a236", "a237", "a238", "a239", "a240", "a241", "a242", "a243", "s36", "s37", "s38", "s39", "s40", "s41", "s42", "s43", "s44", "s45", "s46", "s47", "s48", "s49", "s50", "s51", "s52", "s53", "s54", "s55", "s56", "s57", "s58", "s59", "s60", "s61", "s62", "s63", "s64", "s65", "s66", "s67", "s68", "s69", "s70", "s71", "s72", "s73", "s74", "s75", "s76", "s77", "s78", "s79", "s80", "s81", "s82", "s83", "s84", "s85", "s86", "s87", "s88", "s89", "s90", "s91", "s92", "s93", "s94", "s95", "s96", "s97", "s98", "s99", "vcc", "scc", "memory");
  if (__builtin_expect(flag != 0u, 0)) {
    asm volatile("s_waitcnt vmcnt(0)" ::: "memory");
    const int lane = __builtin_amdgcn_mbcnt_hi(~0u, __builtin_amdgcn_mbcnt_lo(~0u, 0u));
    for (int b = 0; b < 3; ++b) {
      if (flag >> b & 9u) {
        const unsigned n = b * 32u + (bid >> 3), pair = (bid & 7u) * 32u + n / 3u, sub = n % 3u;
        const size_t row = (size_t)pair * 576u + sub * 192u + wv * 48u;
        bool redo = flag >> b & 1u;
        if (!redo) {
          bool bad = false;
          u32x4_t w[12];
#pragma unroll
          for (int i = 0; i < 12; ++i) w[i] = ((const u32x4_t*)(o + row * 128))[i * 64 + lane];
#pragma unroll
          for (int i = 0; i < 12; ++i)
#pragma unroll
            for (int e = 0; e < 4; ++e) bad |= (w[i][e] & 0x7f80u) == 0x7f80u || (w[i][e] & 0x7f800000u) == 0x7f800000u;
          redo = __any(bad);
        }
        if (redo) slow_rows(q + row * 128, k + (size_t)pair * 65536u, v + (size_t)pair * 65536u, o + row * 128, lane);
      }
    }
  }
}

extern "C" void kernel_launch(void* const* d_in, const int* in_sizes, int n_in, void* d_out, int out_size, void* d_ws, size_t ws_size,
                              hipStream_t stream) {
  hipLaunchKernelGGL(gqa_fwd, dim3(256), dim3(256), 0, stream, (const unsigned short*)d_in[0], (const unsigned short*)d_in[1],
                     (const unsigned short*)d_in[2], (unsigned short*)d_out, d_ws);
}
```
